```python
import torch

def _softmax(x, axis=-1):
    return torch.softmax(x, dim=axis)

def _gelu(x):
    return torch.nn.functional.gelu(x, approximate='tanh')

def _silu(x):
    return torch.nn.functional.silu(x)

def _sigmoid(x):
    return torch.sigmoid(x)

def _layernorm(x, axis=-1, eps=1e-05):
    m = x.mean(dim=axis, keepdim=True)
    v = x.var(dim=axis, keepdim=True, unbiased=False)
    return (x - m) / torch.sqrt(v + eps)

def _scatter_add(tgt, idx, src, axis=0):
    return tgt.clone().index_add_(axis, idx, src)

def _segment_sum(src, seg, n):
    out = torch.zeros((n,) + src.shape[1:], dtype=src.dtype)
    return out.index_add_(0, seg, src)

def _pad(x, pw):
    flat = tuple((v for pair in reversed(pw) for v in pair))
    return torch.nn.functional.pad(x, flat)
kernel_name = 'standalone__dilated_atrous__vision'

def setup_inputs():

    _w = lambda a: (lambda t: t.to(torch.bfloat16) if t.is_floating_point() else t)(torch.as_tensor(a))
    H = W = 224
    C = 96
    k = 7
    return {'x': _w((lambda *s: torch.randn(*s, dtype=torch.bfloat16) * 0.05)(1, H, W, C)), 'w': _w((lambda *s: torch.randn(*s, dtype=torch.bfloat16) * 0.05)(k, k, C, C) * (1.0 / (k * C ** 0.5)))}

def reference(x, w):
    B, H, W, C = x.shape
    k = 7
    rate = 2
    pad = (k - 1) * rate // 2
    xp = _pad(x, ((0, 0), (pad, pad), (pad, pad), (0, 0)))
    out = torch.zeros_like(x)
    for i in range(k):
        for j in range(k):
            tap = xp[:, i * rate:i * rate + H, j * rate:j * rate + W, :].clone()
            out = out + torch.einsum('bhwc,co->bhwo', tap, w[i, j])
    return out
```

```cpp
#include <hip/hip_runtime.h>
#include <hip/hip_bf16.h>

__global__ void naive_kernel(const unsigned short* __restrict__ x, const unsigned short* __restrict__ w, unsigned short* __restrict__ out) {
    int o = threadIdx.x;
    int p = blockIdx.x;
    int h = p / 224, wc = p % 224;
    float acc = 0.f;
    for (int i = 0; i < 7; ++i) {
        int hh = h + 2 * i - 6;
        if (hh < 0 || hh >= 224) continue;
        for (int j = 0; j < 7; ++j) {
            int ww = wc + 2 * j - 6;
            if (ww < 0 || ww >= 224) continue;
            const unsigned short* xp = x + (hh * 224 + ww) * 96;
            const unsigned short* wp = w + (i * 7 + j) * 96 * 96 + o;
            for (int c = 0; c < 96; ++c) {
                float a = __uint_as_float((unsigned)xp[c] << 16);
                float b = __uint_as_float((unsigned)wp[c * 96] << 16);
                acc += a * b;
            }
        }
    }
    __hip_bfloat16 r = __float2bfloat16(acc);
    out[p * 96 + o] = *(unsigned short*)&r;
}

extern "C" void kernel_launch(void* const* d_in, const int* in_sizes, int n_in,
                              void* d_out, int out_size, void* d_ws, size_t ws_size,
                              hipStream_t stream) {
    naive_kernel<<<224 * 224, 96, 0, stream>>>((const unsigned short*)d_in[0], (const unsigned short*)d_in[1], (unsigned short*)d_out);
}
```

```cpp
#include <hip/hip_runtime.h>
#include <hip/hip_bf16.h>

__device__ const unsigned kT[2560] = {0x00003f80u, 0x0000c0a8u, 0x000040a8u, 0x0000bf80u, 0x00000000u, 0x00003f80u, 0x0000c0a8u, 0x000040a8u, 0x00000000u, 0x00000000u, 0x00003f80u, 0x0000c0a8u, 0x00000000u, 0x00000000u, 0x00000000u, 0x00003f80u, 0x00000000u, 0x00000000u, 0x00000000u, 0x00000000u, 0x00000000u, 0x00000000u, 0x00000000u, 0x00000000u, 0x00000000u, 0x00000000u, 0x00000000u, 0x00000000u, 0x00000000u, 0x00000000u, 0x00000000u, 0x00000000u, 0xbf800000u, 0x40a80000u, 0xc0a80000u, 0x3f800000u, 0x00000000u, 0xbf800000u, 0x40a80000u, 0xc0a80000u, 0x00000000u, 0x00000000u, 0xbf800000u, 0x40a80000u, 0x00000000u, 0x00000000u, 0x00000000u, 0xbf800000u, 0x00000000u, 0x00000000u, 0x00000000u, 0x00000000u, 0x00000000u, 0x00000000u, 0x00000000u, 0x00000000u, 0x00000000u, 0x00000000u, 0x00000000u, 0x00000000u, 0x00000000u, 0x00000000u, 0x00000000u, 0x00000000u, 0x00000000u, 0x00000000u, 0x00000000u, 0x00000000u, 0x0000bf80u, 0x00000000u, 0x00000000u, 0x00000000u, 0x000040a8u, 0x0000bf80u, 0x00000000u, 0x00000000u, 0x0000c0a8u, 0x000040a8u, 0x0000bf80u, 0x00000000u, 0x00003f80u, 0x0000c0a8u, 0x000040a8u, 0x0000bf80u, 0x00000000u, 0x00003f80u, 0x0000c0a8u, 0x000040a8u, 0x00000000u, 0x00000000u, 0x00003f80u, 0x0000c0a8u, 0x00000000u, 0x00000000u, 0x00000000u, 0x00003f80u, 0x00000000u, 0x00000000u, 0x00000000u, 0x00000000u, 0x3f800000u, 0x00000000u, 0x00000000u, 0x00000000u, 0xc0a80000u, 0x3f800000u, 0x00000000u, 0x00000000u, 0x40a80000u, 0xc0a80000u, 0x3f800000u, 0x00000000u, 0xbf800000u, 0x40a80000u, 0xc0a80000u, 0x3f800000u, 0x00000000u, 0xbf800000u, 0x40a80000u, 0xc0a80000u, 0x00000000u, 0x00000000u, 0xbf800000u, 0x40a80000u, 0x00000000u, 0x00000000u, 0x00000000u, 0xbf800000u, 0x00000000u, 0x00000000u, 0x00000000u, 0x00000000u, 0x00000000u, 0x00000000u, 0x00000000u, 0x00000000u, 0x00000000u, 0x00000000u, 0x00000000u, 0x00000000u, 0x00000000u, 0x00000000u, 0x00000000u, 0x00000000u, 0x00000000u, 0x00000000u, 0x00000000u, 0x00000000u, 0x0000bf80u, 0x00000000u, 0x00000000u, 0x00000000u, 0x000040a8u, 0x0000bf80u, 0x00000000u, 0x00000000u, 0x0000c0a8u, 0x000040a8u, 0x0000bf80u, 0x00000000u, 0x00000000u, 0x00000000u, 0x00000000u, 0x00000000u, 0x00000000u, 0x00000000u, 0x00000000u, 0x00000000u, 0x00000000u, 0x00000000u, 0x00000000u, 0x00000000u, 0x00000000u, 0x00000000u, 0x00000000u, 0x00000000u, 0x00000000u, 0x00000000u, 0x00000000u, 0x00000000u, 0x3f800000u, 0x00000000u, 0x00000000u, 0x00000000u, 0xc0a80000u, 0x3f800000u, 0x00000000u, 0x00000000u, 0x40a80000u, 0xc0a80000u, 0x3f800000u, 0x00000000u, 0x00000000u, 0x00000000u, 0x00000000u, 0x00000000u, 0x00000000u, 0x00000000u, 0x00000000u, 0x00000000u, 0x00000000u, 0x00000000u, 0x00000000u, 0x00000000u, 0x00000000u, 0x00000000u, 0x00000000u, 0x00000000u, 0x00000000u, 0x00000000u, 0x00000000u, 0x00000000u, 0x00000000u, 0x00000000u, 0x00000000u, 0x00000000u, 0x00000000u, 0x00000000u, 0x00000000u, 0x00000000u, 0x00000000u, 0x00000000u, 0x00000000u, 0x00000000u, 0x00000000u, 0x00000000u, 0x00000000u, 0x00000000u, 0x00000000u, 0x00000000u, 0x00000000u, 0x00000000u, 0x00000000u, 0x00000000u, 0x00000000u, 0x00000000u, 0x00000000u, 0x00000000u, 0x00000000u, 0x00000000u, 0x00000000u, 0x00000000u, 0x00000000u, 0x00000000u, 0x00000000u, 0x00000000u, 0x00000000u, 0x00000000u, 0x00000000u, 0x00000000u, 0x00000000u, 0x00000000u, 0x00000000u, 0x00000000u, 0x00000000u, 0x00000000u, 0x40000000u, 0xc0204080u, 0x3f00c0a0u, 0x00003f80u, 0x00000000u, 0x40000000u, 0xc0204080u, 0x3f00c0a0u, 0x00000000u, 0x00000000u, 0x40000000u, 0xc0204080u, 0x00000000u, 0x00000000u, 0x00000000u, 0x40000000u, 0x00000000u, 0x00000000u, 0x00000000u, 0x00000000u, 0x00000000u, 0x00000000u, 0x00000000u, 0x00000000u, 0x00000000u, 0x00000000u, 0x00000000u, 0x00000000u, 0x00000000u, 0x00000000u, 0x00000000u, 0x00000000u, 0x3f000000u, 0xc0203e80u, 0x4000bfa0u, 0x00003f80u, 0x00000000u, 0x3f000000u, 0xc0203e80u, 0x4000bfa0u, 0x00000000u, 0x00000000u, 0x3f000000u, 0xc0203e80u, 0x00000000u, 0x00000000u, 0x00000000u, 0x3f000000u, 0x00000000u, 0x00000000u, 0x00000000u, 0x00000000u, 0x00000000u, 0x00000000u, 0x00000000u, 0x00000000u, 0x00000000u, 0x00000000u, 0x00000000u, 0x00000000u, 0x00000000u, 0x00000000u, 0x00000000u, 0x00000000u, 0x00000000u, 0x00000000u, 0x00000000u, 0x00000000u, 0x00003f80u, 0x00000000u, 0x00000000u, 0x00000000u, 0x3f00c0a0u, 0x00003f80u, 0x00000000u, 0x00000000u, 0xc0204080u, 0x3f00c0a0u, 0x00003f80u, 0x00000000u, 0x40000000u, 0xc0204080u, 0x3f00c0a0u, 0x00003f80u, 0x00000000u, 0x40000000u, 0xc0204080u, 0x3f00c0a0u, 0x00000000u, 0x00000000u, 0x40000000u, 0xc0204080u, 0x00000000u, 0x00000000u, 0x00000000u, 0x40000000u, 0x00000000u, 0x00000000u, 0x00000000u, 0x00000000u, 0x00003f80u, 0x00000000u, 0x00000000u, 0x00000000u, 0x4000bfa0u, 0x00003f80u, 0x00000000u, 0x00000000u, 0xc0203e80u, 0x4000bfa0u, 0x00003f80u, 0x00000000u, 0x3f000000u, 0xc0203e80u, 0x4000bfa0u, 0x00003f80u, 0x00000000u, 0x3f000000u, 0xc0203e80u, 0x4000bfa0u, 0x00000000u, 0x00000000u, 0x3f000000u, 0xc0203e80u, 0x00000000u, 0x00000000u, 0x00000000u, 0x3f000000u, 0x00000000u, 0x00000000u, 0x00000000u, 0x00000000u, 0x00000000u, 0x00000000u, 0x00000000u, 0x00000000u, 0x00000000u, 0x00000000u, 0x00000000u, 0x00000000u, 0x00000000u, 0x00000000u, 0x00000000u, 0x00000000u, 0x00000000u, 0x00000000u, 0x00000000u, 0x00000000u, 0x00003f80u, 0x00000000u, 0x00000000u, 0x00000000u, 0x3f00c0a0u, 0x00003f80u, 0x00000000u, 0x00000000u, 0xc0204080u, 0x3f00c0a0u, 0x00003f80u, 0x00000000u, 0x00000000u, 0x00000000u, 0x00000000u, 0x00000000u, 0x00000000u, 0x00000000u, 0x00000000u, 0x00000000u, 0x00000000u, 0x00000000u, 0x00000000u, 0x00000000u, 0x00000000u, 0x00000000u, 0x00000000u, 0x00000000u, 0x00000000u, 0x00000000u, 0x00000000u, 0x00000000u, 0x00003f80u, 0x00000000u, 0x00000000u, 0x00000000u, 0x4000bfa0u, 0x00003f80u, 0x00000000u, 0x00000000u, 0xc0203e80u, 0x4000bfa0u, 0x00003f80u, 0x00000000u, 0x00000000u, 0x00000000u, 0x00000000u, 0x00000000u, 0x00000000u, 0x00000000u, 0x00000000u, 0x00000000u, 0x00000000u, 0x00000000u, 0x00000000u, 0x00000000u, 0x00000000u, 0x00000000u, 0x00000000u, 0x00000000u, 0x00000000u, 0x00000000u, 0x00000000u, 0x00000000u, 0x00000000u, 0x00000000u, 0x00000000u, 0x00000000u, 0x00000000u, 0x00000000u, 0x00000000u, 0x00000000u, 0x00000000u, 0x00000000u, 0x00000000u, 0x00000000u, 0x00000000u, 0x00000000u, 0x00000000u, 0x00000000u, 0x00000000u, 0x00000000u, 0x00000000u, 0x00000000u, 0x00000000u, 0x00000000u, 0x00000000u, 0x00000000u, 0x00000000u, 0x00000000u, 0x00000000u, 0x00000000u, 0x00000000u, 0x00000000u, 0x00000000u, 0x00000000u, 0x00000000u, 0x00000000u, 0x00000000u, 0x00000000u, 0x00000000u, 0x00000000u, 0x00000000u, 0x00000000u, 0x00000000u, 0x00000000u, 0x00000000u, 0x00000000u, 0xc0000000u, 0x40204080u, 0xbf00c0a0u, 0x00003f80u, 0x00000000u, 0xc0000000u, 0x40204080u, 0xbf00c0a0u, 0x00000000u, 0x00000000u, 0xc0000000u, 0x40204080u, 0x00000000u, 0x00000000u, 0x00000000u, 0xc0000000u, 0x00000000u, 0x00000000u, 0x00000000u, 0x00000000u, 0x00000000u, 0x00000000u, 0x00000000u, 0x00000000u, 0x00000000u, 0x00000000u, 0x00000000u, 0x00000000u, 0x00000000u, 0x00000000u, 0x00000000u, 0x00000000u, 0xbf000000u, 0x40203e80u, 0xc000bfa0u, 0x00003f80u, 0x00000000u, 0xbf000000u, 0x40203e80u, 0xc000bfa0u, 0x00000000u, 0x00000000u, 0xbf000000u, 0x40203e80u, 0x00000000u, 0x00000000u, 0x00000000u, 0xbf000000u, 0x00000000u, 0x00000000u, 0x00000000u, 0x00000000u, 0x00000000u, 0x00000000u, 0x00000000u, 0x00000000u, 0x00000000u, 0x00000000u, 0x00000000u, 0x00000000u, 0x00000000u, 0x00000000u, 0x00000000u, 0x00000000u, 0x00000000u, 0x00000000u, 0x00000000u, 0x00000000u, 0x00003f80u, 0x00000000u, 0x00000000u, 0x00000000u, 0xbf00c0a0u, 0x00003f80u, 0x00000000u, 0x00000000u, 0x40204080u, 0xbf00c0a0u, 0x00003f80u, 0x00000000u, 0xc0000000u, 0x40204080u, 0xbf00c0a0u, 0x00003f80u, 0x00000000u, 0xc0000000u, 0x40204080u, 0xbf00c0a0u, 0x00000000u, 0x00000000u, 0xc0000000u, 0x40204080u, 0x00000000u, 0x00000000u, 0x00000000u, 0xc0000000u, 0x00000000u, 0x00000000u, 0x00000000u, 0x00000000u, 0x00003f80u, 0x00000000u, 0x00000000u, 0x00000000u, 0xc000bfa0u, 0x00003f80u, 0x00000000u, 0x00000000u, 0x40203e80u, 0xc000bfa0u, 0x00003f80u, 0x00000000u, 0xbf000000u, 0x40203e80u, 0xc000bfa0u, 0x00003f80u, 0x00000000u, 0xbf000000u, 0x40203e80u, 0xc000bfa0u, 0x00000000u, 0x00000000u, 0xbf000000u, 0x40203e80u, 0x00000000u, 0x00000000u, 0x00000000u, 0xbf000000u, 0x00000000u, 0x00000000u, 0x00000000u, 0x00000000u, 0x00000000u, 0x00000000u, 0x00000000u, 0x00000000u, 0x00000000u, 0x00000000u, 0x00000000u, 0x00000000u, 0x00000000u, 0x00000000u, 0x00000000u, 0x00000000u, 0x00000000u, 0x00000000u, 0x00000000u, 0x00000000u, 0x00003f80u, 0x00000000u, 0x00000000u, 0x00000000u, 0xbf00c0a0u, 0x00003f80u, 0x00000000u, 0x00000000u, 0x40204080u, 0xbf00c0a0u, 0x00003f80u, 0x00000000u, 0x00000000u, 0x00000000u, 0x00000000u, 0x00000000u, 0x00000000u, 0x00000000u, 0x00000000u, 0x00000000u, 0x00000000u, 0x00000000u, 0x00000000u, 0x00000000u, 0x00000000u, 0x00000000u, 0x00000000u, 0x00000000u, 0x00000000u, 0x00000000u, 0x00000000u, 0x00000000u, 0x00003f80u, 0x00000000u, 0x00000000u, 0x00000000u, 0xc000bfa0u, 0x00003f80u, 0x00000000u, 0x00000000u, 0x40203e80u, 0xc000bfa0u, 0x00003f80u, 0x00000000u, 0x00000000u, 0x00000000u, 0x00000000u, 0x00000000u, 0x00000000u, 0x00000000u, 0x00000000u, 0x00000000u, 0x00000000u, 0x00000000u, 0x00000000u, 0x00000000u, 0x00000000u, 0x00000000u, 0x00000000u, 0x00000000u, 0x00000000u, 0x00000000u, 0x00000000u, 0x00000000u, 0x00000000u, 0x00000000u, 0x00000000u, 0x00000000u, 0x00000000u, 0x00000000u, 0x00000000u, 0x00000000u, 0x00000000u, 0x00000000u, 0x00000000u, 0x00000000u, 0x00000000u, 0x00000000u, 0x00000000u, 0x00000000u, 0x00000000u, 0x00000000u, 0x00000000u, 0x00000000u, 0x00000000u, 0x00000000u, 0x00000000u, 0x00000000u, 0x00000000u, 0x00000000u, 0x00000000u, 0x00000000u, 0x00000000u, 0x00000000u, 0x00000000u, 0x00000000u, 0x00000000u, 0x00000000u, 0x00000000u, 0x00000000u, 0x00000000u, 0x00000000u, 0x00000000u, 0x00000000u, 0x00000000u, 0x00000000u, 0x00000000u, 0x00000000u, 0x3f800000u, 0xc0883f80u, 0x3f80c088u, 0x00003f80u, 0x00000000u, 0x3f800000u, 0xc0883f80u, 0x3f80c088u, 0x00000000u, 0x00000000u, 0x3f800000u, 0xc0883f80u, 0x00000000u, 0x00000000u, 0x00000000u, 0x3f800000u, 0x00000000u, 0x00000000u, 0x00000000u, 0x00000000u, 0x00000000u, 0x00000000u, 0x00000000u, 0x00000000u, 0x00000000u, 0x00000000u, 0x00000000u, 0x00000000u, 0x00000000u, 0x00000000u, 0x00000000u, 0x00000000u, 0xbf800000u, 0x40883f80u, 0xbf80c088u, 0x00003f80u, 0x00000000u, 0xbf800000u, 0x40883f80u, 0xbf80c088u, 0x00000000u, 0x00000000u, 0xbf800000u, 0x40883f80u, 0x00000000u, 0x00000000u, 0x00000000u, 0xbf800000u, 0x00000000u, 0x00000000u, 0x00000000u, 0x00000000u, 0x00000000u, 0x00000000u, 0x00000000u, 0x00000000u, 0x00000000u, 0x00000000u, 0x00000000u, 0x00000000u, 0x00000000u, 0x00000000u, 0x00000000u, 0x00000000u, 0x00000000u, 0x00000000u, 0x00000000u, 0x00000000u, 0x00003f80u, 0x00000000u, 0x00000000u, 0x00000000u, 0x3f80c088u, 0x00003f80u, 0x00000000u, 0x00000000u, 0xc0883f80u, 0x3f80c088u, 0x00003f80u, 0x00000000u, 0x3f800000u, 0xc0883f80u, 0x3f80c088u, 0x00003f80u, 0x00000000u, 0x3f800000u, 0xc0883f80u, 0x3f80c088u, 0x00000000u, 0x00000000u, 0x3f800000u, 0xc0883f80u, 0x00000000u, 0x00000000u, 0x00000000u, 0x3f800000u, 0x00000000u, 0x00000000u, 0x00000000u, 0x00000000u, 0x00003f80u, 0x00000000u, 0x00000000u, 0x00000000u, 0xbf80c088u, 0x00003f80u, 0x00000000u, 0x00000000u, 0x40883f80u, 0xbf80c088u, 0x00003f80u, 0x00000000u, 0xbf800000u, 0x40883f80u, 0xbf80c088u, 0x00003f80u, 0x00000000u, 0xbf800000u, 0x40883f80u, 0xbf80c088u, 0x00000000u, 0x00000000u, 0xbf800000u, 0x40883f80u, 0x00000000u, 0x00000000u, 0x00000000u, 0xbf800000u, 0x00000000u, 0x00000000u, 0x00000000u, 0x00000000u, 0x00000000u, 0x00000000u, 0x00000000u, 0x00000000u, 0x00000000u, 0x00000000u, 0x00000000u, 0x00000000u, 0x00000000u, 0x00000000u, 0x00000000u, 0x00000000u, 0x00000000u, 0x00000000u, 0x00000000u, 0x00000000u, 0x00003f80u, 0x00000000u, 0x00000000u, 0x00000000u, 0x3f80c088u, 0x00003f80u, 0x00000000u, 0x00000000u, 0xc0883f80u, 0x3f80c088u, 0x00003f80u, 0x00000000u, 0x00000000u, 0x00000000u, 0x00000000u, 0x00000000u, 0x00000000u, 0x00000000u, 0x00000000u, 0x00000000u, 0x00000000u, 0x00000000u, 0x00000000u, 0x00000000u, 0x00000000u, 0x00000000u, 0x00000000u, 0x00000000u, 0x00000000u, 0x00000000u, 0x00000000u, 0x00000000u, 0x00003f80u, 0x00000000u, 0x00000000u, 0x00000000u, 0xbf80c088u, 0x00003f80u, 0x00000000u, 0x00000000u, 0x40883f80u, 0xbf80c088u, 0x00003f80u, 0x00000000u, 0x00000000u, 0x00000000u, 0x00000000u, 0x00000000u, 0x00000000u, 0x00000000u, 0x00000000u, 0x00000000u, 0x00000000u, 0x00000000u, 0x00000000u, 0x00000000u, 0x00000000u, 0x00000000u, 0x00000000u, 0x00000000u, 0x00000000u, 0x00000000u, 0x00000000u, 0x00000000u, 0x00000000u, 0x00000000u, 0x00000000u, 0x00000000u, 0x00000000u, 0x00000000u, 0x00000000u, 0x00000000u, 0x00000000u, 0x00000000u, 0x00000000u, 0x00000000u, 0x00000000u, 0x00000000u, 0x00000000u, 0x00000000u, 0x00000000u, 0x00000000u, 0x00000000u, 0x00000000u, 0x00000000u, 0x00000000u, 0x00000000u, 0x00000000u, 0x00000000u, 0x00000000u, 0x00000000u, 0x00000000u, 0x00000000u, 0x00000000u, 0x00000000u, 0x00000000u, 0x00000000u, 0x00000000u, 0x00000000u, 0x00000000u, 0x00000000u, 0x00000000u, 0x00000000u, 0x00000000u, 0x00000000u, 0x00000000u, 0x00000000u, 0x00000000u, 0x00000000u, 0x00001800u, 0x00054f20u, 0x000a8600u, 0x000a9e00u, 0x000fd520u, 0x00150c20u, 0xc0000000u, 0x00000010u, 0x00001810u, 0x00054f30u, 0x000a8610u, 0x000a9e10u, 0x000fd530u, 0x00150c30u, 0xc0000000u, 0x00000020u, 0x00001820u, 0x00054f00u, 0x000a8620u, 0x000a9e20u, 0x000fd500u, 0x00150c00u, 0xc0000000u, 0x00000030u, 0x00001830u, 0x00054f10u, 0x000a8630u, 0x000a9e30u, 0x000fd510u, 0x00150c10u, 0xc0000000u, 0x00000180u, 0x00001980u, 0x000550a0u, 0x000a8780u, 0x000a9f80u, 0x000fd6a0u, 0x00150da0u, 0xc0000000u, 0x00000190u, 0x00001990u, 0x000550b0u, 0x000a8790u, 0x000a9f90u, 0x000fd6b0u, 0x00150db0u, 0xc0000000u, 0x000001a0u, 0x000019a0u, 0x00055080u, 0x000a87a0u, 0x000a9fa0u, 0x000fd680u, 0x00150d80u, 0xc0000000u, 0x000001b0u, 0x000019b0u, 0x00055090u, 0x000a87b0u, 0x000a9fb0u, 0x000fd690u, 0x00150d90u, 0xc0000000u, 0x00000300u, 0x00001b00u, 0x00055220u, 0x000a8900u, 0x000fc000u, 0x000fd800u, 0x00150f20u, 0xc0000000u, 0x00000310u, 0x00001b10u, 0x00055230u, 0x000a8910u, 0x000fc010u, 0x000fd810u, 0x00150f30u, 0xc0000000u, 0x00000320u, 0x00001b20u, 0x00055200u, 0x000a8920u, 0x000fc020u, 0x000fd820u, 0x00150f00u, 0xc0000000u, 0x00000330u, 0x00001b30u, 0x00055210u, 0x000a8930u, 0x000fc030u, 0x000fd830u, 0x00150f10u, 0xc0000000u, 0x00000480u, 0x00001c80u, 0x000553a0u, 0x000a8a80u, 0x000fc180u, 0x000fd980u, 0x001510a0u, 0xc0000000u, 0x00000490u, 0x00001c90u, 0x000553b0u, 0x000a8a90u, 0x000fc190u, 0x000fd990u, 0x001510b0u, 0xc0000000u, 0x000004a0u, 0x00001ca0u, 0x00055380u, 0x000a8aa0u, 0x000fc1a0u, 0x000fd9a0u, 0x00151080u, 0xc0000000u, 0x000004b0u, 0x00001cb0u, 0x00055390u, 0x000a8ab0u, 0x000fc1b0u, 0x000fd9b0u, 0x00151090u, 0xc0000000u, 0x00000600u, 0x00001e00u, 0x00055520u, 0x000a8c20u, 0x000fc300u, 0x000fdb00u, 0x00151220u, 0xc0000000u, 0x00000610u, 0x00001e10u, 0x00055530u, 0x000a8c30u, 0x000fc310u, 0x000fdb10u, 0x00151230u, 0xc0000000u, 0x00000620u, 0x00001e20u, 0x00055500u, 0x000a8c00u, 0x000fc320u, 0x000fdb20u, 0x00151200u, 0xc0000000u, 0x00000630u, 0x00001e30u, 0x00055510u, 0x000a8c10u, 0x000fc330u, 0x000fdb30u, 0x00151210u, 0xc0000000u, 0x00000780u, 0x00001f80u, 0x000556a0u, 0x000a8da0u, 0x000fc480u, 0x000fdc80u, 0x001513a0u, 0xc0000000u, 0x00000790u, 0x00001f90u, 0x000556b0u, 0x000a8db0u, 0x000fc490u, 0x000fdc90u, 0x001513b0u, 0xc0000000u, 0x000007a0u, 0x00001fa0u, 0x00055680u, 0x000a8d80u, 0x000fc4a0u, 0x000fdca0u, 0x00151380u, 0xc0000000u, 0x000007b0u, 0x00001fb0u, 0x00055690u, 0x000a8d90u, 0x000fc4b0u, 0x000fdcb0u, 0x00151390u, 0xc0000000u, 0x00000900u, 0x00054000u, 0x00055800u, 0x000a8f20u, 0x000fc600u, 0x000fde00u, 0x00151520u, 0xc0000000u, 0x00000910u, 0x00054010u, 0x00055810u, 0x000a8f30u, 0x000fc610u, 0x000fde10u, 0x00151530u, 0xc0000000u, 0x00000920u, 0x00054020u, 0x00055820u, 0x000a8f00u, 0x000fc620u, 0x000fde20u, 0x00151500u, 0xc0000000u, 0x00000930u, 0x00054030u, 0x00055830u, 0x000a8f10u, 0x000fc630u, 0x000fde30u, 0x00151510u, 0xc0000000u, 0x00000a80u, 0x00054180u, 0x00055980u, 0x000a90a0u, 0x000fc780u, 0x000fdf80u, 0x001516a0u, 0xc0000000u, 0x00000a90u, 0x00054190u, 0x00055990u, 0x000a90b0u, 0x000fc790u, 0x000fdf90u, 0x001516b0u, 0xc0000000u, 0x00000aa0u, 0x000541a0u, 0x000559a0u, 0x000a9080u, 0x000fc7a0u, 0x000fdfa0u, 0x00151680u, 0xc0000000u, 0x00000ab0u, 0x000541b0u, 0x000559b0u, 0x000a9090u, 0x000fc7b0u, 0x000fdfb0u, 0x00151690u, 0xc0000000u, 0x00000c20u, 0x00054300u, 0x00055b00u, 0x000a9220u, 0x000fc900u, 0x00150000u, 0x00151800u, 0xc0000000u, 0x00000c30u, 0x00054310u, 0x00055b10u, 0x000a9230u, 0x000fc910u, 0x00150010u, 0x00151810u, 0xc0000000u, 0x00000c00u, 0x00054320u, 0x00055b20u, 0x000a9200u, 0x000fc920u, 0x00150020u, 0x00151820u, 0xc0000000u, 0x00000c10u, 0x00054330u, 0x00055b30u, 0x000a9210u, 0x000fc930u, 0x00150030u, 0x00151830u, 0xc0000000u, 0x00000da0u, 0x00054480u, 0x00055c80u, 0x000a93a0u, 0x000fca80u, 0x00150180u, 0x00151980u, 0xc0000000u, 0x00000db0u, 0x00054490u, 0x00055c90u, 0x000a93b0u, 0x000fca90u, 0x00150190u, 0x00151990u, 0xc0000000u, 0x00000d80u, 0x000544a0u, 0x00055ca0u, 0x000a9380u, 0x000fcaa0u, 0x001501a0u, 0x001519a0u, 0xc0000000u, 0x00000d90u, 0x000544b0u, 0x00055cb0u, 0x000a9390u, 0x000fcab0u, 0x001501b0u, 0x001519b0u, 0xc0000000u, 0x00000f20u, 0x00054600u, 0x00055e00u, 0x000a9520u, 0x000fcc20u, 0x00150300u, 0x00151b00u, 0xc0000000u, 0x00000f30u, 0x00054610u, 0x00055e10u, 0x000a9530u, 0x000fcc30u, 0x00150310u, 0x00151b10u, 0xc0000000u, 0x00000f00u, 0x00054620u, 0x00055e20u, 0x000a9500u, 0x000fcc00u, 0x00150320u, 0x00151b20u, 0xc0000000u, 0x00000f10u, 0x00054630u, 0x00055e30u, 0x000a9510u, 0x000fcc10u, 0x00150330u, 0x00151b30u, 0xc0000000u, 0x000010a0u, 0x00054780u, 0x00055f80u, 0x000a96a0u, 0x000fcda0u, 0x00150480u, 0x00151c80u, 0xc0000000u, 0x000010b0u, 0x00054790u, 0x00055f90u, 0x000a96b0u, 0x000fcdb0u, 0x00150490u, 0x00151c90u, 0xc0000000u, 0x00001080u, 0x000547a0u, 0x00055fa0u, 0x000a9680u, 0x000fcd80u, 0x001504a0u, 0x00151ca0u, 0xc0000000u, 0x00001090u, 0x000547b0u, 0x00055fb0u, 0x000a9690u, 0x000fcd90u, 0x001504b0u, 0x00151cb0u, 0xc0000000u, 0x00001220u, 0x00054900u, 0x000a8000u, 0x000a9800u, 0x000fcf20u, 0x00150600u, 0x00151e00u, 0xc0000000u, 0x00001230u, 0x00054910u, 0x000a8010u, 0x000a9810u, 0x000fcf30u, 0x00150610u, 0x00151e10u, 0xc0000000u, 0x00001200u, 0x00054920u, 0x000a8020u, 0x000a9820u, 0x000fcf00u, 0x00150620u, 0x00151e20u, 0xc0000000u, 0x00001210u, 0x00054930u, 0x000a8030u, 0x000a9830u, 0x000fcf10u, 0x00150630u, 0x00151e30u, 0xc0000000u, 0x000013a0u, 0x00054a80u, 0x000a8180u, 0x000a9980u, 0x000fd0a0u, 0x00150780u, 0x00151f80u, 0xc0000000u, 0x000013b0u, 0x00054a90u, 0x000a8190u, 0x000a9990u, 0x000fd0b0u, 0x00150790u, 0x00151f90u, 0xc0000000u, 0x00001380u, 0x00054aa0u, 0x000a81a0u, 0x000a99a0u, 0x000fd080u, 0x001507a0u, 0x00151fa0u, 0xc0000000u, 0x00001390u, 0x00054ab0u, 0x000a81b0u, 0x000a99b0u, 0x000fd090u, 0x001507b0u, 0x00151fb0u, 0xc0000000u, 0x00001520u, 0x00054c20u, 0x000a8300u, 0x000a9b00u, 0x000fd220u, 0x00150900u, 0x001a4000u, 0xc0000000u, 0x00001530u, 0x00054c30u, 0x000a8310u, 0x000a9b10u, 0x000fd230u, 0x00150910u, 0x001a4010u, 0xc0000000u, 0x00001500u, 0x00054c00u, 0x000a8320u, 0x000a9b20u, 0x000fd200u, 0x00150920u, 0x001a4020u, 0xc0000000u, 0x00001510u, 0x00054c10u, 0x000a8330u, 0x000a9b30u, 0x000fd210u, 0x00150930u, 0x001a4030u, 0xc0000000u, 0x000016a0u, 0x00054da0u, 0x000a8480u, 0x000a9c80u, 0x000fd3a0u, 0x00150a80u, 0x001a4180u, 0xc0000000u, 0x000016b0u, 0x00054db0u, 0x000a8490u, 0x000a9c90u, 0x000fd3b0u, 0x00150a90u, 0x001a4190u, 0xc0000000u, 0x00001680u, 0x00054d80u, 0x000a84a0u, 0x000a9ca0u, 0x000fd380u, 0x00150aa0u, 0x001a41a0u, 0xc0000000u, 0x00001690u, 0x00054d90u, 0x000a84b0u, 0x000a9cb0u, 0x000fd390u, 0x00150ab0u, 0x001a41b0u, 0xc0000000u, 0xc0000000u, 0x00001800u, 0x00054f20u, 0x000a8600u, 0x000a9e00u, 0x000fd520u, 0x00150c20u, 0xc0000000u, 0xc0000000u, 0x00001810u, 0x00054f30u, 0x000a8610u, 0x000a9e10u, 0x000fd530u, 0x00150c30u, 0xc0000000u, 0xc0000000u, 0x00001820u, 0x00054f00u, 0x000a8620u, 0x000a9e20u, 0x000fd500u, 0x00150c00u, 0xc0000000u, 0xc0000000u, 0x00001830u, 0x00054f10u, 0x000a8630u, 0x000a9e30u, 0x000fd510u, 0x00150c10u, 0xc0000000u, 0xc0000000u, 0x00001980u, 0x000550a0u, 0x000a8780u, 0x000a9f80u, 0x000fd6a0u, 0x00150da0u, 0xc0000000u, 0xc0000000u, 0x00001990u, 0x000550b0u, 0x000a8790u, 0x000a9f90u, 0x000fd6b0u, 0x00150db0u, 0xc0000000u, 0xc0000000u, 0x000019a0u, 0x00055080u, 0x000a87a0u, 0x000a9fa0u, 0x000fd680u, 0x00150d80u, 0xc0000000u, 0xc0000000u, 0x000019b0u, 0x00055090u, 0x000a87b0u, 0x000a9fb0u, 0x000fd690u, 0x00150d90u, 0xc0000000u, 0xc0000000u, 0x00001b00u, 0x00055220u, 0x000a8900u, 0xc0000000u, 0x000fd800u, 0x00150f20u, 0xc0000000u, 0xc0000000u, 0x00001b10u, 0x00055230u, 0x000a8910u, 0xc0000000u, 0x000fd810u, 0x00150f30u, 0xc0000000u, 0xc0000000u, 0x00001b20u, 0x00055200u, 0x000a8920u, 0xc0000000u, 0x000fd820u, 0x00150f00u, 0xc0000000u, 0xc0000000u, 0x00001b30u, 0x00055210u, 0x000a8930u, 0xc0000000u, 0x000fd830u, 0x00150f10u, 0xc0000000u, 0x00000480u, 0x00001c80u, 0x000553a0u, 0x000a8a80u, 0xc0000000u, 0x000fd980u, 0x001510a0u, 0xc0000000u, 0x00000490u, 0x00001c90u, 0x000553b0u, 0x000a8a90u, 0xc0000000u, 0x000fd990u, 0x001510b0u, 0xc0000000u, 0x000004a0u, 0x00001ca0u, 0x00055380u, 0x000a8aa0u, 0xc0000000u, 0x000fd9a0u, 0x00151080u, 0xc0000000u, 0x000004b0u, 0x00001cb0u, 0x00055390u, 0x000a8ab0u, 0xc0000000u, 0x000fd9b0u, 0x00151090u, 0xc0000000u, 0x00000600u, 0x00001e00u, 0x00055520u, 0x000a8c20u, 0xc0000000u, 0x000fdb00u, 0x00151220u, 0xc0000000u, 0x00000610u, 0x00001e10u, 0x00055530u, 0x000a8c30u, 0xc0000000u, 0x000fdb10u, 0x00151230u, 0xc0000000u, 0x00000620u, 0x00001e20u, 0x00055500u, 0x000a8c00u, 0xc0000000u, 0x000fdb20u, 0x00151200u, 0xc0000000u, 0x00000630u, 0x00001e30u, 0x00055510u, 0x000a8c10u, 0xc0000000u, 0x000fdb30u, 0x00151210u, 0xc0000000u, 0x00000780u, 0x00001f80u, 0x000556a0u, 0x000a8da0u, 0x000fc480u, 0x000fdc80u, 0x001513a0u, 0xc0000000u, 0x00000790u, 0x00001f90u, 0x000556b0u, 0x000a8db0u, 0x000fc490u, 0x000fdc90u, 0x001513b0u, 0xc0000000u, 0x000007a0u, 0x00001fa0u, 0x00055680u, 0x000a8d80u, 0x000fc4a0u, 0x000fdca0u, 0x00151380u, 0xc0000000u, 0x000007b0u, 0x00001fb0u, 0x00055690u, 0x000a8d90u, 0x000fc4b0u, 0x000fdcb0u, 0x00151390u, 0xc0000000u, 0x00000900u, 0xc0000000u, 0x00055800u, 0x000a8f20u, 0x000fc600u, 0x000fde00u, 0x00151520u, 0xc0000000u, 0x00000910u, 0xc0000000u, 0x00055810u, 0x000a8f30u, 0x000fc610u, 0x000fde10u, 0x00151530u, 0xc0000000u, 0x00000920u, 0xc0000000u, 0x00055820u, 0x000a8f00u, 0x000fc620u, 0x000fde20u, 0x00151500u, 0xc0000000u, 0x00000930u, 0xc0000000u, 0x00055830u, 0x000a8f10u, 0x000fc630u, 0x000fde30u, 0x00151510u, 0xc0000000u, 0x00000a80u, 0xc0000000u, 0x00055980u, 0x000a90a0u, 0x000fc780u, 0x000fdf80u, 0x001516a0u, 0xc0000000u, 0x00000a90u, 0xc0000000u, 0x00055990u, 0x000a90b0u, 0x000fc790u, 0x000fdf90u, 0x001516b0u, 0xc0000000u, 0x00000aa0u, 0xc0000000u, 0x000559a0u, 0x000a9080u, 0x000fc7a0u, 0x000fdfa0u, 0x00151680u, 0xc0000000u, 0x00000ab0u, 0xc0000000u, 0x000559b0u, 0x000a9090u, 0x000fc7b0u, 0x000fdfb0u, 0x00151690u, 0xc0000000u, 0x00000c20u, 0xc0000000u, 0x00055b00u, 0x000a9220u, 0x000fc900u, 0xc0000000u, 0x00151800u, 0xc0000000u, 0x00000c30u, 0xc0000000u, 0x00055b10u, 0x000a9230u, 0x000fc910u, 0xc0000000u, 0x00151810u, 0xc0000000u, 0x00000c00u, 0xc0000000u, 0x00055b20u, 0x000a9200u, 0x000fc920u, 0xc0000000u, 0x00151820u, 0xc0000000u, 0x00000c10u, 0xc0000000u, 0x00055b30u, 0x000a9210u, 0x000fc930u, 0xc0000000u, 0x00151830u, 0xc0000000u, 0x00000da0u, 0x00054480u, 0x00055c80u, 0x000a93a0u, 0x000fca80u, 0xc0000000u, 0x00151980u, 0xc0000000u, 0x00000db0u, 0x00054490u, 0x00055c90u, 0x000a93b0u, 0x000fca90u, 0xc0000000u, 0x00151990u, 0xc0000000u, 0x00000d80u, 0x000544a0u, 0x00055ca0u, 0x000a9380u, 0x000fcaa0u, 0xc0000000u, 0x001519a0u, 0xc0000000u, 0x00000d90u, 0x000544b0u, 0x00055cb0u, 0x000a9390u, 0x000fcab0u, 0xc0000000u, 0x001519b0u, 0xc0000000u, 0x00000f20u, 0x00054600u, 0x00055e00u, 0x000a9520u, 0x000fcc20u, 0xc0000000u, 0x00151b00u, 0xc0000000u, 0x00000f30u, 0x00054610u, 0x00055e10u, 0x000a9530u, 0x000fcc30u, 0xc0000000u, 0x00151b10u, 0xc0000000u, 0x00000f00u, 0x00054620u, 0x00055e20u, 0x000a9500u, 0x000fcc00u, 0xc0000000u, 0x00151b20u, 0xc0000000u, 0x00000f10u, 0x00054630u, 0x00055e30u, 0x000a9510u, 0x000fcc10u, 0xc0000000u, 0x00151b30u, 0xc0000000u, 0x000010a0u, 0x00054780u, 0x00055f80u, 0x000a96a0u, 0x000fcda0u, 0x00150480u, 0x00151c80u, 0xc0000000u, 0x000010b0u, 0x00054790u, 0x00055f90u, 0x000a96b0u, 0x000fcdb0u, 0x00150490u, 0x00151c90u, 0xc0000000u, 0x00001080u, 0x000547a0u, 0x00055fa0u, 0x000a9680u, 0x000fcd80u, 0x001504a0u, 0x00151ca0u, 0xc0000000u, 0x00001090u, 0x000547b0u, 0x00055fb0u, 0x000a9690u, 0x000fcd90u, 0x001504b0u, 0x00151cb0u, 0xc0000000u, 0x00001220u, 0x00054900u, 0xc0000000u, 0x000a9800u, 0x000fcf20u, 0x00150600u, 0x00151e00u, 0xc0000000u, 0x00001230u, 0x00054910u, 0xc0000000u, 0x000a9810u, 0x000fcf30u, 0x00150610u, 0x00151e10u, 0xc0000000u, 0x00001200u, 0x00054920u, 0xc0000000u, 0x000a9820u, 0x000fcf00u, 0x00150620u, 0x00151e20u, 0xc0000000u, 0x00001210u, 0x00054930u, 0xc0000000u, 0x000a9830u, 0x000fcf10u, 0x00150630u, 0x00151e30u, 0xc0000000u, 0x000013a0u, 0x00054a80u, 0xc0000000u, 0x000a9980u, 0x000fd0a0u, 0x00150780u, 0x00151f80u, 0xc0000000u, 0x000013b0u, 0x00054a90u, 0xc0000000u, 0x000a9990u, 0x000fd0b0u, 0x00150790u, 0x00151f90u, 0xc0000000u, 0x00001380u, 0x00054aa0u, 0xc0000000u, 0x000a99a0u, 0x000fd080u, 0x001507a0u, 0x00151fa0u, 0xc0000000u, 0x00001390u, 0x00054ab0u, 0xc0000000u, 0x000a99b0u, 0x000fd090u, 0x001507b0u, 0x00151fb0u, 0xc0000000u, 0x00001520u, 0x00054c20u, 0xc0000000u, 0x000a9b00u, 0x000fd220u, 0x00150900u, 0xc0000000u, 0xc0000000u, 0x00001530u, 0x00054c30u, 0xc0000000u, 0x000a9b10u, 0x000fd230u, 0x00150910u, 0xc0000000u, 0xc0000000u, 0x00001500u, 0x00054c00u, 0xc0000000u, 0x000a9b20u, 0x000fd200u, 0x00150920u, 0xc0000000u, 0xc0000000u, 0x00001510u, 0x00054c10u, 0xc0000000u, 0x000a9b30u, 0x000fd210u, 0x00150930u, 0xc0000000u, 0xc0000000u, 0x000016a0u, 0x00054da0u, 0x000a8480u, 0x000a9c80u, 0x000fd3a0u, 0x00150a80u, 0xc0000000u, 0xc0000000u, 0x000016b0u, 0x00054db0u, 0x000a8490u, 0x000a9c90u, 0x000fd3b0u, 0x00150a90u, 0xc0000000u, 0xc0000000u, 0x00001680u, 0x00054d80u, 0x000a84a0u, 0x000a9ca0u, 0x000fd380u, 0x00150aa0u, 0xc0000000u, 0xc0000000u, 0x00001690u, 0x00054d90u, 0x000a84b0u, 0x000a9cb0u, 0x000fd390u, 0x00150ab0u, 0xc0000000u, 0xc0000000u, 0x00000000u, 0x00001800u, 0x00054f20u, 0x000a8600u, 0xc0000000u, 0x000fd520u, 0x00150c20u, 0xc0000000u, 0x00000010u, 0x00001810u, 0x00054f30u, 0x000a8610u, 0xc0000000u, 0x000fd530u, 0x00150c30u, 0xc0000000u, 0x00000020u, 0x00001820u, 0x00054f00u, 0x000a8620u, 0xc0000000u, 0x000fd500u, 0x00150c00u, 0xc0000000u, 0x00000030u, 0x00001830u, 0x00054f10u, 0x000a8630u, 0xc0000000u, 0x000fd510u, 0x00150c10u, 0xc0000000u, 0x00000180u, 0x00001980u, 0x000550a0u, 0x000a8780u, 0xc0000000u, 0x000fd6a0u, 0x00150da0u, 0xc0000000u, 0x00000190u, 0x00001990u, 0x000550b0u, 0x000a8790u, 0xc0000000u, 0x000fd6b0u, 0x00150db0u, 0xc0000000u, 0x000001a0u, 0x000019a0u, 0x00055080u, 0x000a87a0u, 0xc0000000u, 0x000fd680u, 0x00150d80u, 0xc0000000u, 0x000001b0u, 0x000019b0u, 0x00055090u, 0x000a87b0u, 0xc0000000u, 0x000fd690u, 0x00150d90u, 0xc0000000u, 0x00000300u, 0x00001b00u, 0x00055220u, 0x000a8900u, 0x000fc000u, 0x000fd800u, 0x00150f20u, 0xc0000000u, 0x00000310u, 0x00001b10u, 0x00055230u, 0x000a8910u, 0x000fc010u, 0x000fd810u, 0x00150f30u, 0xc0000000u, 0x00000320u, 0x00001b20u, 0x00055200u, 0x000a8920u, 0x000fc020u, 0x000fd820u, 0x00150f00u, 0xc0000000u, 0x00000330u, 0x00001b30u, 0x00055210u, 0x000a8930u, 0x000fc030u, 0x000fd830u, 0x00150f10u, 0xc0000000u, 0x00000480u, 0xc0000000u, 0x000553a0u, 0x000a8a80u, 0x000fc180u, 0x000fd980u, 0x001510a0u, 0xc0000000u, 0x00000490u, 0xc0000000u, 0x000553b0u, 0x000a8a90u, 0x000fc190u, 0x000fd990u, 0x001510b0u, 0xc0000000u, 0x000004a0u, 0xc0000000u, 0x00055380u, 0x000a8aa0u, 0x000fc1a0u, 0x000fd9a0u, 0x00151080u, 0xc0000000u, 0x000004b0u, 0xc0000000u, 0x00055390u, 0x000a8ab0u, 0x000fc1b0u, 0x000fd9b0u, 0x00151090u, 0xc0000000u, 0x00000600u, 0xc0000000u, 0x00055520u, 0x000a8c20u, 0x000fc300u, 0x000fdb00u, 0x00151220u, 0xc0000000u, 0x00000610u, 0xc0000000u, 0x00055530u, 0x000a8c30u, 0x000fc310u, 0x000fdb10u, 0x00151230u, 0xc0000000u, 0x00000620u, 0xc0000000u, 0x00055500u, 0x000a8c00u, 0x000fc320u, 0x000fdb20u, 0x00151200u, 0xc0000000u, 0x00000630u, 0xc0000000u, 0x00055510u, 0x000a8c10u, 0x000fc330u, 0x000fdb30u, 0x00151210u, 0xc0000000u, 0x00000780u, 0xc0000000u, 0x000556a0u, 0x000a8da0u, 0x000fc480u, 0xc0000000u, 0x001513a0u, 0xc0000000u, 0x00000790u, 0xc0000000u, 0x000556b0u, 0x000a8db0u, 0x000fc490u, 0xc0000000u, 0x001513b0u, 0xc0000000u, 0x000007a0u, 0xc0000000u, 0x00055680u, 0x000a8d80u, 0x000fc4a0u, 0xc0000000u, 0x00151380u, 0xc0000000u, 0x000007b0u, 0xc0000000u, 0x00055690u, 0x000a8d90u, 0x000fc4b0u, 0xc0000000u, 0x00151390u, 0xc0000000u, 0x00000900u, 0x00054000u, 0x00055800u, 0x000a8f20u, 0x000fc600u, 0xc0000000u, 0x00151520u, 0xc0000000u, 0x00000910u, 0x00054010u, 0x00055810u, 0x000a8f30u, 0x000fc610u, 0xc0000000u, 0x00151530u, 0xc0000000u, 0x00000920u, 0x00054020u, 0x00055820u, 0x000a8f00u, 0x000fc620u, 0xc0000000u, 0x00151500u, 0xc0000000u, 0x00000930u, 0x00054030u, 0x00055830u, 0x000a8f10u, 0x000fc630u, 0xc0000000u, 0x00151510u, 0xc0000000u, 0x00000a80u, 0x00054180u, 0x00055980u, 0x000a90a0u, 0x000fc780u, 0xc0000000u, 0x001516a0u, 0xc0000000u, 0x00000a90u, 0x00054190u, 0x00055990u, 0x000a90b0u, 0x000fc790u, 0xc0000000u, 0x001516b0u, 0xc0000000u, 0x00000aa0u, 0x000541a0u, 0x000559a0u, 0x000a9080u, 0x000fc7a0u, 0xc0000000u, 0x00151680u, 0xc0000000u, 0x00000ab0u, 0x000541b0u, 0x000559b0u, 0x000a9090u, 0x000fc7b0u, 0xc0000000u, 0x00151690u, 0xc0000000u, 0x00000c20u, 0x00054300u, 0x00055b00u, 0x000a9220u, 0x000fc900u, 0x00150000u, 0x00151800u, 0xc0000000u, 0x00000c30u, 0x00054310u, 0x00055b10u, 0x000a9230u, 0x000fc910u, 0x00150010u, 0x00151810u, 0xc0000000u, 0x00000c00u, 0x00054320u, 0x00055b20u, 0x000a9200u, 0x000fc920u, 0x00150020u, 0x00151820u, 0xc0000000u, 0x00000c10u, 0x00054330u, 0x00055b30u, 0x000a9210u, 0x000fc930u, 0x00150030u, 0x00151830u, 0xc0000000u, 0x00000da0u, 0x00054480u, 0xc0000000u, 0x000a93a0u, 0x000fca80u, 0x00150180u, 0x00151980u, 0xc0000000u, 0x00000db0u, 0x00054490u, 0xc0000000u, 0x000a93b0u, 0x000fca90u, 0x00150190u, 0x00151990u, 0xc0000000u, 0x00000d80u, 0x000544a0u, 0xc0000000u, 0x000a9380u, 0x000fcaa0u, 0x001501a0u, 0x001519a0u, 0xc0000000u, 0x00000d90u, 0x000544b0u, 0xc0000000u, 0x000a9390u, 0x000fcab0u, 0x001501b0u, 0x001519b0u, 0xc0000000u, 0x00000f20u, 0x00054600u, 0xc0000000u, 0x000a9520u, 0x000fcc20u, 0x00150300u, 0x00151b00u, 0xc0000000u, 0x00000f30u, 0x00054610u, 0xc0000000u, 0x000a9530u, 0x000fcc30u, 0x00150310u, 0x00151b10u, 0xc0000000u, 0x00000f00u, 0x00054620u, 0xc0000000u, 0x000a9500u, 0x000fcc00u, 0x00150320u, 0x00151b20u, 0xc0000000u, 0x00000f10u, 0x00054630u, 0xc0000000u, 0x000a9510u, 0x000fcc10u, 0x00150330u, 0x00151b30u, 0xc0000000u, 0x000010a0u, 0x00054780u, 0xc0000000u, 0x000a96a0u, 0x000fcda0u, 0x00150480u, 0xc0000000u, 0xc0000000u, 0x000010b0u, 0x00054790u, 0xc0000000u, 0x000a96b0u, 0x000fcdb0u, 0x00150490u, 0xc0000000u, 0xc0000000u, 0x00001080u, 0x000547a0u, 0xc0000000u, 0x000a9680u, 0x000fcd80u, 0x001504a0u, 0xc0000000u, 0xc0000000u, 0x00001090u, 0x000547b0u, 0xc0000000u, 0x000a9690u, 0x000fcd90u, 0x001504b0u, 0xc0000000u, 0xc0000000u, 0x00001220u, 0x00054900u, 0x000a8000u, 0x000a9800u, 0x000fcf20u, 0x00150600u, 0xc0000000u, 0xc0000000u, 0x00001230u, 0x00054910u, 0x000a8010u, 0x000a9810u, 0x000fcf30u, 0x00150610u, 0xc0000000u, 0xc0000000u, 0x00001200u, 0x00054920u, 0x000a8020u, 0x000a9820u, 0x000fcf00u, 0x00150620u, 0xc0000000u, 0xc0000000u, 0x00001210u, 0x00054930u, 0x000a8030u, 0x000a9830u, 0x000fcf10u, 0x00150630u, 0xc0000000u, 0xc0000000u, 0x000013a0u, 0x00054a80u, 0x000a8180u, 0x000a9980u, 0x000fd0a0u, 0x00150780u, 0xc0000000u, 0xc0000000u, 0x000013b0u, 0x00054a90u, 0x000a8190u, 0x000a9990u, 0x000fd0b0u, 0x00150790u, 0xc0000000u, 0xc0000000u, 0x00001380u, 0x00054aa0u, 0x000a81a0u, 0x000a99a0u, 0x000fd080u, 0x001507a0u, 0xc0000000u, 0xc0000000u, 0x00001390u, 0x00054ab0u, 0x000a81b0u, 0x000a99b0u, 0x000fd090u, 0x001507b0u, 0xc0000000u, 0xc0000000u, 0x00001520u, 0x00054c20u, 0x000a8300u, 0x000a9b00u, 0x000fd220u, 0x00150900u, 0x001a4000u, 0xc0000000u, 0x00001530u, 0x00054c30u, 0x000a8310u, 0x000a9b10u, 0x000fd230u, 0x00150910u, 0x001a4010u, 0xc0000000u, 0x00001500u, 0x00054c00u, 0x000a8320u, 0x000a9b20u, 0x000fd200u, 0x00150920u, 0x001a4020u, 0xc0000000u, 0x00001510u, 0x00054c10u, 0x000a8330u, 0x000a9b30u, 0x000fd210u, 0x00150930u, 0x001a4030u, 0xc0000000u, 0x000016a0u, 0x00054da0u, 0x000a8480u, 0xc0000000u, 0x000fd3a0u, 0x00150a80u, 0x001a4180u, 0xc0000000u, 0x000016b0u, 0x00054db0u, 0x000a8490u, 0xc0000000u, 0x000fd3b0u, 0x00150a90u, 0x001a4190u, 0xc0000000u, 0x00001680u, 0x00054d80u, 0x000a84a0u, 0xc0000000u, 0x000fd380u, 0x00150aa0u, 0x001a41a0u, 0xc0000000u, 0x00001690u, 0x00054d90u, 0x000a84b0u, 0xc0000000u, 0x000fd390u, 0x00150ab0u, 0x001a41b0u, 0xc0000000u};

typedef unsigned u32x4 __attribute__((ext_vector_type(4)));
typedef unsigned u32x2 __attribute__((ext_vector_type(2)));
typedef float f32x2 __attribute__((ext_vector_type(2)));
typedef __bf16 bf16x2 __attribute__((ext_vector_type(2)));
typedef __attribute__((address_space(1))) unsigned long long gu64;

__device__ __forceinline__ unsigned pk2(f32x2 v) { return __builtin_bit_cast(unsigned, __builtin_convertvector(v, bf16x2)); }
__device__ __forceinline__ f32x2 up2(unsigned u) { f32x2 r = {__uint_as_float(u << 16), __uint_as_float(u & 0xffff0000u)}; return r; }

template <int A0, int A1>
__device__ __forceinline__ void put(const u32x4 (&r)[8], unsigned wr, unsigned rd, unsigned l16, char* dst) {
#pragma unroll
  for (int a = A0; a < A1; ++a) asm volatile("ds_write_b128 %0, %1 offset:%2" :: "v"(wr), "v"(r[a]), "i"(a * 2048) : "memory");
  asm volatile("s_waitcnt lgkmcnt(0)\n\ts_barrier" ::: "memory");
#pragma unroll
  for (int a = A0; a < A1; a += 2) {
    u32x2 l0, h0, l1, h1;
    asm volatile("ds_read_b64_tr_b16 %0, %4 offset:%5\n\tds_read_b64_tr_b16 %1, %4 offset:%6\n\tds_read_b64_tr_b16 %2, %4 offset:%7\n\tds_read_b64_tr_b16 %3, %4 offset:%8\n\ts_waitcnt lgkmcnt(0)"
                 : "=&v"(l0), "=&v"(h0), "=&v"(l1), "=&v"(h1) : "v"(rd), "i"(a * 2048), "i"(a * 2048 + 1024), "i"(a * 2048 + 2048), "i"(a * 2048 + 3072) : "memory");
    u32x4 v0 = {l0[0], l0[1], h0[0], h0[1]}, v1 = {l1[0], l1[1], h1[0], h1[1]};
    asm volatile("global_store_dwordx4 %0, %1, %4 sc1\n\tglobal_store_dwordx4 %2, %3, %4 sc1\n\ts_nop 1"
                 :: "v"(l16 + a * 6 * 21504), "v"(v0), "v"(l16 + (a + 1) * 6 * 21504), "v"(v1), "s"(dst) : "memory");
  }
}

__device__ __forceinline__ void producer(const unsigned short* __restrict__ w, char* ws, char* lds) {
  const int tid = threadIdx.x, pb = blockIdx.x;
  asm volatile("s_getpc_b64 s[20:21]\n.Lcpf:\n\t"
               "v_min_u32 v250, .Lcons_end-.Lcpf-4, %0\n\tglobal_load_dword v251, v250, s[20:21]\n\tglobal_load_dword v254, %1, %2"
               :: "v"((((tid & 63) < 20 ? (tid & 63) : 19) * 16 + (tid >> 6) * 4 + (pb >> 3)) * 128),
                  "v"((((tid & 63) < 5 ? (tid & 63) : 4) * 16 + (tid >> 6) * 4 + (pb >> 3)) * 128), "s"((const void*)kT) : "s20", "s21", "v250", "v251", "v254", "memory");
  __builtin_amdgcn_sched_barrier(0);
  const int half = __builtin_amdgcn_readfirstlane(tid >> 7);
  const int u = min(pb * 2 + half, 62);
  const int i = u / 9, c = (u % 9) / 3, th = u % 3, k = (tid & 127) >> 2, q = tid & 3;
  const char* src = (const char*)w + (i * 7 * 96 + c * 32) * 192 + th * 64;
  const unsigned so = k * 192 + q * 16;
  u32x4 g[7];
#pragma unroll
  for (int jj = 0; jj < 7; ++jj) { const int j = (jj + 6) % 7; asm volatile("global_load_dwordx4 %0, %1, %2" : "=v"(g[j]) : "v"(so), "s"(src + j * 18432) : "memory"); }
  unsigned long long ep;
  asm volatile("global_load_dwordx2 %0, %1, %2 sc1" : "=v"(ep) : "v"(0u), "s"(ws) : "memory");
  gu64* words = (gu64*)(ws + 4096);
  gu64* flags = (gu64*)(ws + 1024);
  const int lane = tid & 63, f = __builtin_amdgcn_readfirstlane((tid >> 6) & 1), pp = lane & 3;
  unsigned wr = (unsigned)(size_t)lds + half * 16384 + k * 64 + q * 16;
  unsigned rd = (unsigned)(size_t)lds + half * 16384 + (lane >> 2) * 64 + ((th == 1 ? 32 : 8) * f + pp * (th == 1 ? 8 : 16));
  unsigned l16 = lane * 16;
  const int nh = th == 1 ? f : (th >> 1), t = th == 1 ? 2 : f;
  char* dst = ws + 65536 + (c * 2 + nh) * 21504 + (t * 7 + i) * 1024;
  u32x4 r[8];
  asm volatile("s_waitcnt vmcnt(6)" : "+v"(g[0]), "+v"(g[6]), "+v"(wr), "+v"(rd), "+v"(l16), "+s"(dst) :: "memory");
  r[6] = g[0]; r[7] = g[6];
  put<6, 8>(r, wr, rd, l16, dst);
  asm volatile("s_waitcnt vmcnt(3)" : "+v"(g[1]), "+v"(g[2]), "+v"(g[3]), "+v"(g[4]), "+v"(g[5]) :: "memory");
  f32x2 eh[4], oh[4], e2[4], o2[4], a2[4], b2[4];
#pragma unroll
  for (int d = 0; d < 4; ++d) {
    f32x2 x[7];
#pragma unroll
    for (int j = 0; j < 7; ++j) x[j] = up2(g[j][d]);
    eh[d] = (64.f / 90.f) * x[0] + ((16.f / 90.f) * x[2] + ((4.f / 90.f) * x[4] + (1.f / 90.f) * x[6]));
    oh[d] = (32.f / 90.f) * x[1] + ((8.f / 90.f) * x[3] + (2.f / 90.f) * x[5]);
    e2[d] = (64.f / 45.f) * x[6] + ((16.f / 45.f) * x[4] + ((4.f / 45.f) * x[2] + (1.f / 45.f) * x[0]));
    o2[d] = (32.f / 45.f) * x[5] + ((8.f / 45.f) * x[3] + (2.f / 45.f) * x[1]);
    a2[d] = eh[d] + oh[d]; b2[d] = e2[d] + o2[d];
    r[0][d] = pk2(a2[d]); r[1][d] = pk2(b2[d]);
  }
  asm volatile("s_waitcnt vmcnt(0)\n\ts_barrier" : "+v"(ep), "+v"(r[0]), "+v"(r[1]), "+v"(g[0]), "+v"(g[1]), "+v"(g[2]), "+v"(g[3]), "+v"(g[4]), "+v"(g[5]), "+v"(g[6]) :: "memory");
  const unsigned long long tag = ep + 1ull;
  if (tid == 0) __hip_atomic_store(flags + 4 * pb, tag, __ATOMIC_RELAXED, __HIP_MEMORY_SCOPE_AGENT);
  put<0, 2>(r, wr, rd, l16, dst);
  asm volatile("" : "+v"(g[0]), "+v"(g[1]), "+v"(g[2]), "+v"(g[3]), "+v"(g[4]), "+v"(g[5]), "+v"(g[6]), "+v"(r[0]), "+v"(r[1]) :: "memory");
#pragma unroll
  for (int d = 0; d < 4; ++d) {
    f32x2 x[7];
#pragma unroll
    for (int j = 0; j < 7; ++j) x[j] = up2(g[j][d]);
    const f32x2 e1 = (x[0] + x[2]) + (x[4] + x[6]), o1 = (x[1] + x[3]) + x[5];
    const f32x2 a3 = eh[d] - oh[d], b3 = o2[d] - e2[d];
    r[2][d] = pk2(a3); r[3][d] = pk2(b3);
    const f32x2 p = (up2(r[0][d]) - a2[d]) + 0.5f * (up2(r[1][d]) - b2[d]), m = (up2(r[2][d]) - a3) - 0.5f * (up2(r[3][d]) - b3);
    r[4][d] = pk2((e1 + o1) * (-2.f / 9.f) - (0.735981f * p + 0.081776f * m));
    r[5][d] = pk2((e1 - o1) * (2.f / 9.f) + (0.735981f * m + 0.081776f * p));
  }
  asm volatile("s_waitcnt vmcnt(0)\n\ts_barrier" : "+v"(r[2]), "+v"(r[3]), "+v"(r[4]), "+v"(r[5]) :: "memory");
  if (tid == 0) __hip_atomic_store(flags + 128 + 4 * pb, tag, __ATOMIC_RELAXED, __HIP_MEMORY_SCOPE_AGENT);
  put<2, 6>(r, wr, rd, l16, dst);
  asm volatile("s_waitcnt vmcnt(0)\n\ts_barrier" ::: "memory");
  if (tid == 0) __hip_atomic_store(flags + 256 + 4 * pb, tag, __ATOMIC_RELAXED, __HIP_MEMORY_SCOPE_AGENT);
  if (tid == 0) __hip_atomic_store(words + 16 * pb, tag, __ATOMIC_RELAXED, __HIP_MEMORY_SCOPE_AGENT);
  if (pb == 0) {
#pragma nounroll
    for (int s = 0; s < 0x40000; ++s) {
      const bool ok = __hip_atomic_load(words + 16 * tid, __ATOMIC_RELAXED, __HIP_MEMORY_SCOPE_AGENT) == tag;
      if (__all(ok)) break;
      __builtin_amdgcn_s_sleep(8);
    }
    __syncthreads();
    if (tid == 0) {
      __hip_atomic_store((gu64*)ws, tag, __ATOMIC_RELAXED, __HIP_MEMORY_SCOPE_AGENT);
      asm volatile("s_endpgm\n\t.p2align 12" ::: "memory");
    }
  }
}


__global__ __launch_bounds__(256, 1) __attribute__((aligned(4096))) void dilated_conv7_kernel(const void* __restrict__ x, const void* __restrict__ w, void* __restrict__ o, void* __restrict__ ws) {
  __shared__ __attribute__((aligned(16))) char lds[163840];
  asm volatile("s_load_dwordx8 s[40:47], %[ka], 0x0" :: [ka] "s"(__builtin_amdgcn_kernarg_segment_ptr()) : "s40", "s41", "s42", "s43", "s44", "s45", "s46", "s47", "memory");
  if (__builtin_expect(blockIdx.x < 32, 1)) { producer((const unsigned short*)w, (char*)ws, lds); return; }
  asm volatile(
    ".Lcons_start:\n"
    "v_lshrrev_b32 v5, 6, %[tid]\n"
    "v_and_b32 v4, 63, %[tid]\n"
    "s_bfe_u32 s51, %[bid], 0x10003\n"
    "v_readfirstlane_b32 s48, v5\n"
    "s_and_b32 s54, %[bid], 6\n"
    "s_bfe_u32 s55, %[bid], 0x10004\n"
    "s_add_u32 s54, s54, s55\n"
    "s_mul_i32 s52, s54, 14\n"
    "s_lshr_b32 s55, %[bid], 5\n"
    "s_lshl_b32 s53, s55, 4\n"
    "s_sub_u32 s53, s53, 19\n"
    "s_lshl_b32 s59, s51, 6\n"
    "s_mul_i32 s69, s48, 30720\n"
    "s_add_u32 s54, s48, s52\n"
    "s_sub_u32 s54, s54, 3\n"
    "s_mul_i32 s54, s54, 86016\n"
    "s_and_b32 s56, %[bid], 1\n"
    "s_mul_i32 s56, s56, 43008\n"
    "s_mul_i32 s57, s51, 192\n"
    "s_add_u32 s58, s56, s57\n"
    "s_add_u32 s75, s54, s58\n"
    "s_mov_b32 s61, 344064\n"
    "s_mov_b32 s22, 9633792\n"
    "s_mov_b32 s23, 0x20000\n"
    "v_lshrrev_b32 v5, 3, v4\n"
    "v_and_b32 v10, 7, v4\n"
    "v_lshlrev_b32 v10, 4, v10\n"
    "v_and_b32 v8, 16, v4\n"
    "v_lshlrev_b32 v8, 1, v8\n"
    "v_xor_b32 v10, v10, v8\n"
    "v_mov_b32 v11, 3221225472\n"
    "v_add_u32 v7, s53, v5\n"
    "v_cmp_gt_u32 vcc, 112, v7\n"
    "v_mul_u32_u24 v6, 384, v7\n"
    "v_add3_u32 v6, v6, v10, s75\n"
    "v_cndmask_b32 v76, v11, v6, vcc\n"
    "v_add_u32 v7, 8, v7\n"
    "v_xor_b32 v9, 64, v10\n"
    "v_cmp_gt_u32 vcc, 112, v7\n"
    "v_mul_u32_u24 v6, 384, v7\n"
    "v_add3_u32 v6, v6, v9, s75\n"
    "v_cndmask_b32 v77, v11, v6, vcc\n"
    "v_cmp_lt_u32 vcc, 47, v4\n"
    "v_add_u32 v7, 8, v7\n"
    "v_subrev_u32 v6, 22, v7\n"
    "v_xad_u32 v9, v10, 32, s61\n"
    "v_cndmask_b32 v7, v7, v6, vcc\n"
    "v_cndmask_b32 v9, v10, v9, vcc\n"
    "v_cmp_gt_u32 vcc, 112, v7\n"
    "v_mul_u32_u24 v6, 384, v7\n"
    "v_add3_u32 v6, v6, v9, s75\n"
    "v_cndmask_b32 v78, v11, v6, vcc\n"
    "v_lshlrev_b32 v14, 4, v4\n"
    "s_waitcnt lgkmcnt(0)\n"
    "s_mov_b32 s20, s40\n"
    "s_and_b32 s21, s41, 0xffff\n"
    "s_add_u32 m0, s69, 0\n"
    "s_nop 0\n"
    "buffer_load_dwordx4 v76, s[20:23], s59 offen lds\n"
    "s_add_u32 m0, s69, 1024\n"
    "s_nop 0\n"
    "buffer_load_dwordx4 v77, s[20:23], s59 offen lds\n"
    "s_add_u32 m0, s69, 2048\n"
    "s_nop 0\n"
    "buffer_load_dwordx4 v78, s[20:23], s59 offen lds\n"
    "s_add_u32 m0, s69, 2816\n"
    "v_add_u32 v76, s61, v76\n"
    "buffer_load_dwordx4 v76, s[20:23], s59 offen lds\n"
    "s_add_u32 m0, s69, 3840\n"
    "v_add_u32 v77, s61, v77\n"
    "buffer_load_dwordx4 v77, s[20:23], s59 offen lds\n"
    "s_add_u32 m0, s69, 4864\n"
    "v_add_u32 v78, s61, v78\n"
    "buffer_load_dwordx4 v78, s[20:23], s59 offen lds\n"
    "s_add_u32 m0, s69, 5632\n"
    "v_add_u32 v76, s61, v76\n"
    "buffer_load_dwordx4 v76, s[20:23], s59 offen lds\n"
    "s_add_u32 m0, s69, 6656\n"
    "v_add_u32 v77, s61, v77\n"
    "buffer_load_dwordx4 v77, s[20:23], s59 offen lds\n"
    "s_add_u32 m0, s69, 7680\n"
    "v_add_u32 v78, s61, v78\n"
    "buffer_load_dwordx4 v78, s[20:23], s59 offen lds\n"
    "s_mov_b64 exec, 0\n"
    "s_cmp_eq_u32 s48, 0\n"
    "s_cbranch_scc1 .Lpad0\n"
    "s_cmp_eq_u32 s48, 1\n"
    "s_cbranch_scc1 .Lpad1\n"
    "s_cmp_eq_u32 s48, 2\n"
    "s_cbranch_scc1 .Lpad2\n"
    "s_branch .Lpad3\n"
    ".Lhop_done:\n"
    "s_mov_b64 exec, -1\n"
    "s_nop 0\n"
    "s_mov_b32 s24, s42\n"
    "s_and_b32 s25, s43, 0xffff\n"
    "s_mov_b32 s26, 903168\n"
    "s_mov_b32 s27, 0x20000\n"
    "s_lshr_b32 s49, s48, 1\n"
    "s_and_b32 s50, s48, 1\n"
    "s_mul_i32 s54, s49, 110592\n"
    "s_mul_i32 s65, s59, 96\n"
    "s_add_u32 s65, s65, s54\n"
    "s_cmp_eq_u32 s51, 0\n"
    "s_cselect_b32 s60, 128, 0\n"
    "s_add_u32 s66, s65, 6144\n"
    "s_lshl_b32 s56, s50, 7\n"
    "s_lshl_b32 s57, s50, 5\n"
    "v_mov_b32 v9, s56\n"
    "v_mov_b32 v10, s57\n"
    "v_mul_u32_u24 v6, 171, v4\n"
    "v_lshrrev_b32 v6, 10, v6\n"
    "v_mad_i32_i24 v7, v6, -6, v4\n"
    "v_cmp_gt_u32 vcc, 4, v7\n"
    "v_mul_u32_u24 v6, 96, v6\n"
    "v_add_u32 v6, v6, v14\n"
    "v_cndmask_b32 v8, v10, v9, vcc\n"
    "v_add_u32 v30, v6, v8\n"
    "v_add_u32 v5, 64, v4\n"
    "v_mul_u32_u24 v6, 171, v5\n"
    "v_lshrrev_b32 v6, 10, v6\n"
    "v_mad_i32_i24 v7, v6, -6, v5\n"
    "v_cmp_gt_u32 vcc, 4, v7\n"
    "v_mul_u32_u24 v6, 96, v6\n"
    "v_add_u32 v6, v6, v14\n"
    "v_cndmask_b32 v8, v10, v9, vcc\n"
    "v_add_u32 v31, v6, v8\n"
    "v_add_u32 v5, 128, v4\n"
    "v_mul_u32_u24 v6, 171, v5\n"
    "v_lshrrev_b32 v6, 10, v6\n"
    "v_mad_i32_i24 v7, v6, -6, v5\n"
    "v_cmp_gt_u32 vcc, 4, v7\n"
    "v_mul_u32_u24 v6, 96, v6\n"
    "v_add_u32 v6, v6, v14\n"
    "v_cndmask_b32 v8, v10, v9, vcc\n"
    "v_add_u32 v32, v6, v8\n"
    "v_lshrrev_b32 v5, 4, v4\n"
    "v_cmp_eq_u32 vcc, 3, v5\n"
    "v_and_b32 v6, 1, v5\n"
    "v_bfe_u32 v7, v4, 2, 2\n"
    "v_cndmask_b32 v8, v5, v6, vcc\n"
    "v_lshl_add_u32 v8, v8, 3, v7\n"
    "v_and_b32 v9, 3, v4\n"
    "v_lshl_add_u32 v10, v9, 3, s69\n"
    "v_bfe_u32 v11, v4, 3, 2\n"
    "v_lshl_add_u32 v16, v8, 7, v10\n"
    "v_lshl_add_u32 v16, v11, 5, v16\n"
    "s_add_u32 m0, s69, 20480\n"
    "s_add_u32 s67, s65, 0\n"
    "buffer_load_dwordx4 v30, s[24:27], s67 offen offset:0 lds\n"
    "buffer_load_dwordx4 v31, s[24:27], s67 offen offset:1024 lds\n"
    "buffer_load_dwordx4 v32, s[24:27], s67 offen offset:2048 lds\n"
    "v_xor_b32 v17, 32, v16\n"
    "v_xor_b32 v18, 64, v16\n"
    "v_xor_b32 v19, 96, v16\n"
    "v_lshl_add_u32 v28, v8, 6, v10\n"
    "v_lshl_add_u32 v28, v6, 5, v28\n"
    "v_xor_b32 v29, 32, v28\n"
    "v_lshrrev_b32 v7, 2, v4\n"
    "v_mul_u32_u24 v7, 96, v7\n"
    "v_add3_u32 v34, v7, v10, 64\n"
    "v_lshl_add_u32 v10, v9, 3, v10\n"
    "v_add_u32 v33, v7, v10\n"
    "v_and_b32 v6, 7, v4\n"
    "v_lshrrev_b32 v7, 1, v6\n"
    "v_xor_b32 v7, v7, v5\n"
    "v_lshlrev_b32 v7, 4, v7\n"
    "v_lshl_add_u32 v7, v6, 6, v7\n"
    "v_bfe_u32 v8, v4, 3, 1\n"
    "s_lshl_b32 s54, s48, 10\n"
    "s_add_u32 s54, s54, 122880\n"
    "v_lshl_add_u32 v9, v8, 9, v7\n"
    "v_add_u32 v12, s54, v9\n"
    "s_lshl_b32 s54, s49, 9\n"
    "s_add_u32 s54, s54, 122880\n"
    "v_mul_u32_u24 v9, 7168, v8\n"
    "v_add3_u32 v13, v9, v7, s54\n"
    "s_mov_b32 s36, s46\n"
    "s_and_b32 s37, s47, 0xffff\n"
    "s_mov_b32 s38, 1097728\n"
    "s_mov_b32 s39, 0x20000\n"
    "s_mov_b32 s78, 0xc0a83f80\n"
    "s_mov_b32 s79, 0xbf8040a8\n"
    "v_lshlrev_b32 v5, 2, v5\n"
    "v_sub_u32 v5, v5, v6\n"
    "v_lshlrev_b32 v7, 15, v8\n"
    "v_lshlrev_b32 v8, 4, v8\n"
    "v_cmp_gt_u32 vcc, 4, v5\n"
    "v_lshlrev_b32 v6, 4, v5\n"
    "v_lshrrev_b64 v[42:43], v6, s[78:79]\n"
    "v_and_b32 v6, 0xffff, v42\n"
    "v_xor_b32 v6, v6, v7\n"
    "v_lshlrev_b32 v6, v8, v6\n"
    "v_cndmask_b32 v44, 0, v6, vcc\n"
    "v_add_u32 v5, 1, v5\n"
    "v_cmp_gt_u32 vcc, 4, v5\n"
    "v_lshlrev_b32 v6, 4, v5\n"
    "v_lshrrev_b64 v[42:43], v6, s[78:79]\n"
    "v_and_b32 v6, 0xffff, v42\n"
    "v_xor_b32 v6, v6, v7\n"
    "v_lshlrev_b32 v6, v8, v6\n"
    "v_cndmask_b32 v45, 0, v6, vcc\n"
    "v_add_u32 v5, 1, v5\n"
    "v_cmp_gt_u32 vcc, 4, v5\n"
    "v_lshlrev_b32 v6, 4, v5\n"
    "v_lshrrev_b64 v[42:43], v6, s[78:79]\n"
    "v_and_b32 v6, 0xffff, v42\n"
    "v_xor_b32 v6, v6, v7\n"
    "v_lshlrev_b32 v6, v8, v6\n"
    "v_cndmask_b32 v46, 0, v6, vcc\n"
    "v_add_u32 v5, 1, v5\n"
    "v_cmp_gt_u32 vcc, 4, v5\n"
    "v_lshlrev_b32 v6, 4, v5\n"
    "v_lshrrev_b64 v[42:43], v6, s[78:79]\n"
    "v_and_b32 v6, 0xffff, v42\n"
    "v_xor_b32 v6, v6, v7\n"
    "v_lshlrev_b32 v6, v8, v6\n"
    "v_cndmask_b32 v47, 0, v6, vcc\n"
    "s_cmp_eq_i32 s53, -3\n"
    "s_cselect_b32 s54, 3, 2\n"
    "s_cmp_eq_u32 s53, 93\n"
    "s_cselect_b32 s54, 4, s54\n"
    "s_lshl_b32 s54, s54, 11\n"
    "v_lshl_add_u32 v11, v4, 5, s54\n"
    "s_add_u32 s54, s69, 30592\n"
    "v_mov_b32 v35, s54\n"
    "v_and_b32 v36, 3, v4\n"
    "v_mul_u32_u24 v36, 30720, v36\n"
    "v_mov_b32 v37, 0\n"
    "ds_write_b32 v35, v37\n"
    "s_waitcnt vmcnt(9)\n"
    "ds_read_b64_tr_b16 a[200:201], v16 offset:0\n"
    "ds_read_b64_tr_b16 a[202:203], v16 offset:512\n"
    "ds_read_b64_tr_b16 a[204:205], v17 offset:0\n"
    "ds_read_b64_tr_b16 a[206:207], v17 offset:512\n"
    "s_add_u32 m0, s69, 8448\n"
    "v_add_u32 v76, s61, v76\n"
    "buffer_load_dwordx4 v76, s[20:23], s59 offen lds\n"
    "s_add_u32 m0, s69, 9472\n"
    "v_add_u32 v77, s61, v77\n"
    "buffer_load_dwordx4 v77, s[20:23], s59 offen lds\n"
    "s_add_u32 m0, s69, 10496\n"
    "v_add_u32 v78, s61, v78\n"
    "buffer_load_dwordx4 v78, s[20:23], s59 offen lds\n"
    "s_waitcnt vmcnt(9)\n"
    "ds_read_b64_tr_b16 a[208:209], v16 offset:2816\n"
    "ds_read_b64_tr_b16 a[210:211], v16 offset:3328\n"
    "ds_read_b64_tr_b16 a[212:213], v17 offset:2816\n"
    "ds_read_b64_tr_b16 a[214:215], v17 offset:3328\n"
    "s_add_u32 m0, s69, 14336\n"
    "s_add_u32 s67, s65, 129024\n"
    "buffer_load_dwordx4 v30, s[24:27], s67 offen offset:0 lds\n"
    "buffer_load_dwordx4 v31, s[24:27], s67 offen offset:1024 lds\n"
    "buffer_load_dwordx4 v32, s[24:27], s67 offen offset:2048 lds\n"
    "s_waitcnt lgkmcnt(4)\n"
    "v_mfma_f32_16x16x32_bf16 v[176:179], a[204:207], v[44:47], 0\n"
    "v_mfma_f32_16x16x32_bf16 v[172:175], a[200:203], v[44:47], 0\n"
    "s_waitcnt vmcnt(9)\n"
    "ds_read_b64_tr_b16 a[216:217], v16 offset:5632\n"
    "ds_read_b64_tr_b16 a[218:219], v16 offset:6144\n"
    "ds_read_b64_tr_b16 a[220:221], v17 offset:5632\n"
    "ds_read_b64_tr_b16 a[222:223], v17 offset:6144\n"
    "s_add_u32 m0, s69, 17408\n"
    "s_add_u32 s67, s65, 258048\n"
    "buffer_load_dwordx4 v30, s[24:27], s67 offen offset:0 lds\n"
    "buffer_load_dwordx4 v31, s[24:27], s67 offen offset:1024 lds\n"
    "buffer_load_dwordx4 v32, s[24:27], s67 offen offset:2048 lds\n"
    "s_waitcnt lgkmcnt(4)\n"
    "v_mfma_f32_16x16x32_bf16 v[184:187], a[212:215], v[44:47], 0\n"
    "v_mfma_f32_16x16x32_bf16 v[180:183], a[208:211], v[44:47], 0\n"
    "v_cvt_pk_bf16_f32 v214, v176, v177\n"
    "v_cvt_pk_bf16_f32 v215, v178, v179\n"
    "v_cvt_pk_bf16_f32 v212, v172, v173\n"
    "v_cvt_pk_bf16_f32 v213, v174, v175\n"
    "ds_write_b128 v12, v[212:215] offset:0\n"
    "s_waitcnt lgkmcnt(1)\n"
    "v_mfma_f32_16x16x32_bf16 v[192:195], a[220:223], v[44:47], 0\n"
    "v_mfma_f32_16x16x32_bf16 v[188:191], a[216:219], v[44:47], 0\n"
    "v_add_u32 v20, s61, v76\n"
    "v_add_u32 v21, s61, v77\n"
    "v_add_u32 v22, s61, v78\n"
    "v_cvt_pk_bf16_f32 v218, v184, v185\n"
    "v_cvt_pk_bf16_f32 v219, v186, v187\n"
    "v_cvt_pk_bf16_f32 v216, v180, v181\n"
    "v_cvt_pk_bf16_f32 v217, v182, v183\n"
    "ds_write_b128 v12, v[216:219] offset:4096\n"
    "v_cvt_pk_bf16_f32 v222, v192, v193\n"
    "v_cvt_pk_bf16_f32 v223, v194, v195\n"
    "v_cvt_pk_bf16_f32 v220, v188, v189\n"
    "v_cvt_pk_bf16_f32 v221, v190, v191\n"
    "ds_write_b128 v12, v[220:223] offset:8192\n"
    "s_waitcnt vmcnt(9)\n"
    "ds_read_b64_tr_b16 a[0:1], v33 offset:20480\n"
    "ds_read_b64_tr_b16 a[2:3], v33 offset:22016\n"
    "ds_read_b64_tr_b16 a[28:29], v33 offset:20488\n"
    "ds_read_b64_tr_b16 a[30:31], v33 offset:22024\n"
    "ds_read_b64_tr_b16 a[56:57], v34 offset:20480\n"
    "ds_read_b64_tr_b16 a[58:59], v34 offset:22016\n"
    "s_waitcnt lgkmcnt(6)\n"
    "s_barrier\n"
    "ds_read_b128 a[168:171], v13 offset:0\n"
    "ds_read_b128 a[172:175], v13 offset:1024\n"
    "ds_read_b128 a[176:179], v13 offset:2048\n"
    "ds_read_b128 a[180:183], v13 offset:3072\n"
    "ds_read_b128 a[184:187], v13 offset:4096\n"
    "s_add_u32 m0, s69, 23552\n"
    "s_add_u32 s67, s65, 387072\n"
    "buffer_load_dwordx4 v30, s[24:27], s67 offen offset:0 lds\n"
    "buffer_load_dwordx4 v31, s[24:27], s67 offen offset:1024 lds\n"
    "buffer_load_dwordx4 v32, s[24:27], s67 offen offset:2048 lds\n"
    "s_waitcnt lgkmcnt(4)\n"
    "v_mfma_f32_16x16x32_bf16 v[88:91], a[0:3], a[168:171], 0\n"
    "s_waitcnt vmcnt(6)\n"
    "ds_read_b64_tr_b16 a[4:5], v33 offset:14336\n"
    "ds_read_b64_tr_b16 a[6:7], v33 offset:15872\n"
    "v_mfma_f32_16x16x32_bf16 v[92:95], a[28:31], a[168:171], 0\n"
    "s_waitcnt lgkmcnt(5)\n"
    "ds_read_b64_tr_b16 a[32:33], v33 offset:14344\n"
    "ds_read_b64_tr_b16 a[34:35], v33 offset:15880\n"
    "v_mfma_f32_16x16x32_bf16 v[96:99], a[56:59], a[168:171], 0\n"
    "s_add_u32 m0, s69, 26624\n"
    "s_add_u32 s67, s65, 516096\n"
    "ds_read_b64_tr_b16 a[60:61], v34 offset:14336\n"
    "ds_read_b64_tr_b16 a[62:63], v34 offset:15872\n"
    "v_mfma_f32_16x16x32_bf16 v[108:111], a[56:59], a[172:175], 0\n"
    "buffer_load_dwordx4 v30, s[24:27], s67 offen offset:0 lds\n"
    "v_mfma_f32_16x16x32_bf16 v[104:107], a[28:31], a[172:175], 0\n"
    "s_waitcnt lgkmcnt(8)\n"
    "v_mfma_f32_16x16x32_bf16 v[100:103], a[0:3], a[172:175], 0\n"
    "buffer_load_dwordx4 v31, s[24:27], s67 offen offset:1024 lds\n"
    "v_mfma_f32_16x16x32_bf16 v[112:115], a[0:3], a[176:179], 0\n"
    "ds_read_b64_tr_b16 a[224:225], v16 offset:8448\n"
    "v_mfma_f32_16x16x32_bf16 v[116:119], a[28:31], a[176:179], 0\n"
    "ds_read_b64_tr_b16 a[226:227], v16 offset:8960\n"
    "v_mfma_f32_16x16x32_bf16 v[120:123], a[56:59], a[176:179], 0\n"
    "s_waitcnt lgkmcnt(8)\n"
    "ds_read_b64_tr_b16 a[228:229], v17 offset:8448\n"
    "v_mfma_f32_16x16x32_bf16 v[132:135], a[56:59], a[180:183], 0\n"
    "ds_read_b64_tr_b16 a[230:231], v17 offset:8960\n"
    "v_mfma_f32_16x16x32_bf16 v[128:131], a[28:31], a[180:183], 0\n"
    "buffer_load_dwordx4 v32, s[24:27], s67 offen offset:2048 lds\n"
    "v_mfma_f32_16x16x32_bf16 v[124:127], a[0:3], a[180:183], 0\n"
    "s_add_u32 m0, s69, 11264\n"
    "ds_read_b64_tr_b16 a[240:241], v18 offset:0\n"
    "v_mfma_f32_16x16x32_bf16 v[136:139], a[0:3], a[184:187], 0\n"
    "s_waitcnt vmcnt(6)\n"
    "ds_read_b64_tr_b16 a[8:9], v33 offset:17408\n"
    "ds_read_b64_tr_b16 a[10:11], v33 offset:18944\n"
    "v_mfma_f32_16x16x32_bf16 v[140:143], a[28:31], a[184:187], 0\n"
    "ds_read_b64_tr_b16 a[36:37], v33 offset:17416\n"
    "ds_read_b64_tr_b16 a[38:39], v33 offset:18952\n"
    "v_mfma_f32_16x16x32_bf16 v[144:147], a[56:59], a[184:187], 0\n"
    "s_waitcnt lgkmcnt(9)\n"
    "ds_read_b64_tr_b16 a[64:65], v34 offset:17408\n"
    "ds_read_b64_tr_b16 a[66:67], v34 offset:18944\n"
    "v_mfma_f32_16x16x32_bf16 v[88:91], a[4:7], a[172:175], v[88:91]\n"
    "s_waitcnt lgkmcnt(7)\n"
    "ds_read_b64_tr_b16 a[242:243], v18 offset:512\n"
    "v_mfma_f32_16x16x32_bf16 v[200:203], a[228:231], v[44:47], 0\n"
    "v_mfma_f32_16x16x32_bf16 v[196:199], a[224:227], v[44:47], 0\n"
    "v_mfma_f32_16x16x32_bf16 v[92:95], a[32:35], a[172:175], v[92:95]\n"
    "buffer_load_dwordx4 v20, s[20:23], s59 offen lds\n"
    "v_mfma_f32_16x16x32_bf16 v[96:99], a[60:63], a[172:175], v[96:99]\n"
    "ds_read_b64_tr_b16 a[244:245], v19 offset:0\n"
    "v_mfma_f32_16x16x32_bf16 v[108:111], a[60:63], a[176:179], v[108:111]\n"
    "s_add_u32 m0, s69, 12288\n"
    "ds_read_b64_tr_b16 a[246:247], v19 offset:512\n"
    "v_mfma_f32_16x16x32_bf16 v[104:107], a[32:35], a[176:179], v[104:107]\n"
    "buffer_load_dwordx4 v21, s[20:23], s59 offen lds\n"
    "v_mfma_f32_16x16x32_bf16 v[100:103], a[4:7], a[176:179], v[100:103]\n"
    "v_mfma_f32_16x16x32_bf16 v[112:115], a[4:7], a[180:183], v[112:115]\n"
    "s_add_u32 m0, s69, 13312\n"
    "v_cvt_pk_bf16_f32 v226, v200, v201\n"
    "v_cvt_pk_bf16_f32 v227, v202, v203\n"
    "v_mfma_f32_16x16x32_bf16 v[116:119], a[32:35], a[180:183], v[116:119]\n"
    "v_cvt_pk_bf16_f32 v224, v196, v197\n"
    "v_cvt_pk_bf16_f32 v225, v198, v199\n"
    "v_mfma_f32_16x16x32_bf16 v[120:123], a[60:63], a[180:183], v[120:123]\n"
    "ds_write_b128 v12, v[224:227] offset:12288\n"
    "v_mfma_f32_16x16x32_bf16 v[132:135], a[60:63], a[184:187], v[132:135]\n"
    "buffer_load_dwordx4 v22, s[20:23], s59 offen lds\n"
    "v_mfma_f32_16x16x32_bf16 v[128:131], a[32:35], a[184:187], v[128:131]\n"
    "s_add_u32 m0, s69, 20480\n"
    "s_add_u32 s67, s65, 645120\n"
    "v_and_b32 v15, 31, v4\n"
    "v_mfma_f32_16x16x32_bf16 v[124:127], a[4:7], a[184:187], v[124:127]\n"
    "buffer_load_dwordx4 v30, s[24:27], s67 offen offset:0 lds\n"
    "s_waitcnt lgkmcnt(2)\n"
    "v_mfma_f32_16x16x32_bf16 v[88:91], a[8:11], a[176:179], v[88:91]\n"
    "s_waitcnt lgkmcnt(0)\n"
    "s_barrier\n"
    "ds_read_b128 a[188:191], v13 offset:5120\n"
    "v_mfma_f32_16x16x32_bf16 v[64:67], a[244:247], v[44:47], 0\n"
    "v_mfma_f32_16x16x32_bf16 v[60:63], a[240:243], v[44:47], 0\n"
    "v_mfma_f32_16x16x32_bf16 v[92:95], a[36:39], a[176:179], v[92:95]\n"
    "ds_read_b128 a[192:195], v13 offset:6144\n"
    "v_lshlrev_b32 v15, 5, v15\n"
    "v_mfma_f32_16x16x32_bf16 v[96:99], a[64:67], a[176:179], v[96:99]\n"
    "ds_read_b128 a[196:199], v13 offset:7168\n"
    "v_add_u32 v15, 1024, v15\n"
    "v_mfma_f32_16x16x32_bf16 v[108:111], a[64:67], a[180:183], v[108:111]\n"
    "s_mul_i32 s54, s49, 129024\n"
    "s_mul_i32 s55, s50, 21504\n"
    "ds_read_b128 a[200:203], v13 offset:8192\n"
    "v_mfma_f32_16x16x32_bf16 v[104:107], a[36:39], a[180:183], v[104:107]\n"
    "buffer_load_dwordx4 v31, s[24:27], s67 offen offset:1024 lds\n"
    "v_mfma_f32_16x16x32_bf16 v[100:103], a[8:11], a[180:183], v[100:103]\n"
    "s_add_u32 s54, s54, s55\n"
    "s_add_u32 s54, s54, 65536\n"
    "s_mul_i32 s62, s51, 43008\n"
    "v_mfma_f32_16x16x32_bf16 v[112:115], a[8:11], a[184:187], v[112:115]\n"
    "buffer_load_dwordx4 v32, s[24:27], s67 offen offset:2048 lds\n"
    "v_mfma_f32_16x16x32_bf16 v[116:119], a[36:39], a[184:187], v[116:119]\n"
    "s_add_u32 s62, s62, s54\n"
    "s_add_u32 s63, s62, 43008\n"
    "s_mul_i32 s64, s60, 672\n"
    "v_mfma_f32_16x16x32_bf16 v[120:123], a[64:67], a[184:187], v[120:123]\n"
    "buffer_load_dwordx2 v[40:41], off, s[36:39], 0 sc1\n"
    "s_waitcnt lgkmcnt(3)\n"
    "v_mfma_f32_16x16x32_bf16 v[124:127], a[8:11], a[188:191], v[124:127]\n"
    "s_add_u32 s64, s64, s54\n"
    "v_cvt_pk_bf16_f32 v78, v64, v65\n"
    "v_cvt_pk_bf16_f32 v79, v66, v67\n"
    "v_mfma_f32_16x16x32_bf16 v[128:131], a[36:39], a[188:191], v[128:131]\n"
    "s_cmp_eq_u32 s49, 0\n"
    "s_cselect_b32 s70, -0.5, 0x3fc00000\n"
    "s_cselect_b32 s71, 1.0, -1.0\n"
    "v_mfma_f32_16x16x32_bf16 v[132:135], a[64:67], a[188:191], v[132:135]\n"
    "s_mov_b32 s72, 0x40000\n"
    "v_cvt_pk_bf16_f32 v76, v60, v61\n"
    "v_cvt_pk_bf16_f32 v77, v62, v63\n"
    "s_waitcnt lgkmcnt(1)\n"
    "v_mfma_f32_16x16x32_bf16 v[136:139], a[8:11], a[192:195], v[136:139]\n"
    "ds_write_b64 v12, v[78:79] offset:20488\n"
    "v_mfma_f32_16x16x32_bf16 v[136:139], a[4:7], a[188:191], v[136:139]\n"
    "s_waitcnt vmcnt(10)\n"
    "ds_read_b64_tr_b16 a[12:13], v33 offset:23552\n"
    "ds_read_b64_tr_b16 a[14:15], v33 offset:25088\n"
    "v_mfma_f32_16x16x32_bf16 v[140:143], a[32:35], a[188:191], v[140:143]\n"
    "ds_read_b64_tr_b16 a[40:41], v33 offset:23560\n"
    "ds_read_b64_tr_b16 a[42:43], v33 offset:25096\n"
    "v_mfma_f32_16x16x32_bf16 v[140:143], a[36:39], a[192:195], v[140:143]\n"
    "ds_read_b64_tr_b16 a[68:69], v34 offset:23552\n"
    "ds_read_b64_tr_b16 a[70:71], v34 offset:25088\n"
    "v_mfma_f32_16x16x32_bf16 v[144:147], a[64:67], a[192:195], v[144:147]\n"
    "ds_write_b64 v12, v[76:77] offset:20480\n"
    "v_mfma_f32_16x16x32_bf16 v[144:147], a[60:63], a[188:191], v[144:147]\n"
    "ds_read_b64_tr_b16 a[248:249], v18 offset:2816\n"
    "v_mfma_f32_16x16x32_bf16 v[148:151], a[0:3], a[188:191], 0\n"
    "ds_read_b64_tr_b16 a[250:251], v18 offset:3328\n"
    "v_mfma_f32_16x16x32_bf16 v[148:151], a[4:7], a[192:195], v[148:151]\n"
    "ds_read_b64_tr_b16 a[252:253], v19 offset:2816\n"
    "v_mfma_f32_16x16x32_bf16 v[148:151], a[8:11], a[196:199], v[148:151]\n"
    "ds_read_b64_tr_b16 a[254:255], v19 offset:3328\n"
    "v_mfma_f32_16x16x32_bf16 v[152:155], a[36:39], a[196:199], 0\n"
    "s_waitcnt vmcnt(7)\n"
    "ds_read_b64_tr_b16 a[16:17], v33 offset:26624\n"
    "ds_read_b64_tr_b16 a[18:19], v33 offset:28160\n"
    "v_mfma_f32_16x16x32_bf16 v[152:155], a[32:35], a[192:195], v[152:155]\n"
    "s_add_u32 m0, s69, 23552\n"
    "s_add_u32 s67, s65, 774144\n"
    "ds_read_b64_tr_b16 a[44:45], v33 offset:26632\n"
    "ds_read_b64_tr_b16 a[46:47], v33 offset:28168\n"
    "v_mfma_f32_16x16x32_bf16 v[152:155], a[28:31], a[188:191], v[152:155]\n"
    "s_waitcnt lgkmcnt(9)\n"
    "ds_read_b64_tr_b16 a[72:73], v34 offset:26624\n"
    "ds_read_b64_tr_b16 a[74:75], v34 offset:28160\n"
    "v_mfma_f32_16x16x32_bf16 v[156:159], a[56:59], a[188:191], 0\n"
    "buffer_load_dwordx4 v30, s[24:27], s67 offen offset:0 lds\n"
    "v_mfma_f32_16x16x32_bf16 v[156:159], a[60:63], a[192:195], v[156:159]\n"
    "ds_read_b64_tr_b16 a[240:241], v18 offset:5632\n"
    "v_mfma_f32_16x16x32_bf16 v[156:159], a[64:67], a[196:199], v[156:159]\n"
    "buffer_load_dwordx4 v31, s[24:27], s67 offen offset:1024 lds\n"
    "v_mfma_f32_16x16x32_bf16 v[160:163], a[8:11], a[200:203], 0\n"
    "ds_read_b64_tr_b16 a[242:243], v18 offset:6144\n"
    "v_mfma_f32_16x16x32_bf16 v[160:163], a[4:7], a[196:199], v[160:163]\n"
    "buffer_load_dwordx4 v32, s[24:27], s67 offen offset:2048 lds\n"
    "v_mfma_f32_16x16x32_bf16 v[160:163], a[0:3], a[192:195], v[160:163]\n"
    "s_waitcnt lgkmcnt(8)\n"
    "s_waitcnt vmcnt(7)\n"
    "ds_read_b64_tr_b16 a[232:233], v16 offset:11264\n"
    "v_mfma_f32_16x16x32_bf16 v[72:75], a[252:255], v[44:47], 0\n"
    "v_mfma_f32_16x16x32_bf16 v[68:71], a[248:251], v[44:47], 0\n"
    "v_mfma_f32_16x16x32_bf16 v[164:167], a[28:31], a[192:195], 0\n"
    "ds_read_b64_tr_b16 a[234:235], v16 offset:11776\n"
    "v_mfma_f32_16x16x32_bf16 v[164:167], a[32:35], a[196:199], v[164:167]\n"
    "ds_read_b64_tr_b16 a[236:237], v17 offset:11264\n"
    "v_mfma_f32_16x16x32_bf16 v[164:167], a[36:39], a[200:203], v[164:167]\n"
    "global_load_dwordx4 v[48:51], v14, %[tab] offset:1024\n"
    "v_mfma_f32_16x16x32_bf16 v[168:171], a[64:67], a[200:203], 0\n"
    "ds_read_b64_tr_b16 a[238:239], v17 offset:11776\n"
    "v_mfma_f32_16x16x32_bf16 v[168:171], a[60:63], a[196:199], v[168:171]\n"
    "ds_read_b64_tr_b16 a[244:245], v19 offset:5632\n"
    "v_mfma_f32_16x16x32_bf16 v[168:171], a[56:59], a[192:195], v[168:171]\n"
    "s_waitcnt lgkmcnt(4)\n"
    "ds_read_b64_tr_b16 a[246:247], v19 offset:6144\n"
    "v_mfma_f32_16x16x32_bf16 v[88:91], a[12:15], a[180:183], v[88:91]\n"
    "global_load_dwordx4 v[20:23], v11, %[tab] offset:0\n"
    "v_mfma_f32_16x16x32_bf16 v[88:91], a[16:19], a[184:187], v[88:91]\n"
    "v_cvt_pk_bf16_f32 v82, v72, v73\n"
    "v_cvt_pk_bf16_f32 v83, v74, v75\n"
    "v_mfma_f32_16x16x32_bf16 v[92:95], a[44:47], a[184:187], v[92:95]\n"
    "s_waitcnt lgkmcnt(2)\n"
    "v_cvt_pk_bf16_f32 v80, v68, v69\n"
    "v_cvt_pk_bf16_f32 v81, v70, v71\n"
    "v_mfma_f32_16x16x32_bf16 v[92:95], a[40:43], a[180:183], v[92:95]\n"
    "ds_write_b64 v12, v[82:83] offset:24584\n"
    "v_mfma_f32_16x16x32_bf16 v[208:211], a[236:239], v[44:47], 0\n"
    "v_mfma_f32_16x16x32_bf16 v[204:207], a[232:235], v[44:47], 0\n"
    "v_mfma_f32_16x16x32_bf16 v[96:99], a[68:71], a[180:183], v[96:99]\n"
    "global_load_dwordx4 v[24:27], v11, %[tab] offset:16\n"
    "v_mfma_f32_16x16x32_bf16 v[96:99], a[72:75], a[184:187], v[96:99]\n"
    "ds_write_b64 v12, v[80:81] offset:24576\n"
    "v_mfma_f32_16x16x32_bf16 v[100:103], a[16:19], a[188:191], v[100:103]\n"
    "ds_read_b64_tr_b16 a[248:249], v18 offset:8448\n"
    "v_mfma_f32_16x16x32_bf16 v[100:103], a[12:15], a[184:187], v[100:103]\n"
    "s_waitcnt lgkmcnt(3)\n"
    "ds_read_b64_tr_b16 a[250:251], v18 offset:8960\n"
    "v_mfma_f32_16x16x32_bf16 v[64:67], a[244:247], v[44:47], 0\n"
    "v_mfma_f32_16x16x32_bf16 v[60:63], a[240:243], v[44:47], 0\n"
    "v_mfma_f32_16x16x32_bf16 v[104:107], a[40:43], a[184:187], v[104:107]\n"
    "ds_read_b64_tr_b16 a[252:253], v19 offset:8448\n"
    "v_mfma_f32_16x16x32_bf16 v[104:107], a[44:47], a[188:191], v[104:107]\n"
    "v_cvt_pk_bf16_f32 v230, v208, v209\n"
    "v_cvt_pk_bf16_f32 v231, v210, v211\n"
    "v_mfma_f32_16x16x32_bf16 v[108:111], a[72:75], a[188:191], v[108:111]\n"
    "v_cvt_pk_bf16_f32 v228, v204, v205\n"
    "v_cvt_pk_bf16_f32 v229, v206, v207\n"
    "v_mfma_f32_16x16x32_bf16 v[108:111], a[68:71], a[184:187], v[108:111]\n"
    "ds_write_b128 v12, v[228:231] offset:16384\n"
    "v_mfma_f32_16x16x32_bf16 v[112:115], a[12:15], a[188:191], v[112:115]\n"
    "ds_read_b64_tr_b16 a[254:255], v19 offset:8960\n"
    "v_mfma_f32_16x16x32_bf16 v[112:115], a[16:19], a[192:195], v[112:115]\n"
    "ds_read_b64_tr_b16 a[240:241], v18 offset:11264\n"
    "v_mfma_f32_16x16x32_bf16 v[116:119], a[44:47], a[192:195], v[116:119]\n"
    "v_cvt_pk_bf16_f32 v78, v64, v65\n"
    "v_cvt_pk_bf16_f32 v79, v66, v67\n"
    "v_mfma_f32_16x16x32_bf16 v[116:119], a[40:43], a[188:191], v[116:119]\n"
    "s_waitcnt lgkmcnt(2)\n"
    "s_barrier\n"
    "ds_read_b128 a[204:207], v13 offset:9216\n"
    "v_mfma_f32_16x16x32_bf16 v[120:123], a[68:71], a[188:191], v[120:123]\n"
    "ds_read_b128 a[208:211], v13 offset:10240\n"
    "v_cvt_pk_bf16_f32 v76, v60, v61\n"
    "v_mfma_f32_16x16x32_bf16 v[120:123], a[72:75], a[192:195], v[120:123]\n"
    "ds_read_b64_tr_b16 a[242:243], v18 offset:11776\n"
    "v_cvt_pk_bf16_f32 v77, v62, v63\n"
    "v_mfma_f32_16x16x32_bf16 v[124:127], a[16:19], a[196:199], v[124:127]\n"
    "ds_write_b64 v12, v[78:79] offset:28680\n"
    "v_mfma_f32_16x16x32_bf16 v[124:127], a[12:15], a[192:195], v[124:127]\n"
    "s_waitcnt lgkmcnt(5)\n"
    "ds_read_b64_tr_b16 a[244:245], v19 offset:11264\n"
    "v_mfma_f32_16x16x32_bf16 v[128:131], a[40:43], a[192:195], v[128:131]\n"
    "ds_write_b64 v12, v[76:77] offset:28672\n"
    "v_mfma_f32_16x16x32_bf16 v[72:75], a[252:255], v[44:47], 0\n"
    "v_mfma_f32_16x16x32_bf16 v[68:71], a[248:251], v[44:47], 0\n"
    "v_mfma_f32_16x16x32_bf16 v[128:131], a[44:47], a[196:199], v[128:131]\n"
    "s_waitcnt vmcnt(7)\n"
    "ds_read_b64_tr_b16 a[20:21], v33 offset:20480\n"
    "v_mfma_f32_16x16x32_bf16 v[132:135], a[72:75], a[196:199], v[132:135]\n"
    "ds_read_b64_tr_b16 a[22:23], v33 offset:22016\n"
    "v_mfma_f32_16x16x32_bf16 v[132:135], a[68:71], a[192:195], v[132:135]\n"
    "ds_read_b64_tr_b16 a[48:49], v33 offset:20488\n"
    "v_mfma_f32_16x16x32_bf16 v[136:139], a[12:15], a[196:199], v[136:139]\n"
    "ds_read_b64_tr_b16 a[50:51], v33 offset:22024\n"
    "v_mfma_f32_16x16x32_bf16 v[136:139], a[16:19], a[200:203], v[136:139]\n"
    "ds_read_b64_tr_b16 a[76:77], v34 offset:20480\n"
    "v_mfma_f32_16x16x32_bf16 v[140:143], a[44:47], a[200:203], v[140:143]\n"
    "ds_read_b64_tr_b16 a[78:79], v34 offset:22016\n"
    "v_mfma_f32_16x16x32_bf16 v[140:143], a[40:43], a[196:199], v[140:143]\n"
    "buffer_load_dwordx2 v[42:43], v15, s[36:39], 0 offen offset:0 sc1\n"
    "v_mfma_f32_16x16x32_bf16 v[144:147], a[68:71], a[196:199], v[144:147]\n"
    "s_waitcnt lgkmcnt(6)\n"
    "v_cvt_pk_bf16_f32 v82, v72, v73\n"
    "v_cvt_pk_bf16_f32 v83, v74, v75\n"
    "v_mfma_f32_16x16x32_bf16 v[144:147], a[72:75], a[200:203], v[144:147]\n"
    "s_waitcnt vmcnt(4)\n"
    "ds_read_b64_tr_b16 a[24:25], v33 offset:23552\n"
    "v_cvt_pk_bf16_f32 v80, v68, v69\n"
    "v_mfma_f32_16x16x32_bf16 v[148:151], a[16:19], a[204:207], v[148:151]\n"
    "ds_read_b64_tr_b16 a[26:27], v33 offset:25088\n"
    "v_cvt_pk_bf16_f32 v81, v70, v71\n"
    "v_mfma_f32_16x16x32_bf16 v[148:151], a[12:15], a[200:203], v[148:151]\n"
    "ds_read_b64_tr_b16 a[52:53], v33 offset:23560\n"
    "v_mfma_f32_16x16x32_bf16 v[152:155], a[40:43], a[200:203], v[152:155]\n"
    "ds_read_b64_tr_b16 a[54:55], v33 offset:25096\n"
    "v_mfma_f32_16x16x32_bf16 v[152:155], a[44:47], a[204:207], v[152:155]\n"
    "ds_read_b64_tr_b16 a[80:81], v34 offset:23552\n"
    "v_mfma_f32_16x16x32_bf16 v[156:159], a[72:75], a[204:207], v[156:159]\n"
    "ds_read_b64_tr_b16 a[82:83], v34 offset:25088\n"
    "v_mfma_f32_16x16x32_bf16 v[156:159], a[68:71], a[200:203], v[156:159]\n"
    "ds_read_b128 a[212:215], v13 offset:11264\n"
    "v_add_co_u32 v40, s[76:77], 1, v40\n"
    "v_mfma_f32_16x16x32_bf16 v[160:163], a[12:15], a[204:207], v[160:163]\n"
    "ds_write_b64 v12, v[82:83] offset:32776\n"
    "v_mfma_f32_16x16x32_bf16 v[160:163], a[16:19], a[208:211], v[160:163]\n"
    "ds_read_b64_tr_b16 a[246:247], v19 offset:11776\n"
    "v_addc_co_u32 v41, s[76:77], 0, v41, s[76:77]\n"
    "v_mfma_f32_16x16x32_bf16 v[164:167], a[44:47], a[208:211], v[164:167]\n"
    "ds_write_b64 v12, v[80:81] offset:32768\n"
    "v_mfma_f32_16x16x32_bf16 v[164:167], a[40:43], a[204:207], v[164:167]\n"
    "ds_read_b128 a[216:219], v13 offset:12288\n"
    "v_mfma_f32_16x16x32_bf16 v[168:171], a[68:71], a[204:207], v[168:171]\n"
    ".Lpad0:\n"
    "s_cbranch_execz .Lpad4\n"
    "v_mfma_f32_16x16x32_bf16 v[168:171], a[72:75], a[208:211], v[168:171]\n"
    "s_waitcnt lgkmcnt(7)\n"
    "v_add_u32 v37, 1, v37\n"
    "v_mfma_f32_16x16x32_bf16 v[88:91], a[20:23], a[188:191], v[88:91]\n"
    "v_mfma_f32_16x16x32_bf16 v[88:91], a[24:27], a[192:195], v[88:91]\n"
    "v_mfma_f32_16x16x32_bf16 v[92:95], a[52:55], a[192:195], v[92:95]\n"
    "v_mfma_f32_16x16x32_bf16 v[92:95], a[48:51], a[188:191], v[92:95]\n"
    "s_waitcnt lgkmcnt(2)\n"
    "v_mfma_f32_16x16x32_bf16 v[64:67], a[244:247], v[44:47], 0\n"
    "v_mfma_f32_16x16x32_bf16 v[60:63], a[240:243], v[44:47], 0\n"
    "v_mfma_f32_16x16x32_bf16 v[96:99], a[76:79], a[188:191], v[96:99]\n"
    "v_mfma_f32_16x16x32_bf16 v[96:99], a[80:83], a[192:195], v[96:99]\n"
    "s_waitcnt vmcnt(0)\n"
    "v_cmp_ne_u64 vcc, v[42:43], v[40:41]\n"
    "v_mfma_f32_16x16x32_bf16 v[100:103], a[24:27], a[196:199], v[100:103]\n"
    "v_mfma_f32_16x16x32_bf16 v[100:103], a[20:23], a[192:195], v[100:103]\n"
    "s_cbranch_vccnz .Lpoll0\n"
    ".Lpoll_ok0:\n"
    "v_mfma_f32_16x16x32_bf16 v[104:107], a[48:51], a[192:195], v[104:107]\n"
    "s_add_u32 s68, s63, 774144\n"
    "v_mfma_f32_16x16x32_bf16 v[104:107], a[52:55], a[196:199], v[104:107]\n"
    "buffer_load_dwordx4 a[84:87], v14, s[36:39], s68 offen offset:0 sc1\n"
    "v_mfma_f32_16x16x32_bf16 v[108:111], a[80:83], a[196:199], v[108:111]\n"
    "v_cvt_pk_bf16_f32 v78, v64, v65\n"
    "v_cvt_pk_bf16_f32 v79, v66, v67\n"
    "v_mfma_f32_16x16x32_bf16 v[108:111], a[76:79], a[192:195], v[108:111]\n"
    "v_cvt_pk_bf16_f32 v76, v60, v61\n"
    "v_cvt_pk_bf16_f32 v77, v62, v63\n"
    "v_mfma_f32_16x16x32_bf16 v[112:115], a[20:23], a[196:199], v[112:115]\n"
    "ds_write_b64 v12, v[78:79] offset:36872\n"
    "v_mfma_f32_16x16x32_bf16 v[112:115], a[24:27], a[200:203], v[112:115]\n"
    "v_mfma_f32_16x16x32_bf16 v[116:119], a[52:55], a[200:203], v[116:119]\n"
    "buffer_load_dwordx4 a[88:91], v14, s[36:39], s68 offen offset:1024 sc1\n"
    "v_mfma_f32_16x16x32_bf16 v[116:119], a[48:51], a[196:199], v[116:119]\n"
    "ds_write_b64 v12, v[76:77] offset:36864\n"
    "v_mfma_f32_16x16x32_bf16 v[120:123], a[76:79], a[196:199], v[120:123]\n"
    "ds_write_b32 v35, v37\n"
    "v_mfma_f32_16x16x32_bf16 v[120:123], a[80:83], a[200:203], v[120:123]\n"
    "v_mfma_f32_16x16x32_bf16 v[124:127], a[24:27], a[204:207], v[124:127]\n"
    "v_mfma_f32_16x16x32_bf16 v[124:127], a[20:23], a[200:203], v[124:127]\n"
    "buffer_load_dwordx4 a[92:95], v14, s[36:39], s68 offen offset:2048 sc1\n"
    "v_mfma_f32_16x16x32_bf16 v[128:131], a[48:51], a[200:203], v[128:131]\n"
    "ds_read_b32 v38, v36 offset:30592\n"
    "v_mfma_f32_16x16x32_bf16 v[128:131], a[52:55], a[204:207], v[128:131]\n"
    "v_mfma_f32_16x16x32_bf16 v[132:135], a[80:83], a[204:207], v[132:135]\n"
    "v_mfma_f32_16x16x32_bf16 v[132:135], a[76:79], a[200:203], v[132:135]\n"
    "v_mfma_f32_16x16x32_bf16 v[136:139], a[20:23], a[204:207], v[136:139]\n"
    "buffer_load_dwordx4 a[96:99], v14, s[36:39], s68 offen offset:3072 sc1\n"
    "v_mfma_f32_16x16x32_bf16 v[136:139], a[24:27], a[208:211], v[136:139]\n"
    "s_waitcnt lgkmcnt(0)\n"
    "v_cmp_gt_u32 vcc, 1, v38\n"
    "v_mfma_f32_16x16x32_bf16 v[140:143], a[52:55], a[208:211], v[140:143]\n"
    "v_mfma_f32_16x16x32_bf16 v[140:143], a[48:51], a[204:207], v[140:143]\n"
    "s_cbranch_vccnz .Lsl1\n"
    ".Lbk1:\n"
    "v_mfma_f32_16x16x32_bf16 v[144:147], a[76:79], a[204:207], v[144:147]\n"
    "ds_read_b128 a[220:223], v13 offset:20480\n"
    "v_mfma_f32_16x16x32_bf16 v[144:147], a[80:83], a[208:211], v[144:147]\n"
    "s_addk_i32 s68, 0x1000\n"
    "ds_read_b128 a[224:227], v13 offset:21504\n"
    "v_mfma_f32_16x16x32_bf16 v[148:151], a[24:27], a[212:215], v[148:151]\n"
    "buffer_load_dwordx4 a[100:103], v14, s[36:39], s68 offen offset:0 sc1\n"
    "v_mfma_f32_16x16x32_bf16 v[148:151], a[20:23], a[208:211], v[148:151]\n"
    "ds_read_b128 a[228:231], v13 offset:22528\n"
    "v_mfma_f32_16x16x32_bf16 v[152:155], a[48:51], a[208:211], v[152:155]\n"
    "ds_read_b128 a[232:235], v13 offset:23552\n"
    "v_mfma_f32_16x16x32_bf16 v[152:155], a[52:55], a[212:215], v[152:155]\n"
    "ds_read_b128 a[236:239], v13 offset:24576\n"
    "v_mfma_f32_16x16x32_bf16 v[156:159], a[80:83], a[212:215], v[156:159]\n"
    "ds_read_b128 a[168:171], v13 offset:25600\n"
    "v_mfma_f32_16x16x32_bf16 v[156:159], a[76:79], a[208:211], v[156:159]\n"
    "buffer_load_dwordx4 a[104:107], v14, s[36:39], s68 offen offset:1024 sc1\n"
    "v_mfma_f32_16x16x32_bf16 v[160:163], a[20:23], a[212:215], v[160:163]\n"
    "ds_read_b128 a[172:175], v13 offset:26624\n"
    "v_mfma_f32_16x16x32_bf16 v[160:163], a[24:27], a[216:219], v[160:163]\n"
    "ds_read_b128 a[176:179], v13 offset:27648\n"
    "v_mfma_f32_16x16x32_bf16 v[164:167], a[52:55], a[216:219], v[164:167]\n"
    "ds_read_b128 a[180:183], v13 offset:28672\n"
    "v_mfma_f32_16x16x32_bf16 v[164:167], a[48:51], a[212:215], v[164:167]\n"
    "ds_read_b128 a[184:187], v13 offset:29696\n"
    "v_mfma_f32_16x16x32_bf16 v[168:171], a[76:79], a[212:215], v[168:171]\n"
    "buffer_load_dwordx4 a[108:111], v14, s[36:39], s68 offen offset:2048 sc1\n"
    "v_mfma_f32_16x16x32_bf16 v[168:171], a[80:83], a[216:219], v[168:171]\n"
    "s_waitcnt lgkmcnt(6)\n"
    "s_waitcnt vmcnt(6)\n"
    "ds_read_b128 a[188:191], v13 offset:30720\n"
    "v_mfma_f32_16x16x32_bf16 v[88:91], a[84:87], a[220:223], v[88:91]\n"
    "ds_read_b128 a[192:195], v13 offset:31744\n"
    "v_mfma_f32_16x16x32_bf16 v[100:103], a[84:87], a[224:227], v[100:103]\n"
    "ds_read_b128 a[196:199], v13 offset:32768\n"
    "v_mfma_f32_16x16x32_bf16 v[112:115], a[84:87], a[228:231], v[112:115]\n"
    "s_waitcnt lgkmcnt(6)\n"
    "v_mfma_f32_16x16x32_bf16 v[124:127], a[84:87], a[232:235], v[124:127]\n"
    "buffer_load_dwordx4 a[112:115], v14, s[36:39], s68 offen offset:3072 sc1\n"
    "v_mfma_f32_16x16x32_bf16 v[136:139], a[84:87], a[236:239], v[136:139]\n"
    "v_mfma_f32_16x16x32_bf16 v[148:151], a[84:87], a[168:171], v[148:151]\n"
    "v_mfma_f32_16x16x32_bf16 v[160:163], a[84:87], a[172:175], v[160:163]\n"
    "s_waitcnt vmcnt(6)\n"
    "v_mfma_f32_16x16x32_bf16 v[88:91], a[88:91], a[224:227], v[88:91]\n"
    "v_mfma_f32_16x16x32_bf16 v[100:103], a[88:91], a[228:231], v[100:103]\n"
    "s_addk_i32 s68, 0x1000\n"
    "ds_read_b64_tr_b16 a[240:241], v16 offset:0\n"
    "v_mfma_f32_16x16x32_bf16 v[112:115], a[88:91], a[232:235], v[112:115]\n"
    "buffer_load_dwordx4 a[116:119], v14, s[36:39], s68 offen offset:0 sc1\n"
    "v_mfma_f32_16x16x32_bf16 v[124:127], a[88:91], a[236:239], v[124:127]\n"
    "ds_read_b64_tr_b16 a[242:243], v16 offset:512\n"
    "v_mfma_f32_16x16x32_bf16 v[136:139], a[88:91], a[168:171], v[136:139]\n"
    "ds_read_b64_tr_b16 a[244:245], v17 offset:0\n"
    "v_mfma_f32_16x16x32_bf16 v[148:151], a[88:91], a[172:175], v[148:151]\n"
    "s_waitcnt lgkmcnt(3)\n"
    "ds_read_b64_tr_b16 a[246:247], v17 offset:512\n"
    "v_mfma_f32_16x16x32_bf16 v[160:163], a[88:91], a[176:179], v[160:163]\n"
    "s_waitcnt vmcnt(6)\n"
    "v_mfma_f32_16x16x32_bf16 v[88:91], a[92:95], a[228:231], v[88:91]\n"
    "buffer_load_dwordx4 a[120:123], v14, s[36:39], s68 offen offset:1024 sc1\n"
    "v_mfma_f32_16x16x32_bf16 v[100:103], a[92:95], a[232:235], v[100:103]\n"
    "v_mfma_f32_16x16x32_bf16 v[112:115], a[92:95], a[236:239], v[112:115]\n"
    "v_mfma_f32_16x16x32_bf16 v[124:127], a[92:95], a[168:171], v[124:127]\n"
    "v_mfma_f32_16x16x32_bf16 v[136:139], a[92:95], a[172:175], v[136:139]\n"
    "v_mfma_f32_16x16x32_bf16 v[148:151], a[92:95], a[176:179], v[148:151]\n"
    "buffer_load_dwordx4 a[124:127], v14, s[36:39], s68 offen offset:2048 sc1\n"
    "v_mfma_f32_16x16x32_bf16 v[160:163], a[92:95], a[180:183], v[160:163]\n"
    "s_waitcnt lgkmcnt(0)\n"
    "v_mfma_f32_16x16x32_bf16 v[64:67], a[244:247], v[48:51], 0\n"
    "v_mfma_f32_16x16x32_bf16 v[60:63], a[240:243], v[48:51], 0\n"
    "s_waitcnt vmcnt(7)\n"
    "v_mfma_f32_16x16x32_bf16 v[88:91], a[96:99], a[232:235], v[88:91]\n"
    "v_mfma_f32_16x16x32_bf16 v[100:103], a[96:99], a[236:239], v[100:103]\n"
    "ds_read_b64_tr_b16 a[248:249], v16 offset:2816\n"
    "v_mfma_f32_16x16x32_bf16 v[112:115], a[96:99], a[168:171], v[112:115]\n"
    "ds_read_b64_tr_b16 a[250:251], v16 offset:3328\n"
    "v_mfma_f32_16x16x32_bf16 v[124:127], a[96:99], a[172:175], v[124:127]\n"
    "buffer_load_dwordx4 a[128:131], v14, s[36:39], s68 offen offset:3072 sc1\n"
    "v_mfma_f32_16x16x32_bf16 v[136:139], a[96:99], a[176:179], v[136:139]\n"
    "ds_read_b64_tr_b16 a[252:253], v17 offset:2816\n"
    "v_mfma_f32_16x16x32_bf16 v[148:151], a[96:99], a[180:183], v[148:151]\n"
    "s_waitcnt vmcnt(7)\n"
    "ds_read_b64_tr_b16 a[254:255], v17 offset:3328\n"
    "v_mfma_f32_16x16x32_bf16 v[160:163], a[96:99], a[184:187], v[160:163]\n"
    "v_cvt_pk_bf16_f32 v78, v64, v65\n"
    "v_cvt_pk_bf16_f32 v79, v66, v67\n"
    "v_mfma_f32_16x16x32_bf16 v[88:91], a[100:103], a[236:239], v[88:91]\n"
    "v_cvt_pk_bf16_f32 v76, v60, v61\n"
    "v_cvt_pk_bf16_f32 v77, v62, v63\n"
    "v_mfma_f32_16x16x32_bf16 v[100:103], a[100:103], a[168:171], v[100:103]\n"
    "s_addk_i32 s68, 0x1000\n"
    "ds_write_b32 v12, v78 offset:8\n"
    "v_mfma_f32_16x16x32_bf16 v[112:115], a[100:103], a[172:175], v[112:115]\n"
    "buffer_load_dwordx4 a[132:135], v14, s[36:39], s68 offen offset:0 sc1\n"
    "v_mfma_f32_16x16x32_bf16 v[124:127], a[100:103], a[176:179], v[124:127]\n"
    "ds_write_b32 v12, v79 offset:12\n"
    "v_mfma_f32_16x16x32_bf16 v[136:139], a[100:103], a[180:183], v[136:139]\n"
    "v_mfma_f32_16x16x32_bf16 v[148:151], a[100:103], a[184:187], v[148:151]\n"
    "ds_write_b32 v12, v76 offset:0\n"
    "v_mfma_f32_16x16x32_bf16 v[160:163], a[100:103], a[188:191], v[160:163]\n"
    "s_waitcnt lgkmcnt(3)\n"
    "v_mfma_f32_16x16x32_bf16 v[72:75], a[252:255], v[48:51], 0\n"
    "v_mfma_f32_16x16x32_bf16 v[68:71], a[248:251], v[48:51], 0\n"
    "s_waitcnt vmcnt(7)\n"
    "v_mfma_f32_16x16x32_bf16 v[88:91], a[104:107], a[168:171], v[88:91]\n"
    "buffer_load_dwordx4 a[136:139], v14, s[36:39], s68 offen offset:1024 sc1\n"
    "v_mfma_f32_16x16x32_bf16 v[100:103], a[104:107], a[172:175], v[100:103]\n"
    "ds_write_b32 v12, v77 offset:4\n"
    "v_mfma_f32_16x16x32_bf16 v[112:115], a[104:107], a[176:179], v[112:115]\n"
    "ds_read_b64_tr_b16 a[240:241], v16 offset:5632\n"
    "v_mfma_f32_16x16x32_bf16 v[124:127], a[104:107], a[180:183], v[124:127]\n"
    "ds_read_b64_tr_b16 a[242:243], v16 offset:6144\n"
    "v_mfma_f32_16x16x32_bf16 v[136:139], a[104:107], a[184:187], v[136:139]\n"
    "s_waitcnt vmcnt(7)\n"
    "ds_read_b64_tr_b16 a[244:245], v17 offset:5632\n"
    "v_mfma_f32_16x16x32_bf16 v[148:151], a[104:107], a[188:191], v[148:151]\n"
    "buffer_load_dwordx2 v[42:43], v15, s[36:39], 0 offen offset:1024 sc1\n"
    "v_mfma_f32_16x16x32_bf16 v[160:163], a[104:107], a[192:195], v[160:163]\n"
    "v_cvt_pk_bf16_f32 v82, v72, v73\n"
    "v_cvt_pk_bf16_f32 v83, v74, v75\n"
    "v_mfma_f32_16x16x32_bf16 v[88:91], a[108:111], a[172:175], v[88:91]\n"
    "buffer_load_dwordx4 a[140:143], v14, s[36:39], s68 offen offset:2048 sc1\n"
    "v_mfma_f32_16x16x32_bf16 v[100:103], a[108:111], a[176:179], v[100:103]\n"
    "v_cvt_pk_bf16_f32 v80, v68, v69\n"
    "v_cvt_pk_bf16_f32 v81, v70, v71\n"
    "v_mfma_f32_16x16x32_bf16 v[112:115], a[108:111], a[180:183], v[112:115]\n"
    "ds_write_b32 v12, v82 offset:4104\n"
    "v_mfma_f32_16x16x32_bf16 v[124:127], a[108:111], a[184:187], v[124:127]\n"
    "s_waitcnt vmcnt(7)\n"
    "ds_read_b64_tr_b16 a[246:247], v17 offset:6144\n"
    "v_mfma_f32_16x16x32_bf16 v[136:139], a[108:111], a[188:191], v[136:139]\n"
    "ds_write_b32 v12, v83 offset:4108\n"
    "v_mfma_f32_16x16x32_bf16 v[148:151], a[108:111], a[192:195], v[148:151]\n"
    "buffer_load_dwordx4 a[144:147], v14, s[36:39], s68 offen offset:3072 sc1\n"
    "v_mfma_f32_16x16x32_bf16 v[160:163], a[108:111], a[196:199], v[160:163]\n"
    "ds_write_b32 v12, v80 offset:4096\n"
    "v_mfma_f32_16x16x32_bf16 v[92:95], a[112:115], a[220:223], v[92:95]\n"
    "v_mfma_f32_16x16x32_bf16 v[92:95], a[116:119], a[224:227], v[92:95]\n"
    "ds_write_b32 v12, v81 offset:4100\n"
    "v_mfma_f32_16x16x32_bf16 v[104:107], a[116:119], a[228:231], v[104:107]\n"
    "ds_read_b64_tr_b16 a[248:249], v16 offset:8448\n"
    "v_mul_f32 v172, -0.5, v88\n"
    "v_mfma_f32_16x16x32_bf16 v[104:107], a[112:115], a[224:227], v[104:107]\n"
    "s_addk_i32 s68, 0x1000\n"
    "ds_read_b64_tr_b16 a[250:251], v16 offset:8960\n"
    "v_mul_f32 v173, -0.5, v89\n"
    "v_mfma_f32_16x16x32_bf16 v[116:119], a[112:115], a[228:231], v[116:119]\n"
    "buffer_load_dwordx4 a[148:151], v14, s[36:39], s68 offen offset:0 sc1\n"
    "v_mfma_f32_16x16x32_bf16 v[116:119], a[116:119], a[232:235], v[116:119]\n"
    "ds_read_b64_tr_b16 a[252:253], v17 offset:8448\n"
    "v_mul_f32 v174, -0.5, v90\n"
    "v_mfma_f32_16x16x32_bf16 v[128:131], a[116:119], a[236:239], v[128:131]\n"
    "s_waitcnt lgkmcnt(5)\n"
    "ds_read_b64_tr_b16 a[254:255], v17 offset:8960\n"
    "v_mul_f32 v175, -0.5, v91\n"
    "v_mfma_f32_16x16x32_bf16 v[64:67], a[244:247], v[48:51], 0\n"
    "v_mfma_f32_16x16x32_bf16 v[60:63], a[240:243], v[48:51], 0\n"
    "v_mfma_f32_16x16x32_bf16 v[128:131], a[112:115], a[232:235], v[128:131]\n"
    "v_mul_f32 v184, -0.5, v100\n"
    "v_mul_f32 v185, -0.5, v101\n"
    "v_mfma_f32_16x16x32_bf16 v[140:143], a[112:115], a[236:239], v[140:143]\n"
    "v_mul_f32 v186, -0.5, v102\n"
    "v_mul_f32 v187, -0.5, v103\n"
    "v_mfma_f32_16x16x32_bf16 v[140:143], a[116:119], a[168:171], v[140:143]\n"
    "buffer_load_dwordx4 a[152:155], v14, s[36:39], s68 offen offset:1024 sc1\n"
    "v_mfma_f32_16x16x32_bf16 v[152:155], a[116:119], a[172:175], v[152:155]\n"
    "v_mul_f32 v196, -0.5, v112\n"
    "v_mul_f32 v197, -0.5, v113\n"
    "v_mfma_f32_16x16x32_bf16 v[152:155], a[112:115], a[168:171], v[152:155]\n"
    "v_mul_f32 v198, -0.5, v114\n"
    "v_mul_f32 v199, -0.5, v115\n"
    "v_mfma_f32_16x16x32_bf16 v[164:167], a[112:115], a[172:175], v[164:167]\n"
    "s_waitcnt vmcnt(8)\n"
    "v_mul_f32 v208, -0.5, v124\n"
    "v_mul_f32 v209, -0.5, v125\n"
    "v_mfma_f32_16x16x32_bf16 v[164:167], a[116:119], a[176:179], v[164:167]\n"
    "v_cvt_pk_bf16_f32 v78, v64, v65\n"
    "v_cvt_pk_bf16_f32 v79, v66, v67\n"
    "v_mfma_f32_16x16x32_bf16 v[92:95], a[120:123], a[228:231], v[92:95]\n"
    "buffer_load_dwordx4 a[156:159], v14, s[36:39], s68 offen offset:2048 sc1\n"
    "v_mfma_f32_16x16x32_bf16 v[92:95], a[124:127], a[232:235], v[92:95]\n"
    "v_cvt_pk_bf16_f32 v76, v60, v61\n"
    "v_cvt_pk_bf16_f32 v77, v62, v63\n"
    "v_mfma_f32_16x16x32_bf16 v[104:107], a[124:127], a[236:239], v[104:107]\n"
    "ds_write_b32 v12, v78 offset:8200\n"
    "v_mfma_f32_16x16x32_bf16 v[104:107], a[120:123], a[232:235], v[104:107]\n"
    "s_waitcnt lgkmcnt(1)\n"
    "ds_read_b64_tr_b16 a[240:241], v16 offset:11264\n"
    "v_mul_f32 v210, -0.5, v126\n"
    "v_mfma_f32_16x16x32_bf16 v[116:119], a[120:123], a[236:239], v[116:119]\n"
    "ds_write_b32 v12, v79 offset:8204\n"
    "v_mfma_f32_16x16x32_bf16 v[116:119], a[124:127], a[168:171], v[116:119]\n"
    "buffer_load_dwordx4 a[160:163], v14, s[36:39], s68 offen offset:3072 sc1\n"
    "v_mfma_f32_16x16x32_bf16 v[128:131], a[124:127], a[172:175], v[128:131]\n"
    "ds_write_b32 v12, v76 offset:8192\n"
    "v_mfma_f32_16x16x32_bf16 v[72:75], a[252:255], v[48:51], 0\n"
    "v_mfma_f32_16x16x32_bf16 v[68:71], a[248:251], v[48:51], 0\n"
    "v_mfma_f32_16x16x32_bf16 v[128:131], a[120:123], a[168:171], v[128:131]\n"
    "ds_read_b64_tr_b16 a[242:243], v16 offset:11776\n"
    "v_mul_f32 v211, -0.5, v127\n"
    "v_mfma_f32_16x16x32_bf16 v[140:143], a[120:123], a[172:175], v[140:143]\n"
    "ds_write_b32 v12, v77 offset:8196\n"
    "v_mfma_f32_16x16x32_bf16 v[140:143], a[124:127], a[176:179], v[140:143]\n"
    "ds_read_b64_tr_b16 a[244:245], v17 offset:11264\n"
    "v_mul_f32 v220, -0.5, v136\n"
    "v_mfma_f32_16x16x32_bf16 v[152:155], a[124:127], a[180:183], v[152:155]\n"
    "s_addk_i32 s68, 0x1000\n"
    "ds_read_b64_tr_b16 a[246:247], v17 offset:11776\n"
    "v_mul_f32 v221, -0.5, v137\n"
    "v_mfma_f32_16x16x32_bf16 v[152:155], a[120:123], a[176:179], v[152:155]\n"
    "buffer_load_dwordx4 a[164:167], v14, s[36:39], s68 offen offset:0 sc1\n"
    "v_mfma_f32_16x16x32_bf16 v[164:167], a[120:123], a[180:183], v[164:167]\n"
    "s_waitcnt vmcnt(7)\n"
    "v_cmp_ne_u64 vcc, v[42:43], v[40:41]\n"
    "v_mul_f32 v222, -0.5, v138\n"
    "v_mfma_f32_16x16x32_bf16 v[164:167], a[124:127], a[184:187], v[164:167]\n"
    "v_cvt_pk_bf16_f32 v82, v72, v73\n"
    "v_cvt_pk_bf16_f32 v83, v74, v75\n"
    "v_mfma_f32_16x16x32_bf16 v[92:95], a[128:131], a[236:239], v[92:95]\n"
    "s_cbranch_vccnz .Lpoll1\n"
    ".Lpoll_ok1:\n"
    "v_mfma_f32_16x16x32_bf16 v[92:95], a[132:135], a[168:171], v[92:95]\n"
    "s_add_u32 s68, s62, 0\n"
    "v_cvt_pk_bf16_f32 v80, v68, v69\n"
    "v_cvt_pk_bf16_f32 v81, v70, v71\n"
    "v_mfma_f32_16x16x32_bf16 v[92:95], a[136:139], a[172:175], v[92:95]\n"
    "buffer_load_dwordx4 a[0:3], v14, s[36:39], s68 offen offset:0 sc1\n"
    "v_mfma_f32_16x16x32_bf16 v[104:107], a[136:139], a[176:179], v[104:107]\n"
    "ds_write_b32 v12, v82 offset:12296\n"
    "v_mfma_f32_16x16x32_bf16 v[104:107], a[132:135], a[172:175], v[104:107]\n"
    "v_mul_f32 v223, -0.5, v139\n"
    "v_mul_f32 v232, -0.5, v148\n"
    "v_mfma_f32_16x16x32_bf16 v[104:107], a[128:131], a[168:171], v[104:107]\n"
    "ds_write_b32 v12, v83 offset:12300\n"
    "v_mfma_f32_16x16x32_bf16 v[116:119], a[128:131], a[172:175], v[116:119]\n"
    "s_waitcnt lgkmcnt(2)\n"
    "v_add_u32 v37, 1, v37\n"
    "v_mul_f32 v233, -0.5, v149\n"
    "v_mfma_f32_16x16x32_bf16 v[64:67], a[244:247], v[48:51], 0\n"
    "v_mfma_f32_16x16x32_bf16 v[60:63], a[240:243], v[48:51], 0\n"
    "v_mfma_f32_16x16x32_bf16 v[116:119], a[132:135], a[176:179], v[116:119]\n"
    "buffer_load_dwordx4 a[4:7], v14, s[36:39], s68 offen offset:1024 sc1\n"
    "v_mfma_f32_16x16x32_bf16 v[116:119], a[136:139], a[180:183], v[116:119]\n"
    "ds_write_b32 v12, v80 offset:12288\n"
    "v_mfma_f32_16x16x32_bf16 v[128:131], a[136:139], a[184:187], v[128:131]\n"
    "v_mul_f32 v234, -0.5, v150\n"
    "v_mul_f32 v235, -0.5, v151\n"
    "v_mfma_f32_16x16x32_bf16 v[128:131], a[132:135], a[180:183], v[128:131]\n"
    "ds_write_b32 v12, v81 offset:12292\n"
    "v_mfma_f32_16x16x32_bf16 v[128:131], a[128:131], a[176:179], v[128:131]\n"
    "v_mul_f32 v244, -0.5, v160\n"
    "v_mul_f32 v245, -0.5, v161\n"
    "v_mfma_f32_16x16x32_bf16 v[140:143], a[128:131], a[180:183], v[140:143]\n"
    "buffer_load_dwordx4 a[8:11], v14, s[36:39], s68 offen offset:2048 sc1\n"
    "v_mfma_f32_16x16x32_bf16 v[140:143], a[132:135], a[184:187], v[140:143]\n"
    "v_cvt_pk_bf16_f32 v78, v64, v65\n"
    "v_cvt_pk_bf16_f32 v79, v66, v67\n"
    "v_mfma_f32_16x16x32_bf16 v[140:143], a[136:139], a[188:191], v[140:143]\n"
    "v_cvt_pk_bf16_f32 v76, v60, v61\n"
    "v_cvt_pk_bf16_f32 v77, v62, v63\n"
    "v_mfma_f32_16x16x32_bf16 v[152:155], a[136:139], a[192:195], v[152:155]\n"
    "ds_write_b32 v12, v78 offset:16392\n"
    "v_mfma_f32_16x16x32_bf16 v[152:155], a[132:135], a[188:191], v[152:155]\n"
    "v_mul_f32 v246, -0.5, v162\n"
    "v_mul_f32 v247, -0.5, v163\n"
    "v_mfma_f32_16x16x32_bf16 v[152:155], a[128:131], a[184:187], v[152:155]\n"
    "buffer_load_dwordx4 a[12:15], v14, s[36:39], s68 offen offset:3072 sc1\n"
    "v_mfma_f32_16x16x32_bf16 v[164:167], a[128:131], a[188:191], v[164:167]\n"
    "ds_write_b32 v12, v79 offset:16396\n"
    "v_mfma_f32_16x16x32_bf16 v[164:167], a[132:135], a[192:195], v[164:167]\n"
    "s_waitcnt vmcnt(9)\n"
    "v_mul_f32 v176, -0.5, v92\n"
    "v_mul_f32 v177, -0.5, v93\n"
    "v_mfma_f32_16x16x32_bf16 v[164:167], a[136:139], a[196:199], v[164:167]\n"
    "ds_write_b32 v12, v76 offset:16384\n"
    "v_mfma_f32_16x16x32_bf16 v[96:99], a[140:143], a[220:223], v[96:99]\n"
    "v_mul_f32 v178, -0.5, v94\n"
    "v_mul_f32 v179, -0.5, v95\n"
    "v_mfma_f32_16x16x32_bf16 v[96:99], a[144:147], a[224:227], v[96:99]\n"
    "s_addk_i32 s68, 0x1000\n"
    "ds_write_b32 v12, v77 offset:16388\n"
    "v_mfma_f32_16x16x32_bf16 v[108:111], a[144:147], a[228:231], v[108:111]\n"
    "buffer_load_dwordx4 a[16:19], v14, s[36:39], s68 offen offset:0 sc1\n"
    "v_mfma_f32_16x16x32_bf16 v[108:111], a[140:143], a[224:227], v[108:111]\n"
    "ds_write_b32 v35, v37\n"
    "v_mfma_f32_16x16x32_bf16 v[120:123], a[140:143], a[228:231], v[120:123]\n"
    "v_mul_f32 v188, -0.5, v104\n"
    "v_mul_f32 v189, -0.5, v105\n"
    "v_mfma_f32_16x16x32_bf16 v[120:123], a[144:147], a[232:235], v[120:123]\n"
    "v_mul_f32 v190, -0.5, v106\n"
    "v_mul_f32 v191, -0.5, v107\n"
    "v_mfma_f32_16x16x32_bf16 v[132:135], a[144:147], a[236:239], v[132:135]\n"
    "v_mul_f32 v200, -0.5, v116\n"
    "v_mul_f32 v201, -0.5, v117\n"
    "v_mfma_f32_16x16x32_bf16 v[132:135], a[140:143], a[232:235], v[132:135]\n"
    "buffer_load_dwordx4 a[20:23], v14, s[36:39], s68 offen offset:1024 sc1\n"
    "v_mfma_f32_16x16x32_bf16 v[144:147], a[140:143], a[236:239], v[144:147]\n"
    "v_mul_f32 v202, -0.5, v118\n"
    "v_mul_f32 v203, -0.5, v119\n"
    "v_mfma_f32_16x16x32_bf16 v[144:147], a[144:147], a[168:171], v[144:147]\n"
    "v_mul_f32 v212, -0.5, v128\n"
    "v_mul_f32 v213, -0.5, v129\n"
    "v_mfma_f32_16x16x32_bf16 v[156:159], a[144:147], a[172:175], v[156:159]\n"
    "v_mul_f32 v214, -0.5, v130\n"
    "v_mul_f32 v215, -0.5, v131\n"
    "v_mfma_f32_16x16x32_bf16 v[156:159], a[140:143], a[168:171], v[156:159]\n"
    "s_waitcnt vmcnt(9)\n"
    "ds_read_b32 v38, v36 offset:30592\n"
    "v_mul_f32 v224, -0.5, v140\n"
    "v_mfma_f32_16x16x32_bf16 v[168:171], a[140:143], a[172:175], v[168:171]\n"
    "buffer_load_dwordx4 a[24:27], v14, s[36:39], s68 offen offset:2048 sc1\n"
    "v_mfma_f32_16x16x32_bf16 v[168:171], a[144:147], a[176:179], v[168:171]\n"
    "v_mul_f32 v225, -0.5, v141\n"
    "v_mul_f32 v226, -0.5, v142\n"
    "v_mfma_f32_16x16x32_bf16 v[96:99], a[148:151], a[228:231], v[96:99]\n"
    "v_mul_f32 v227, -0.5, v143\n"
    "v_mul_f32 v236, -0.5, v152\n"
    "v_mfma_f32_16x16x32_bf16 v[96:99], a[152:155], a[232:235], v[96:99]\n"
    "v_mul_f32 v237, -0.5, v153\n"
    "v_mul_f32 v238, -0.5, v154\n"
    "v_mfma_f32_16x16x32_bf16 v[108:111], a[152:155], a[236:239], v[108:111]\n"
    "s_waitcnt lgkmcnt(0)\n"
    "v_cmp_gt_u32 vcc, 2, v38\n"
    "v_mul_f32 v239, -0.5, v155\n"
    "v_mfma_f32_16x16x32_bf16 v[108:111], a[148:151], a[232:235], v[108:111]\n"
    "buffer_load_dwordx4 a[28:31], v14, s[36:39], s68 offen offset:3072 sc1\n"
    "v_mfma_f32_16x16x32_bf16 v[120:123], a[148:151], a[236:239], v[120:123]\n"
    "s_cbranch_vccnz .Lsl2\n"
    ".Lbk2:\n"
    "v_mfma_f32_16x16x32_bf16 v[120:123], a[152:155], a[168:171], v[120:123]\n"
    "ds_read_b128 a[200:203], v13 offset:0\n"
    "v_mul_f32 v248, -0.5, v164\n"
    "v_mfma_f32_16x16x32_bf16 v[132:135], a[152:155], a[172:175], v[132:135]\n"
    "ds_read_b128 a[204:207], v13 offset:1024\n"
    "v_mul_f32 v249, -0.5, v165\n"
    "v_mfma_f32_16x16x32_bf16 v[132:135], a[148:151], a[168:171], v[132:135]\n"
    "ds_read_b128 a[208:211], v13 offset:2048\n"
    "v_mul_f32 v250, -0.5, v166\n"
    "v_mfma_f32_16x16x32_bf16 v[144:147], a[148:151], a[172:175], v[144:147]\n"
    "s_addk_i32 s68, 0x1000\n"
    "ds_read_b128 a[212:215], v13 offset:3072\n"
    "v_mul_f32 v251, -0.5, v167\n"
    "v_mfma_f32_16x16x32_bf16 v[144:147], a[152:155], a[176:179], v[144:147]\n"
    "buffer_load_dwordx4 a[32:35], v14, s[36:39], s68 offen offset:0 sc1\n"
    "v_mfma_f32_16x16x32_bf16 v[156:159], a[152:155], a[180:183], v[156:159]\n"
    "ds_read_b128 a[216:219], v13 offset:4096\n"
    "v_mfma_f32_16x16x32_bf16 v[156:159], a[148:151], a[176:179], v[156:159]\n"
    "ds_read_b128 a[220:223], v13 offset:5120\n"
    "v_mfma_f32_16x16x32_bf16 v[168:171], a[148:151], a[180:183], v[168:171]\n"
    "ds_read_b128 a[224:227], v13 offset:6144\n"
    "v_mfma_f32_16x16x32_bf16 v[168:171], a[152:155], a[184:187], v[168:171]\n"
    "s_waitcnt vmcnt(9)\n"
    "ds_read_b128 a[228:231], v13 offset:7168\n"
    "v_mfma_f32_16x16x32_bf16 v[96:99], a[156:159], a[236:239], v[96:99]\n"
    "buffer_load_dwordx4 a[36:39], v14, s[36:39], s68 offen offset:1024 sc1\n"
    "v_mfma_f32_16x16x32_bf16 v[96:99], a[160:163], a[168:171], v[96:99]\n"
    "ds_read_b128 a[232:235], v13 offset:8192\n"
    "v_mfma_f32_16x16x32_bf16 v[96:99], a[164:167], a[172:175], v[96:99]\n"
    "ds_read_b128 a[236:239], v13 offset:9216\n"
    "v_mfma_f32_16x16x32_bf16 v[108:111], a[164:167], a[176:179], v[108:111]\n"
    "v_mfma_f32_16x16x32_bf16 v[108:111], a[160:163], a[172:175], v[108:111]\n"
    "v_mfma_f32_16x16x32_bf16 v[108:111], a[156:159], a[168:171], v[108:111]\n"
    "buffer_load_dwordx4 a[40:43], v14, s[36:39], s68 offen offset:2048 sc1\n"
    "v_mfma_f32_16x16x32_bf16 v[120:123], a[156:159], a[172:175], v[120:123]\n"
    "ds_read_b128 a[168:171], v13 offset:10240\n"
    "v_mfma_f32_16x16x32_bf16 v[120:123], a[160:163], a[176:179], v[120:123]\n"
    "ds_read_b128 a[172:175], v13 offset:11264\n"
    "v_mfma_f32_16x16x32_bf16 v[120:123], a[164:167], a[180:183], v[120:123]\n"
    "v_mfma_f32_16x16x32_bf16 v[132:135], a[164:167], a[184:187], v[132:135]\n"
    "v_mfma_f32_16x16x32_bf16 v[132:135], a[160:163], a[180:183], v[132:135]\n"
    "buffer_load_dwordx4 a[44:47], v14, s[36:39], s68 offen offset:3072 sc1\n"
    "v_mfma_f32_16x16x32_bf16 v[132:135], a[156:159], a[176:179], v[132:135]\n"
    "ds_read_b128 a[176:179], v13 offset:12288\n"
    "v_mul_f32 v180, -0.5, v96\n"
    "v_mfma_f32_16x16x32_bf16 v[144:147], a[156:159], a[180:183], v[144:147]\n"
    "v_mul_f32 v181, -0.5, v97\n"
    "v_mul_f32 v182, -0.5, v98\n"
    "v_mfma_f32_16x16x32_bf16 v[144:147], a[160:163], a[184:187], v[144:147]\n"
    "v_mul_f32 v183, -0.5, v99\n"
    "v_mul_f32 v192, -0.5, v108\n"
    "v_mfma_f32_16x16x32_bf16 v[144:147], a[164:167], a[188:191], v[144:147]\n"
    "v_mul_f32 v193, -0.5, v109\n"
    "v_mul_f32 v194, -0.5, v110\n"
    "v_mfma_f32_16x16x32_bf16 v[156:159], a[164:167], a[192:195], v[156:159]\n"
    "s_addk_i32 s68, 0x1000\n"
    "v_mul_f32 v195, -0.5, v111\n"
    "v_mfma_f32_16x16x32_bf16 v[156:159], a[160:163], a[188:191], v[156:159]\n"
    "buffer_load_dwordx4 a[48:51], v14, s[36:39], s68 offen offset:0 sc1\n"
    "v_mfma_f32_16x16x32_bf16 v[156:159], a[156:159], a[184:187], v[156:159]\n"
    "v_mul_f32 v204, -0.5, v120\n"
    "v_mul_f32 v205, -0.5, v121\n"
    "v_mfma_f32_16x16x32_bf16 v[168:171], a[156:159], a[188:191], v[168:171]\n"
    "v_mul_f32 v206, -0.5, v122\n"
    "v_mul_f32 v207, -0.5, v123\n"
    "v_mfma_f32_16x16x32_bf16 v[168:171], a[160:163], a[192:195], v[168:171]\n"
    "s_waitcnt vmcnt(10)\n"
    "v_mul_f32 v216, -0.5, v132\n"
    "v_mul_f32 v217, -0.5, v133\n"
    "v_mfma_f32_16x16x32_bf16 v[168:171], a[164:167], a[196:199], v[168:171]\n"
    "s_waitcnt lgkmcnt(0)\n"
    "v_mul_f32 v218, -0.5, v134\n"
    "v_mul_f32 v219, -0.5, v135\n"
    "v_mfma_f32_16x16x32_bf16 v[88:91], a[0:3], a[200:203], v[88:91]\n"
    "buffer_load_dwordx4 a[52:55], v14, s[36:39], s68 offen offset:1024 sc1\n"
    "v_mfma_f32_16x16x32_bf16 v[88:91], a[4:7], a[204:207], v[88:91]\n"
    "v_mul_f32 v228, -0.5, v144\n"
    "v_mul_f32 v229, -0.5, v145\n"
    "v_mfma_f32_16x16x32_bf16 v[88:91], a[8:11], a[208:211], v[88:91]\n"
    "v_mul_f32 v230, -0.5, v146\n"
    "v_mul_f32 v231, -0.5, v147\n"
    "v_mfma_f32_16x16x32_bf16 v[100:103], a[8:11], a[212:215], v[100:103]\n"
    "v_mfma_f32_16x16x32_bf16 v[100:103], a[4:7], a[208:211], v[100:103]\n"
    "v_mul_f32 v240, -0.5, v156\n"
    "v_mul_f32 v241, -0.5, v157\n"
    "v_mfma_f32_16x16x32_bf16 v[100:103], a[0:3], a[204:207], v[100:103]\n"
    "buffer_load_dwordx4 a[56:59], v14, s[36:39], s68 offen offset:2048 sc1\n"
    "v_mfma_f32_16x16x32_bf16 v[112:115], a[0:3], a[208:211], v[112:115]\n"
    "v_mul_f32 v242, -0.5, v158\n"
    "v_mul_f32 v243, -0.5, v159\n"
    "v_mfma_f32_16x16x32_bf16 v[112:115], a[4:7], a[212:215], v[112:115]\n"
    "v_mul_f32 v252, -0.5, v168\n"
    "v_mul_f32 v253, -0.5, v169\n"
    "v_mfma_f32_16x16x32_bf16 v[112:115], a[8:11], a[216:219], v[112:115]\n"
    "ds_read_b64_tr_b16 a[240:241], v18 offset:0\n"
    "v_mul_f32 v254, -0.5, v170\n"
    "v_mfma_f32_16x16x32_bf16 v[124:127], a[8:11], a[220:223], v[124:127]\n"
    "ds_read_b64_tr_b16 a[242:243], v18 offset:512\n"
    "v_mul_f32 v255, -0.5, v171\n"
    "v_mfma_f32_16x16x32_bf16 v[124:127], a[4:7], a[216:219], v[124:127]\n"
    "buffer_load_dwordx4 a[60:63], v14, s[36:39], s68 offen offset:3072 sc1\n"
    "v_mfma_f32_16x16x32_bf16 v[124:127], a[0:3], a[212:215], v[124:127]\n"
    "ds_read_b64_tr_b16 a[244:245], v19 offset:0\n"
    "v_mfma_f32_16x16x32_bf16 v[136:139], a[0:3], a[216:219], v[136:139]\n"
    "ds_read_b64_tr_b16 a[246:247], v19 offset:512\n"
    "v_mfma_f32_16x16x32_bf16 v[136:139], a[4:7], a[220:223], v[136:139]\n"
    "v_mfma_f32_16x16x32_bf16 v[136:139], a[8:11], a[224:227], v[136:139]\n"
    "v_mfma_f32_16x16x32_bf16 v[148:151], a[8:11], a[228:231], v[148:151]\n"
    "s_addk_i32 s68, 0x1000\n"
    "v_mfma_f32_16x16x32_bf16 v[148:151], a[4:7], a[224:227], v[148:151]\n"
    "buffer_load_dwordx4 a[64:67], v14, s[36:39], s68 offen offset:0 sc1\n"
    "v_mfma_f32_16x16x32_bf16 v[148:151], a[0:3], a[220:223], v[148:151]\n"
    "v_mfma_f32_16x16x32_bf16 v[160:163], a[0:3], a[224:227], v[160:163]\n"
    "v_mfma_f32_16x16x32_bf16 v[160:163], a[4:7], a[228:231], v[160:163]\n"
    "v_mfma_f32_16x16x32_bf16 v[160:163], a[8:11], a[232:235], v[160:163]\n"
    "s_waitcnt lgkmcnt(0)\n"
    "v_mfma_f32_16x16x32_bf16 v[64:67], a[244:247], v[48:51], 0\n"
    "v_mfma_f32_16x16x32_bf16 v[60:63], a[240:243], v[48:51], 0\n"
    "s_waitcnt vmcnt(10)\n"
    "v_mfma_f32_16x16x32_bf16 v[88:91], a[12:15], a[212:215], v[88:91]\n"
    "buffer_load_dwordx4 a[68:71], v14, s[36:39], s68 offen offset:1024 sc1\n"
    "v_mfma_f32_16x16x32_bf16 v[88:91], a[16:19], a[216:219], v[88:91]\n"
    "ds_read_b64_tr_b16 a[248:249], v18 offset:2816\n"
    "v_mfma_f32_16x16x32_bf16 v[88:91], a[20:23], a[220:223], v[88:91]\n"
    "ds_read_b64_tr_b16 a[250:251], v18 offset:3328\n"
    "v_mfma_f32_16x16x32_bf16 v[88:91], a[24:27], a[224:227], v[88:91]\n"
    "ds_read_b64_tr_b16 a[252:253], v19 offset:2816\n"
    "v_mfma_f32_16x16x32_bf16 v[100:103], a[24:27], a[228:231], v[100:103]\n"
    "ds_read_b64_tr_b16 a[254:255], v19 offset:3328\n"
    "v_mfma_f32_16x16x32_bf16 v[100:103], a[20:23], a[224:227], v[100:103]\n"
    "buffer_load_dwordx4 a[72:75], v14, s[36:39], s68 offen offset:2048 sc1\n"
    "v_mfma_f32_16x16x32_bf16 v[100:103], a[16:19], a[220:223], v[100:103]\n"
    "v_cvt_pk_bf16_f32 v78, v64, v65\n"
    "v_cvt_pk_bf16_f32 v79, v66, v67\n"
    "v_mfma_f32_16x16x32_bf16 v[100:103], a[12:15], a[216:219], v[100:103]\n"
    "v_cvt_pk_bf16_f32 v76, v60, v61\n"
    "v_cvt_pk_bf16_f32 v77, v62, v63\n"
    "v_mfma_f32_16x16x32_bf16 v[112:115], a[12:15], a[220:223], v[112:115]\n"
    "ds_write_b32 v12, v78 offset:20488\n"
    "v_mfma_f32_16x16x32_bf16 v[112:115], a[16:19], a[224:227], v[112:115]\n"
    "v_mfma_f32_16x16x32_bf16 v[112:115], a[20:23], a[228:231], v[112:115]\n"
    "buffer_load_dwordx4 a[76:79], v14, s[36:39], s68 offen offset:3072 sc1\n"
    "v_mfma_f32_16x16x32_bf16 v[112:115], a[24:27], a[232:235], v[112:115]\n"
    "ds_write_b32 v12, v79 offset:20492\n"
    "v_mfma_f32_16x16x32_bf16 v[124:127], a[24:27], a[236:239], v[124:127]\n"
    "s_waitcnt lgkmcnt(2)\n"
    "v_mfma_f32_16x16x32_bf16 v[124:127], a[20:23], a[232:235], v[124:127]\n"
    "ds_write_b32 v12, v76 offset:20480\n"
    "v_mfma_f32_16x16x32_bf16 v[72:75], a[252:255], v[48:51], 0\n"
    "v_mfma_f32_16x16x32_bf16 v[68:71], a[248:251], v[48:51], 0\n"
    "v_mfma_f32_16x16x32_bf16 v[124:127], a[16:19], a[228:231], v[124:127]\n"
    "v_mfma_f32_16x16x32_bf16 v[124:127], a[12:15], a[224:227], v[124:127]\n"
    "s_addk_i32 s68, 0x1000\n"
    "ds_write_b32 v12, v77 offset:20484\n"
    "v_mfma_f32_16x16x32_bf16 v[136:139], a[12:15], a[228:231], v[136:139]\n"
    "buffer_load_dwordx4 a[80:83], v14, s[36:39], s68 offen offset:0 sc1\n"
    "v_mfma_f32_16x16x32_bf16 v[136:139], a[16:19], a[232:235], v[136:139]\n"
    "ds_read_b64_tr_b16 a[240:241], v18 offset:5632\n"
    "v_mfma_f32_16x16x32_bf16 v[136:139], a[20:23], a[236:239], v[136:139]\n"
    "ds_read_b64_tr_b16 a[242:243], v18 offset:6144\n"
    "v_mfma_f32_16x16x32_bf16 v[136:139], a[24:27], a[168:171], v[136:139]\n"
    "ds_read_b64_tr_b16 a[244:245], v19 offset:5632\n"
    "v_mfma_f32_16x16x32_bf16 v[148:151], a[24:27], a[172:175], v[148:151]\n"
    "v_cvt_pk_bf16_f32 v82, v72, v73\n"
    "v_cvt_pk_bf16_f32 v83, v74, v75\n"
    "v_mfma_f32_16x16x32_bf16 v[148:151], a[20:23], a[168:171], v[148:151]\n"
    "global_load_dwordx4 v[52:55], v14, %[tab] offset:2048\n"
    "v_mfma_f32_16x16x32_bf16 v[148:151], a[16:19], a[236:239], v[148:151]\n"
    "v_cvt_pk_bf16_f32 v80, v68, v69\n"
    "v_cvt_pk_bf16_f32 v81, v70, v71\n"
    "v_mfma_f32_16x16x32_bf16 v[148:151], a[12:15], a[232:235], v[148:151]\n"
    "ds_write_b32 v12, v82 offset:24584\n"
    "v_mfma_f32_16x16x32_bf16 v[160:163], a[12:15], a[236:239], v[160:163]\n"
    "ds_read_b64_tr_b16 a[246:247], v19 offset:6144\n"
    "v_mfma_f32_16x16x32_bf16 v[160:163], a[16:19], a[168:171], v[160:163]\n"
    "ds_write_b32 v12, v83 offset:24588\n"
    "v_mfma_f32_16x16x32_bf16 v[160:163], a[20:23], a[172:175], v[160:163]\n"
    "s_waitcnt vmcnt(12)\n"
    "s_add_u32 s68, s63, 0\n"
    "v_mfma_f32_16x16x32_bf16 v[160:163], a[24:27], a[176:179], v[160:163]\n"
    "buffer_load_dwordx4 a[84:87], v14, s[36:39], s68 offen offset:0 sc1\n"
    "v_mfma_f32_16x16x32_bf16 v[92:95], a[28:31], a[200:203], v[92:95]\n"
    "ds_write_b32 v12, v80 offset:24576\n"
    "v_mfma_f32_16x16x32_bf16 v[92:95], a[32:35], a[204:207], v[92:95]\n"
    "ds_read_b64_tr_b16 a[248:249], v18 offset:8448\n"
    "v_mfma_f32_16x16x32_bf16 v[92:95], a[36:39], a[208:211], v[92:95]\n"
    "ds_write_b32 v12, v81 offset:24580\n"
    "v_mfma_f32_16x16x32_bf16 v[104:107], a[36:39], a[212:215], v[104:107]\n"
    "ds_read_b64_tr_b16 a[250:251], v18 offset:8960\n"
    "v_mfma_f32_16x16x32_bf16 v[104:107], a[32:35], a[208:211], v[104:107]\n"
    "buffer_load_dwordx4 a[88:91], v14, s[36:39], s68 offen offset:1024 sc1\n"
    "v_mfma_f32_16x16x32_bf16 v[104:107], a[28:31], a[204:207], v[104:107]\n"
    "ds_read_b64_tr_b16 a[252:253], v19 offset:8448\n"
    "v_mfma_f32_16x16x32_bf16 v[116:119], a[28:31], a[208:211], v[116:119]\n"
    "s_waitcnt lgkmcnt(5)\n"
    "ds_read_b64_tr_b16 a[254:255], v19 offset:8960\n"
    "v_mfma_f32_16x16x32_bf16 v[64:67], a[244:247], v[48:51], 0\n"
    "v_mfma_f32_16x16x32_bf16 v[60:63], a[240:243], v[48:51], 0\n"
    "v_mfma_f32_16x16x32_bf16 v[116:119], a[32:35], a[212:215], v[116:119]\n"
    "v_mfma_f32_16x16x32_bf16 v[116:119], a[36:39], a[216:219], v[116:119]\n"
    ".Lpad1:\n"
    "s_cbranch_execz .Lpad5\n"
    "v_mfma_f32_16x16x32_bf16 v[128:131], a[36:39], a[220:223], v[128:131]\n"
    "buffer_load_dwordx4 a[92:95], v14, s[36:39], s68 offen offset:2048 sc1\n"
    "v_mfma_f32_16x16x32_bf16 v[128:131], a[32:35], a[216:219], v[128:131]\n"
    "v_mfma_f32_16x16x32_bf16 v[128:131], a[28:31], a[212:215], v[128:131]\n"
    "v_mfma_f32_16x16x32_bf16 v[140:143], a[28:31], a[216:219], v[140:143]\n"
    "v_mfma_f32_16x16x32_bf16 v[140:143], a[32:35], a[220:223], v[140:143]\n"
    "v_cvt_pk_bf16_f32 v78, v64, v65\n"
    "v_cvt_pk_bf16_f32 v79, v66, v67\n"
    "v_mfma_f32_16x16x32_bf16 v[140:143], a[36:39], a[224:227], v[140:143]\n"
    "buffer_load_dwordx4 a[96:99], v14, s[36:39], s68 offen offset:3072 sc1\n"
    "v_mfma_f32_16x16x32_bf16 v[152:155], a[36:39], a[228:231], v[152:155]\n"
    "v_cvt_pk_bf16_f32 v76, v60, v61\n"
    "v_cvt_pk_bf16_f32 v77, v62, v63\n"
    "v_mfma_f32_16x16x32_bf16 v[152:155], a[32:35], a[224:227], v[152:155]\n"
    "ds_write_b32 v12, v78 offset:28680\n"
    "v_mfma_f32_16x16x32_bf16 v[152:155], a[28:31], a[220:223], v[152:155]\n"
    "ds_read_b64_tr_b16 a[240:241], v18 offset:11264\n"
    "v_mfma_f32_16x16x32_bf16 v[164:167], a[28:31], a[224:227], v[164:167]\n"
    "ds_write_b32 v12, v79 offset:28684\n"
    "v_mfma_f32_16x16x32_bf16 v[164:167], a[32:35], a[228:231], v[164:167]\n"
    "s_waitcnt vmcnt(12)\n"
    "s_addk_i32 s68, 0x1000\n"
    "ds_read_b64_tr_b16 a[242:243], v18 offset:11776\n"
    "v_mfma_f32_16x16x32_bf16 v[164:167], a[36:39], a[232:235], v[164:167]\n"
    "buffer_load_dwordx4 a[100:103], v14, s[36:39], s68 offen offset:0 sc1\n"
    "v_mfma_f32_16x16x32_bf16 v[92:95], a[40:43], a[212:215], v[92:95]\n"
    "ds_write_b32 v12, v76 offset:28672\n"
    "s_waitcnt lgkmcnt(5)\n"
    "v_mfma_f32_16x16x32_bf16 v[72:75], a[252:255], v[48:51], 0\n"
    "v_mfma_f32_16x16x32_bf16 v[68:71], a[248:251], v[48:51], 0\n"
    "v_mfma_f32_16x16x32_bf16 v[92:95], a[44:47], a[216:219], v[92:95]\n"
    "ds_read_b64_tr_b16 a[244:245], v19 offset:11264\n"
    "v_mfma_f32_16x16x32_bf16 v[92:95], a[48:51], a[220:223], v[92:95]\n"
    "ds_write_b32 v12, v77 offset:28676\n"
    "v_mfma_f32_16x16x32_bf16 v[92:95], a[52:55], a[224:227], v[92:95]\n"
    "ds_read_b64_tr_b16 a[246:247], v19 offset:11776\n"
    "v_mfma_f32_16x16x32_bf16 v[104:107], a[52:55], a[228:231], v[104:107]\n"
    "buffer_load_dwordx4 a[104:107], v14, s[36:39], s68 offen offset:1024 sc1\n"
    "v_mfma_f32_16x16x32_bf16 v[104:107], a[48:51], a[224:227], v[104:107]\n"
    "v_mfma_f32_16x16x32_bf16 v[104:107], a[44:47], a[220:223], v[104:107]\n"
    "v_mfma_f32_16x16x32_bf16 v[104:107], a[40:43], a[216:219], v[104:107]\n"
    "v_cvt_pk_bf16_f32 v82, v72, v73\n"
    "v_cvt_pk_bf16_f32 v83, v74, v75\n"
    "v_mfma_f32_16x16x32_bf16 v[116:119], a[40:43], a[220:223], v[116:119]\n"
    "v_cvt_pk_bf16_f32 v80, v68, v69\n"
    "v_cvt_pk_bf16_f32 v81, v70, v71\n"
    "v_mfma_f32_16x16x32_bf16 v[116:119], a[44:47], a[224:227], v[116:119]\n"
    "buffer_load_dwordx4 a[108:111], v14, s[36:39], s68 offen offset:2048 sc1\n"
    "v_mfma_f32_16x16x32_bf16 v[116:119], a[48:51], a[228:231], v[116:119]\n"
    "ds_write_b32 v12, v82 offset:32776\n"
    "v_mfma_f32_16x16x32_bf16 v[116:119], a[52:55], a[232:235], v[116:119]\n"
    "v_mfma_f32_16x16x32_bf16 v[128:131], a[52:55], a[236:239], v[128:131]\n"
    "ds_write_b32 v12, v83 offset:32780\n"
    "v_mfma_f32_16x16x32_bf16 v[128:131], a[48:51], a[232:235], v[128:131]\n"
    "s_waitcnt lgkmcnt(2)\n"
    "v_mfma_f32_16x16x32_bf16 v[64:67], a[244:247], v[48:51], 0\n"
    "v_mfma_f32_16x16x32_bf16 v[60:63], a[240:243], v[48:51], 0\n"
    "v_mfma_f32_16x16x32_bf16 v[128:131], a[44:47], a[228:231], v[128:131]\n"
    "buffer_load_dwordx4 a[112:115], v14, s[36:39], s68 offen offset:3072 sc1\n"
    "v_mfma_f32_16x16x32_bf16 v[128:131], a[40:43], a[224:227], v[128:131]\n"
    "ds_write_b32 v12, v80 offset:32768\n"
    "v_mfma_f32_16x16x32_bf16 v[140:143], a[40:43], a[228:231], v[140:143]\n"
    "v_add_u32 v37, 1, v37\n"
    "v_mfma_f32_16x16x32_bf16 v[140:143], a[44:47], a[232:235], v[140:143]\n"
    "ds_write_b32 v12, v81 offset:32772\n"
    "v_mfma_f32_16x16x32_bf16 v[140:143], a[48:51], a[236:239], v[140:143]\n"
    "v_mfma_f32_16x16x32_bf16 v[140:143], a[52:55], a[168:171], v[140:143]\n"
    "s_addk_i32 s68, 0x1000\n"
    "v_mfma_f32_16x16x32_bf16 v[152:155], a[52:55], a[172:175], v[152:155]\n"
    "buffer_load_dwordx4 a[116:119], v14, s[36:39], s68 offen offset:0 sc1\n"
    "v_mfma_f32_16x16x32_bf16 v[152:155], a[48:51], a[168:171], v[152:155]\n"
    "v_cvt_pk_bf16_f32 v78, v64, v65\n"
    "v_cvt_pk_bf16_f32 v79, v66, v67\n"
    "v_mfma_f32_16x16x32_bf16 v[152:155], a[44:47], a[236:239], v[152:155]\n"
    "v_cvt_pk_bf16_f32 v76, v60, v61\n"
    "v_cvt_pk_bf16_f32 v77, v62, v63\n"
    "v_mfma_f32_16x16x32_bf16 v[152:155], a[40:43], a[232:235], v[152:155]\n"
    "ds_write_b32 v12, v78 offset:36872\n"
    "v_mfma_f32_16x16x32_bf16 v[164:167], a[40:43], a[236:239], v[164:167]\n"
    "v_mfma_f32_16x16x32_bf16 v[164:167], a[44:47], a[168:171], v[164:167]\n"
    "buffer_load_dwordx4 a[120:123], v14, s[36:39], s68 offen offset:1024 sc1\n"
    "v_mfma_f32_16x16x32_bf16 v[164:167], a[48:51], a[172:175], v[164:167]\n"
    "ds_write_b32 v12, v79 offset:36876\n"
    "v_mfma_f32_16x16x32_bf16 v[164:167], a[52:55], a[176:179], v[164:167]\n"
    "s_waitcnt vmcnt(15)\n"
    "v_mfma_f32_16x16x32_bf16 v[96:99], a[56:59], a[200:203], v[96:99]\n"
    "ds_write_b32 v12, v76 offset:36864\n"
    "v_mfma_f32_16x16x32_bf16 v[96:99], a[60:63], a[204:207], v[96:99]\n"
    "v_mfma_f32_16x16x32_bf16 v[96:99], a[64:67], a[208:211], v[96:99]\n"
    "buffer_load_dwordx4 a[124:127], v14, s[36:39], s68 offen offset:2048 sc1\n"
    "v_mfma_f32_16x16x32_bf16 v[108:111], a[64:67], a[212:215], v[108:111]\n"
    "ds_write_b32 v12, v77 offset:36868\n"
    "v_mfma_f32_16x16x32_bf16 v[108:111], a[60:63], a[208:211], v[108:111]\n"
    "ds_write_b32 v35, v37\n"
    "v_mfma_f32_16x16x32_bf16 v[108:111], a[56:59], a[204:207], v[108:111]\n"
    "v_mfma_f32_16x16x32_bf16 v[120:123], a[56:59], a[208:211], v[120:123]\n"
    "v_mfma_f32_16x16x32_bf16 v[120:123], a[60:63], a[212:215], v[120:123]\n"
    "buffer_load_dwordx4 a[128:131], v14, s[36:39], s68 offen offset:3072 sc1\n"
    "v_mfma_f32_16x16x32_bf16 v[120:123], a[64:67], a[216:219], v[120:123]\n"
    "v_mfma_f32_16x16x32_bf16 v[132:135], a[64:67], a[220:223], v[132:135]\n"
    "v_mfma_f32_16x16x32_bf16 v[132:135], a[60:63], a[216:219], v[132:135]\n"
    "v_mfma_f32_16x16x32_bf16 v[132:135], a[56:59], a[212:215], v[132:135]\n"
    "v_mfma_f32_16x16x32_bf16 v[144:147], a[56:59], a[216:219], v[144:147]\n"
    "s_addk_i32 s68, 0x1000\n"
    "ds_read_b32 v38, v36 offset:30592\n"
    "v_mfma_f32_16x16x32_bf16 v[144:147], a[60:63], a[220:223], v[144:147]\n"
    "buffer_load_dwordx4 a[132:135], v14, s[36:39], s68 offen offset:0 sc1\n"
    "v_mfma_f32_16x16x32_bf16 v[144:147], a[64:67], a[224:227], v[144:147]\n"
    "v_mfma_f32_16x16x32_bf16 v[156:159], a[64:67], a[228:231], v[156:159]\n"
    "v_mfma_f32_16x16x32_bf16 v[156:159], a[60:63], a[224:227], v[156:159]\n"
    "v_mfma_f32_16x16x32_bf16 v[156:159], a[56:59], a[220:223], v[156:159]\n"
    "s_waitcnt lgkmcnt(0)\n"
    "v_cmp_gt_u32 vcc, 3, v38\n"
    "v_mfma_f32_16x16x32_bf16 v[168:171], a[56:59], a[224:227], v[168:171]\n"
    "buffer_load_dwordx4 a[136:139], v14, s[36:39], s68 offen offset:1024 sc1\n"
    "v_mfma_f32_16x16x32_bf16 v[168:171], a[60:63], a[228:231], v[168:171]\n"
    "s_cbranch_vccnz .Lsl3\n"
    ".Lbk3:\n"
    "v_mfma_f32_16x16x32_bf16 v[168:171], a[64:67], a[232:235], v[168:171]\n"
    "s_waitcnt vmcnt(15)\n"
    "ds_read_b128 a[180:183], v13 offset:20480\n"
    "v_mfma_f32_16x16x32_bf16 v[96:99], a[68:71], a[212:215], v[96:99]\n"
    "ds_read_b128 a[184:187], v13 offset:21504\n"
    "v_mfma_f32_16x16x32_bf16 v[96:99], a[72:75], a[216:219], v[96:99]\n"
    "ds_read_b128 a[188:191], v13 offset:22528\n"
    "v_mfma_f32_16x16x32_bf16 v[96:99], a[76:79], a[220:223], v[96:99]\n"
    "buffer_load_dwordx2 v[42:43], v15, s[36:39], 0 offen offset:2048 sc1\n"
    "v_mfma_f32_16x16x32_bf16 v[96:99], a[80:83], a[224:227], v[96:99]\n"
    "ds_read_b128 a[192:195], v13 offset:23552\n"
    "v_mfma_f32_16x16x32_bf16 v[108:111], a[80:83], a[228:231], v[108:111]\n"
    "buffer_load_dwordx4 a[140:143], v14, s[36:39], s68 offen offset:2048 sc1\n"
    "v_mfma_f32_16x16x32_bf16 v[108:111], a[76:79], a[224:227], v[108:111]\n"
    "ds_read_b128 a[196:199], v13 offset:24576\n"
    "v_mfma_f32_16x16x32_bf16 v[108:111], a[72:75], a[220:223], v[108:111]\n"
    "ds_read_b128 a[200:203], v13 offset:25600\n"
    "v_mfma_f32_16x16x32_bf16 v[108:111], a[68:71], a[216:219], v[108:111]\n"
    "ds_read_b128 a[204:207], v13 offset:26624\n"
    "v_mfma_f32_16x16x32_bf16 v[120:123], a[68:71], a[220:223], v[120:123]\n"
    "ds_read_b128 a[208:211], v13 offset:27648\n"
    "v_mfma_f32_16x16x32_bf16 v[120:123], a[72:75], a[224:227], v[120:123]\n"
    "buffer_load_dwordx4 a[144:147], v14, s[36:39], s68 offen offset:3072 sc1\n"
    "v_mfma_f32_16x16x32_bf16 v[120:123], a[76:79], a[228:231], v[120:123]\n"
    "ds_read_b128 a[212:215], v13 offset:28672\n"
    "v_mfma_f32_16x16x32_bf16 v[120:123], a[80:83], a[232:235], v[120:123]\n"
    "ds_read_b128 a[216:219], v13 offset:29696\n"
    "v_mfma_f32_16x16x32_bf16 v[132:135], a[80:83], a[236:239], v[132:135]\n"
    "ds_read_b128 a[220:223], v13 offset:30720\n"
    "v_mfma_f32_16x16x32_bf16 v[132:135], a[76:79], a[232:235], v[132:135]\n"
    "v_mfma_f32_16x16x32_bf16 v[132:135], a[72:75], a[228:231], v[132:135]\n"
    "s_addk_i32 s68, 0x1000\n"
    "v_mfma_f32_16x16x32_bf16 v[132:135], a[68:71], a[224:227], v[132:135]\n"
    "buffer_load_dwordx4 a[148:151], v14, s[36:39], s68 offen offset:0 sc1\n"
    "v_mfma_f32_16x16x32_bf16 v[144:147], a[68:71], a[228:231], v[144:147]\n"
    "ds_read_b128 a[224:227], v13 offset:31744\n"
    "v_mfma_f32_16x16x32_bf16 v[144:147], a[72:75], a[232:235], v[144:147]\n"
    "ds_read_b128 a[228:231], v13 offset:32768\n"
    "v_mfma_f32_16x16x32_bf16 v[144:147], a[76:79], a[236:239], v[144:147]\n"
    "v_mfma_f32_16x16x32_bf16 v[144:147], a[80:83], a[168:171], v[144:147]\n"
    "v_mfma_f32_16x16x32_bf16 v[156:159], a[80:83], a[172:175], v[156:159]\n"
    "buffer_load_dwordx4 a[152:155], v14, s[36:39], s68 offen offset:1024 sc1\n"
    "v_mfma_f32_16x16x32_bf16 v[156:159], a[76:79], a[168:171], v[156:159]\n"
    "v_mfma_f32_16x16x32_bf16 v[156:159], a[72:75], a[236:239], v[156:159]\n"
    "v_mfma_f32_16x16x32_bf16 v[156:159], a[68:71], a[232:235], v[156:159]\n"
    "v_mfma_f32_16x16x32_bf16 v[168:171], a[68:71], a[236:239], v[168:171]\n"
    "v_mfma_f32_16x16x32_bf16 v[168:171], a[72:75], a[168:171], v[168:171]\n"
    "buffer_load_dwordx4 a[156:159], v14, s[36:39], s68 offen offset:2048 sc1\n"
    "v_mfma_f32_16x16x32_bf16 v[168:171], a[76:79], a[172:175], v[168:171]\n"
    "v_mfma_f32_16x16x32_bf16 v[168:171], a[80:83], a[176:179], v[168:171]\n"
    "s_waitcnt lgkmcnt(0)\n"
    "s_waitcnt vmcnt(13)\n"
    "v_mfma_f32_16x16x32_bf16 v[88:91], a[84:87], a[180:183], v[88:91]\n"
    "v_mfma_f32_16x16x32_bf16 v[88:91], a[88:91], a[184:187], v[88:91]\n"
    "v_mfma_f32_16x16x32_bf16 v[88:91], a[92:95], a[188:191], v[88:91]\n"
    "buffer_load_dwordx4 a[160:163], v14, s[36:39], s68 offen offset:3072 sc1\n"
    "v_mfma_f32_16x16x32_bf16 v[88:91], a[96:99], a[192:195], v[88:91]\n"
    "v_mfma_f32_16x16x32_bf16 v[88:91], a[100:103], a[196:199], v[88:91]\n"
    "v_mfma_f32_16x16x32_bf16 v[88:91], a[104:107], a[200:203], v[88:91]\n"
    "v_mfma_f32_16x16x32_bf16 v[88:91], a[108:111], a[204:207], v[88:91]\n"
    "v_mfma_f32_16x16x32_bf16 v[100:103], a[108:111], a[208:211], v[100:103]\n"
    "s_addk_i32 s68, 0x1000\n"
    "v_mfma_f32_16x16x32_bf16 v[100:103], a[104:107], a[204:207], v[100:103]\n"
    "buffer_load_dwordx4 a[164:167], v14, s[36:39], s68 offen offset:0 sc1\n"
    "v_mfma_f32_16x16x32_bf16 v[100:103], a[100:103], a[200:203], v[100:103]\n"
    "s_waitcnt vmcnt(7)\n"
    "ds_read_b64_tr_b16 a[240:241], v16 offset:0\n"
    "v_cmp_ne_u64 vcc, v[42:43], v[40:41]\n"
    "v_mfma_f32_16x16x32_bf16 v[100:103], a[96:99], a[196:199], v[100:103]\n"
    "ds_read_b64_tr_b16 a[242:243], v16 offset:512\n"
    "v_mfma_f32_16x16x32_bf16 v[100:103], a[92:95], a[192:195], v[100:103]\n"
    "s_cbranch_vccnz .Lpoll2\n"
    ".Lpoll_ok2:\n"
    "v_mfma_f32_16x16x32_bf16 v[100:103], a[88:91], a[188:191], v[100:103]\n"
    "s_add_u32 s68, s62, 258048\n"
    "ds_read_b64_tr_b16 a[244:245], v17 offset:0\n"
    "v_mfma_f32_16x16x32_bf16 v[100:103], a[84:87], a[184:187], v[100:103]\n"
    "buffer_load_dwordx4 a[0:3], v14, s[36:39], s68 offen offset:0 sc1\n"
    "v_mfma_f32_16x16x32_bf16 v[112:115], a[84:87], a[188:191], v[112:115]\n"
    "ds_read_b64_tr_b16 a[246:247], v17 offset:512\n"
    "v_add_f32 v172, v172, v88\n"
    "v_mfma_f32_16x16x32_bf16 v[112:115], a[88:91], a[192:195], v[112:115]\n"
    "v_add_f32 v173, v173, v89\n"
    "v_add_f32 v174, v174, v90\n"
    "v_mfma_f32_16x16x32_bf16 v[112:115], a[92:95], a[196:199], v[112:115]\n"
    "v_add_f32 v175, v175, v91\n"
    "v_mfma_f32_16x16x32_bf16 v[112:115], a[96:99], a[200:203], v[112:115]\n"
    "v_mfma_f32_16x16x32_bf16 v[112:115], a[100:103], a[204:207], v[112:115]\n"
    "buffer_load_dwordx4 a[4:7], v14, s[36:39], s68 offen offset:1024 sc1\n"
    "v_mfma_f32_16x16x32_bf16 v[112:115], a[104:107], a[208:211], v[112:115]\n"
    "v_mfma_f32_16x16x32_bf16 v[112:115], a[108:111], a[212:215], v[112:115]\n"
    "v_mfma_f32_16x16x32_bf16 v[124:127], a[108:111], a[216:219], v[124:127]\n"
    "v_add_f32 v184, v184, v100\n"
    "v_add_f32 v185, v185, v101\n"
    "v_mfma_f32_16x16x32_bf16 v[124:127], a[104:107], a[212:215], v[124:127]\n"
    "ds_read_b64_tr_b16 a[248:249], v16 offset:2816\n"
    "v_add_f32 v186, v186, v102\n"
    "v_mfma_f32_16x16x32_bf16 v[124:127], a[100:103], a[208:211], v[124:127]\n"
    "buffer_load_dwordx4 a[8:11], v14, s[36:39], s68 offen offset:2048 sc1\n"
    "v_mfma_f32_16x16x32_bf16 v[124:127], a[96:99], a[204:207], v[124:127]\n"
    "ds_read_b64_tr_b16 a[250:251], v16 offset:3328\n"
    "v_add_f32 v187, v187, v103\n"
    "v_mfma_f32_16x16x32_bf16 v[124:127], a[92:95], a[200:203], v[124:127]\n"
    "ds_read_b64_tr_b16 a[252:253], v17 offset:2816\n"
    "v_mfma_f32_16x16x32_bf16 v[124:127], a[88:91], a[196:199], v[124:127]\n"
    "ds_read_b64_tr_b16 a[254:255], v17 offset:3328\n"
    "v_mfma_f32_16x16x32_bf16 v[124:127], a[84:87], a[192:195], v[124:127]\n"
    "s_waitcnt lgkmcnt(4)\n"
    "v_mfma_f32_16x16x32_bf16 v[64:67], a[244:247], v[52:55], 0\n"
    "v_mfma_f32_16x16x32_bf16 v[60:63], a[240:243], v[52:55], 0\n"
    "v_mfma_f32_16x16x32_bf16 v[136:139], a[84:87], a[196:199], v[136:139]\n"
    "buffer_load_dwordx4 a[12:15], v14, s[36:39], s68 offen offset:3072 sc1\n"
    "v_mfma_f32_16x16x32_bf16 v[136:139], a[88:91], a[200:203], v[136:139]\n"
    "v_add_f32 v196, v196, v112\n"
    "v_add_f32 v197, v197, v113\n"
    "v_mfma_f32_16x16x32_bf16 v[136:139], a[92:95], a[204:207], v[136:139]\n"
    "v_add_f32 v198, v198, v114\n"
    "v_add_f32 v199, v199, v115\n"
    "v_mfma_f32_16x16x32_bf16 v[136:139], a[96:99], a[208:211], v[136:139]\n"
    "v_mfma_f32_16x16x32_bf16 v[136:139], a[100:103], a[212:215], v[136:139]\n"
    "v_mfma_f32_16x16x32_bf16 v[136:139], a[104:107], a[216:219], v[136:139]\n"
    "s_addk_i32 s68, 0x1000\n"
    "v_mfma_f32_16x16x32_bf16 v[136:139], a[108:111], a[220:223], v[136:139]\n"
    "buffer_load_dwordx4 a[16:19], v14, s[36:39], s68 offen offset:0 sc1\n"
    "v_mfma_f32_16x16x32_bf16 v[148:151], a[108:111], a[224:227], v[148:151]\n"
    "s_waitcnt lgkmcnt(0)\n"
    "v_cvt_pk_bf16_f32 v78, v64, v65\n"
    "v_cvt_pk_bf16_f32 v79, v66, v67\n"
    "v_mfma_f32_16x16x32_bf16 v[72:75], a[252:255], v[52:55], 0\n"
    "v_mfma_f32_16x16x32_bf16 v[68:71], a[248:251], v[52:55], 0\n"
    "v_mfma_f32_16x16x32_bf16 v[148:151], a[104:107], a[220:223], v[148:151]\n"
    "v_cvt_pk_bf16_f32 v76, v60, v61\n"
    "v_cvt_pk_bf16_f32 v77, v62, v63\n"
    "v_mfma_f32_16x16x32_bf16 v[148:151], a[100:103], a[216:219], v[148:151]\n"
    "ds_write_b32 v12, v78 offset:8\n"
    "v_mfma_f32_16x16x32_bf16 v[148:151], a[96:99], a[212:215], v[148:151]\n"
    "ds_read_b64_tr_b16 a[240:241], v16 offset:5632\n"
    "v_add_f32 v208, v208, v124\n"
    "v_mfma_f32_16x16x32_bf16 v[148:151], a[92:95], a[208:211], v[148:151]\n"
    "buffer_load_dwordx4 a[20:23], v14, s[36:39], s68 offen offset:1024 sc1\n"
    "v_mfma_f32_16x16x32_bf16 v[148:151], a[88:91], a[204:207], v[148:151]\n"
    "ds_write_b32 v12, v79 offset:12\n"
    "v_mfma_f32_16x16x32_bf16 v[148:151], a[84:87], a[200:203], v[148:151]\n"
    "ds_read_b64_tr_b16 a[242:243], v16 offset:6144\n"
    "v_add_f32 v209, v209, v125\n"
    "v_mfma_f32_16x16x32_bf16 v[160:163], a[84:87], a[204:207], v[160:163]\n"
    "ds_write_b32 v12, v76 offset:0\n"
    "v_mfma_f32_16x16x32_bf16 v[160:163], a[88:91], a[208:211], v[160:163]\n"
    "v_cvt_pk_bf16_f32 v82, v72, v73\n"
    "v_cvt_pk_bf16_f32 v83, v74, v75\n"
    "v_mfma_f32_16x16x32_bf16 v[160:163], a[92:95], a[212:215], v[160:163]\n"
    "buffer_load_dwordx4 a[24:27], v14, s[36:39], s68 offen offset:2048 sc1\n"
    "v_mfma_f32_16x16x32_bf16 v[160:163], a[96:99], a[216:219], v[160:163]\n"
    "ds_write_b32 v12, v77 offset:4\n"
    "v_mfma_f32_16x16x32_bf16 v[160:163], a[100:103], a[220:223], v[160:163]\n"
    "v_cvt_pk_bf16_f32 v80, v68, v69\n"
    "v_cvt_pk_bf16_f32 v81, v70, v71\n"
    "v_mfma_f32_16x16x32_bf16 v[160:163], a[104:107], a[224:227], v[160:163]\n"
    "ds_write_b32 v12, v82 offset:4104\n"
    "v_mfma_f32_16x16x32_bf16 v[160:163], a[108:111], a[228:231], v[160:163]\n"
    "ds_read_b64_tr_b16 a[244:245], v17 offset:5632\n"
    "v_add_f32 v210, v210, v126\n"
    "v_mfma_f32_16x16x32_bf16 v[92:95], a[112:115], a[180:183], v[92:95]\n"
    "buffer_load_dwordx4 a[28:31], v14, s[36:39], s68 offen offset:3072 sc1\n"
    "v_mfma_f32_16x16x32_bf16 v[92:95], a[116:119], a[184:187], v[92:95]\n"
    "ds_write_b32 v12, v83 offset:4108\n"
    "v_mfma_f32_16x16x32_bf16 v[92:95], a[120:123], a[188:191], v[92:95]\n"
    "ds_read_b64_tr_b16 a[246:247], v17 offset:6144\n"
    "v_add_f32 v211, v211, v127\n"
    "v_mfma_f32_16x16x32_bf16 v[92:95], a[124:127], a[192:195], v[92:95]\n"
    "ds_write_b32 v12, v80 offset:4096\n"
    "v_mfma_f32_16x16x32_bf16 v[92:95], a[128:131], a[196:199], v[92:95]\n"
    "ds_read_b64_tr_b16 a[248:249], v16 offset:8448\n"
    "v_add_f32 v220, v220, v136\n"
    "v_mfma_f32_16x16x32_bf16 v[92:95], a[132:135], a[200:203], v[92:95]\n"
    "s_addk_i32 s68, 0x1000\n"
    "ds_write_b32 v12, v81 offset:4100\n"
    "v_mfma_f32_16x16x32_bf16 v[92:95], a[136:139], a[204:207], v[92:95]\n"
    "buffer_load_dwordx4 a[32:35], v14, s[36:39], s68 offen offset:0 sc1\n"
    "v_mfma_f32_16x16x32_bf16 v[104:107], a[136:139], a[208:211], v[104:107]\n"
    "ds_read_b64_tr_b16 a[250:251], v16 offset:8960\n"
    "v_add_f32 v221, v221, v137\n"
    "v_mfma_f32_16x16x32_bf16 v[104:107], a[132:135], a[204:207], v[104:107]\n"
    "ds_read_b64_tr_b16 a[252:253], v17 offset:8448\n"
    "v_add_f32 v222, v222, v138\n"
    "v_mfma_f32_16x16x32_bf16 v[104:107], a[128:131], a[200:203], v[104:107]\n"
    "ds_read_b64_tr_b16 a[254:255], v17 offset:8960\n"
    "v_add_f32 v223, v223, v139\n"
    "v_mfma_f32_16x16x32_bf16 v[104:107], a[124:127], a[196:199], v[104:107]\n"
    "v_add_f32 v232, v232, v148\n"
    "v_add_f32 v233, v233, v149\n"
    "v_mfma_f32_16x16x32_bf16 v[104:107], a[120:123], a[192:195], v[104:107]\n"
    "buffer_load_dwordx4 a[36:39], v14, s[36:39], s68 offen offset:1024 sc1\n"
    "v_mfma_f32_16x16x32_bf16 v[104:107], a[116:119], a[188:191], v[104:107]\n"
    "v_add_f32 v234, v234, v150\n"
    "v_add_f32 v235, v235, v151\n"
    "v_mfma_f32_16x16x32_bf16 v[104:107], a[112:115], a[184:187], v[104:107]\n"
    "s_waitcnt lgkmcnt(4)\n"
    "v_add_f32 v244, v244, v160\n"
    "v_add_f32 v245, v245, v161\n"
    "v_mfma_f32_16x16x32_bf16 v[64:67], a[244:247], v[52:55], 0\n"
    "v_mfma_f32_16x16x32_bf16 v[60:63], a[240:243], v[52:55], 0\n"
    "v_mfma_f32_16x16x32_bf16 v[116:119], a[112:115], a[188:191], v[116:119]\n"
    "v_add_f32 v246, v246, v162\n"
    "v_add_f32 v247, v247, v163\n"
    "v_mfma_f32_16x16x32_bf16 v[116:119], a[116:119], a[192:195], v[116:119]\n"
    "ds_read_b64_tr_b16 a[240:241], v16 offset:11264\n"
    "v_add_f32 v176, v176, v92\n"
    "v_mfma_f32_16x16x32_bf16 v[116:119], a[120:123], a[196:199], v[116:119]\n"
    "buffer_load_dwordx4 a[40:43], v14, s[36:39], s68 offen offset:2048 sc1\n"
    "v_mfma_f32_16x16x32_bf16 v[116:119], a[124:127], a[200:203], v[116:119]\n"
    "ds_read_b64_tr_b16 a[242:243], v16 offset:11776\n"
    "v_add_f32 v177, v177, v93\n"
    "v_mfma_f32_16x16x32_bf16 v[116:119], a[128:131], a[204:207], v[116:119]\n"
    "ds_read_b64_tr_b16 a[244:245], v17 offset:11264\n"
    "v_add_f32 v178, v178, v94\n"
    "v_mfma_f32_16x16x32_bf16 v[116:119], a[132:135], a[208:211], v[116:119]\n"
    "ds_read_b64_tr_b16 a[246:247], v17 offset:11776\n"
    "v_add_f32 v179, v179, v95\n"
    "v_mfma_f32_16x16x32_bf16 v[116:119], a[136:139], a[212:215], v[116:119]\n"
    "s_waitcnt lgkmcnt(4)\n"
    "v_cvt_pk_bf16_f32 v78, v64, v65\n"
    "v_cvt_pk_bf16_f32 v79, v66, v67\n"
    "v_mfma_f32_16x16x32_bf16 v[72:75], a[252:255], v[52:55], 0\n"
    "v_mfma_f32_16x16x32_bf16 v[68:71], a[248:251], v[52:55], 0\n"
    "v_mfma_f32_16x16x32_bf16 v[128:131], a[136:139], a[216:219], v[128:131]\n"
    "buffer_load_dwordx4 a[44:47], v14, s[36:39], s68 offen offset:3072 sc1\n"
    "v_mfma_f32_16x16x32_bf16 v[128:131], a[132:135], a[212:215], v[128:131]\n"
    "v_cvt_pk_bf16_f32 v76, v60, v61\n"
    "v_cvt_pk_bf16_f32 v77, v62, v63\n"
    "v_mfma_f32_16x16x32_bf16 v[128:131], a[128:131], a[208:211], v[128:131]\n"
    "s_mul_i32 s57, s53, 384\n"
    "s_add_u32 s57, s57, s75\n"
    "v_add_u32 v20, s57, v20\n"
    "v_mfma_f32_16x16x32_bf16 v[128:131], a[124:127], a[204:207], v[128:131]\n"
    "v_add_u32 v21, s57, v21\n"
    "v_add_u32 v22, s57, v22\n"
    "v_mfma_f32_16x16x32_bf16 v[128:131], a[120:123], a[200:203], v[128:131]\n"
    "v_add_u32 v23, s57, v23\n"
    "v_add_u32 v24, s57, v24\n"
    "v_mfma_f32_16x16x32_bf16 v[128:131], a[116:119], a[196:199], v[128:131]\n"
    "s_addk_i32 s68, 0x1000\n"
    "v_add_u32 v25, s57, v25\n"
    "v_add_u32 v26, s57, v26\n"
    "v_mfma_f32_16x16x32_bf16 v[128:131], a[112:115], a[192:195], v[128:131]\n"
    "buffer_load_dwordx4 a[48:51], v14, s[36:39], s68 offen offset:0 sc1\n"
    "v_mfma_f32_16x16x32_bf16 v[140:143], a[112:115], a[196:199], v[140:143]\n"
    "ds_write_b32 v12, v78 offset:8200\n"
    "v_mfma_f32_16x16x32_bf16 v[140:143], a[116:119], a[200:203], v[140:143]\n"
    "v_cvt_pk_bf16_f32 v82, v72, v73\n"
    "v_cvt_pk_bf16_f32 v83, v74, v75\n"
    "v_mfma_f32_16x16x32_bf16 v[140:143], a[120:123], a[204:207], v[140:143]\n"
    "ds_write_b32 v12, v79 offset:8204\n"
    "v_mfma_f32_16x16x32_bf16 v[140:143], a[124:127], a[208:211], v[140:143]\n"
    "v_cvt_pk_bf16_f32 v80, v68, v69\n"
    "v_cvt_pk_bf16_f32 v81, v70, v71\n"
    "v_mfma_f32_16x16x32_bf16 v[140:143], a[128:131], a[212:215], v[140:143]\n"
    "buffer_load_dwordx4 a[52:55], v14, s[36:39], s68 offen offset:1024 sc1\n"
    "v_mfma_f32_16x16x32_bf16 v[140:143], a[132:135], a[216:219], v[140:143]\n"
    "ds_write_b32 v12, v76 offset:8192\n"
    "v_mfma_f32_16x16x32_bf16 v[140:143], a[136:139], a[220:223], v[140:143]\n"
    "ds_write_b32 v12, v82 offset:12296\n"
    "v_mfma_f32_16x16x32_bf16 v[152:155], a[136:139], a[224:227], v[152:155]\n"
    "ds_write_b32 v12, v77 offset:8196\n"
    "v_mfma_f32_16x16x32_bf16 v[152:155], a[132:135], a[220:223], v[152:155]\n"
    "ds_write_b32 v12, v83 offset:12300\n"
    "v_mfma_f32_16x16x32_bf16 v[152:155], a[128:131], a[216:219], v[152:155]\n"
    "s_add_u32 m0, s69, 14336\n"
    "v_add_f32 v188, v188, v104\n"
    "v_add_f32 v189, v189, v105\n"
    "v_mfma_f32_16x16x32_bf16 v[152:155], a[124:127], a[212:215], v[152:155]\n"
    "buffer_load_dwordx4 v20, s[20:23], s60 offen lds\n"
    "v_mfma_f32_16x16x32_bf16 v[152:155], a[120:123], a[208:211], v[152:155]\n"
    "ds_write_b32 v12, v80 offset:12288\n"
    "v_mfma_f32_16x16x32_bf16 v[152:155], a[116:119], a[204:207], v[152:155]\n"
    "s_waitcnt lgkmcnt(5)\n"
    "v_add_u32 v37, 1, v37\n"
    "v_add_f32 v190, v190, v106\n"
    "v_mfma_f32_16x16x32_bf16 v[152:155], a[112:115], a[200:203], v[152:155]\n"
    "ds_write_b32 v12, v81 offset:12292\n"
    "v_mfma_f32_16x16x32_bf16 v[64:67], a[244:247], v[52:55], 0\n"
    "v_mfma_f32_16x16x32_bf16 v[60:63], a[240:243], v[52:55], 0\n"
    "v_mfma_f32_16x16x32_bf16 v[164:167], a[112:115], a[204:207], v[164:167]\n"
    "v_add_f32 v191, v191, v107\n"
    "v_add_f32 v200, v200, v116\n"
    "v_mfma_f32_16x16x32_bf16 v[164:167], a[116:119], a[208:211], v[164:167]\n"
    "v_add_f32 v201, v201, v117\n"
    "v_add_f32 v202, v202, v118\n"
    "v_mfma_f32_16x16x32_bf16 v[164:167], a[120:123], a[212:215], v[164:167]\n"
    "v_add_f32 v203, v203, v119\n"
    "v_add_f32 v212, v212, v128\n"
    "v_mfma_f32_16x16x32_bf16 v[164:167], a[124:127], a[216:219], v[164:167]\n"
    "s_add_u32 m0, s69, 15360\n"
    "v_add_f32 v213, v213, v129\n"
    "v_add_f32 v214, v214, v130\n"
    "v_mfma_f32_16x16x32_bf16 v[164:167], a[128:131], a[220:223], v[164:167]\n"
    "buffer_load_dwordx4 v21, s[20:23], s60 offen lds\n"
    "v_mfma_f32_16x16x32_bf16 v[164:167], a[132:135], a[224:227], v[164:167]\n"
    "s_waitcnt vmcnt(16)\n"
    "v_add_f32 v215, v215, v131\n"
    "v_add_f32 v224, v224, v140\n"
    "v_mfma_f32_16x16x32_bf16 v[164:167], a[136:139], a[228:231], v[164:167]\n"
    "v_cvt_pk_bf16_f32 v78, v64, v65\n"
    "v_cvt_pk_bf16_f32 v79, v66, v67\n"
    "v_mfma_f32_16x16x32_bf16 v[96:99], a[140:143], a[180:183], v[96:99]\n"
    "v_cvt_pk_bf16_f32 v76, v60, v61\n"
    "v_cvt_pk_bf16_f32 v77, v62, v63\n"
    "v_mfma_f32_16x16x32_bf16 v[96:99], a[144:147], a[184:187], v[96:99]\n"
    "ds_write_b32 v12, v78 offset:16392\n"
    "v_mfma_f32_16x16x32_bf16 v[96:99], a[148:151], a[188:191], v[96:99]\n"
    "v_add_f32 v225, v225, v141\n"
    "v_add_f32 v226, v226, v142\n"
    "v_mfma_f32_16x16x32_bf16 v[96:99], a[152:155], a[192:195], v[96:99]\n"
    "ds_write_b32 v12, v79 offset:16396\n"
    "v_mfma_f32_16x16x32_bf16 v[96:99], a[156:159], a[196:199], v[96:99]\n"
    "buffer_load_dwordx4 a[56:59], v14, s[36:39], s68 offen offset:2048 sc1\n"
    "v_mfma_f32_16x16x32_bf16 v[96:99], a[160:163], a[200:203], v[96:99]\n"
    "ds_write_b32 v12, v76 offset:16384\n"
    "v_mfma_f32_16x16x32_bf16 v[96:99], a[164:167], a[204:207], v[96:99]\n"
    "v_add_f32 v227, v227, v143\n"
    "v_add_f32 v236, v236, v152\n"
    "v_mfma_f32_16x16x32_bf16 v[108:111], a[164:167], a[208:211], v[108:111]\n"
    "ds_write_b32 v12, v77 offset:16388\n"
    "v_mfma_f32_16x16x32_bf16 v[108:111], a[160:163], a[204:207], v[108:111]\n"
    "ds_write_b32 v35, v37\n"
    "v_mfma_f32_16x16x32_bf16 v[108:111], a[156:159], a[200:203], v[108:111]\n"
    "buffer_load_dwordx4 a[60:63], v14, s[36:39], s68 offen offset:3072 sc1\n"
    "v_mfma_f32_16x16x32_bf16 v[108:111], a[152:155], a[196:199], v[108:111]\n"
    "v_add_f32 v237, v237, v153\n"
    "v_add_f32 v238, v238, v154\n"
    "v_mfma_f32_16x16x32_bf16 v[108:111], a[148:151], a[192:195], v[108:111]\n"
    "v_add_f32 v239, v239, v155\n"
    "v_add_f32 v248, v248, v164\n"
    "v_mfma_f32_16x16x32_bf16 v[108:111], a[144:147], a[188:191], v[108:111]\n"
    "v_add_f32 v249, v249, v165\n"
    "v_add_f32 v250, v250, v166\n"
    "v_mfma_f32_16x16x32_bf16 v[108:111], a[140:143], a[184:187], v[108:111]\n"
    "v_add_f32 v251, v251, v167\n"
    "v_mfma_f32_16x16x32_bf16 v[120:123], a[140:143], a[188:191], v[120:123]\n"
    "s_addk_i32 s68, 0x1000\n"
    "v_add_f32 v180, v180, v96\n"
    "v_add_f32 v181, v181, v97\n"
    "v_mfma_f32_16x16x32_bf16 v[120:123], a[144:147], a[192:195], v[120:123]\n"
    "buffer_load_dwordx4 a[64:67], v14, s[36:39], s68 offen offset:0 sc1\n"
    "v_mfma_f32_16x16x32_bf16 v[120:123], a[148:151], a[196:199], v[120:123]\n"
    "ds_read_b32 v38, v36 offset:30592\n"
    "v_add_f32 v182, v182, v98\n"
    "v_mfma_f32_16x16x32_bf16 v[120:123], a[152:155], a[200:203], v[120:123]\n"
    "v_add_f32 v183, v183, v99\n"
    "v_mfma_f32_16x16x32_bf16 v[120:123], a[156:159], a[204:207], v[120:123]\n"
    "v_mfma_f32_16x16x32_bf16 v[120:123], a[160:163], a[208:211], v[120:123]\n"
    "v_mfma_f32_16x16x32_bf16 v[120:123], a[164:167], a[212:215], v[120:123]\n"
    "buffer_load_dwordx4 a[68:71], v14, s[36:39], s68 offen offset:1024 sc1\n"
    "v_mfma_f32_16x16x32_bf16 v[132:135], a[164:167], a[216:219], v[132:135]\n"
    "s_waitcnt lgkmcnt(0)\n"
    "v_cmp_gt_u32 vcc, 4, v38\n"
    "v_add_f32 v192, v192, v108\n"
    "v_mfma_f32_16x16x32_bf16 v[132:135], a[160:163], a[212:215], v[132:135]\n"
    "v_add_f32 v193, v193, v109\n"
    "v_add_f32 v194, v194, v110\n"
    "v_mfma_f32_16x16x32_bf16 v[132:135], a[156:159], a[208:211], v[132:135]\n"
    "s_cbranch_vccnz .Lsl4\n"
    ".Lbk4:\n"
    "v_mfma_f32_16x16x32_bf16 v[132:135], a[152:155], a[204:207], v[132:135]\n"
    "ds_read_b128 a[232:235], v13 offset:0\n"
    "v_add_f32 v195, v195, v111\n"
    "v_mfma_f32_16x16x32_bf16 v[132:135], a[148:151], a[200:203], v[132:135]\n"
    "buffer_load_dwordx4 a[72:75], v14, s[36:39], s68 offen offset:2048 sc1\n"
    "v_mfma_f32_16x16x32_bf16 v[132:135], a[144:147], a[196:199], v[132:135]\n"
    "ds_read_b128 a[236:239], v13 offset:1024\n"
    "v_mfma_f32_16x16x32_bf16 v[132:135], a[140:143], a[192:195], v[132:135]\n"
    "ds_read_b128 a[168:171], v13 offset:2048\n"
    "v_mfma_f32_16x16x32_bf16 v[144:147], a[140:143], a[196:199], v[144:147]\n"
    "ds_read_b128 a[172:175], v13 offset:3072\n"
    "v_add_f32 v204, v204, v120\n"
    "v_mfma_f32_16x16x32_bf16 v[144:147], a[144:147], a[200:203], v[144:147]\n"
    "ds_read_b128 a[176:179], v13 offset:4096\n"
    "v_add_f32 v205, v205, v121\n"
    "v_mfma_f32_16x16x32_bf16 v[144:147], a[148:151], a[204:207], v[144:147]\n"
    "buffer_load_dwordx4 a[76:79], v14, s[36:39], s68 offen offset:3072 sc1\n"
    "v_mfma_f32_16x16x32_bf16 v[144:147], a[152:155], a[208:211], v[144:147]\n"
    "ds_read_b128 a[180:183], v13 offset:5120\n"
    "v_add_f32 v206, v206, v122\n"
    "v_mfma_f32_16x16x32_bf16 v[144:147], a[156:159], a[212:215], v[144:147]\n"
    "ds_read_b128 a[184:187], v13 offset:6144\n"
    "v_add_f32 v207, v207, v123\n"
    "v_mfma_f32_16x16x32_bf16 v[144:147], a[160:163], a[216:219], v[144:147]\n"
    "ds_read_b128 a[188:191], v13 offset:7168\n"
    "v_mfma_f32_16x16x32_bf16 v[144:147], a[164:167], a[220:223], v[144:147]\n"
    "ds_read_b128 a[192:195], v13 offset:8192\n"
    "v_mfma_f32_16x16x32_bf16 v[156:159], a[164:167], a[224:227], v[156:159]\n"
    "s_addk_i32 s68, 0x1000\n"
    "ds_read_b128 a[196:199], v13 offset:9216\n"
    "v_add_f32 v216, v216, v132\n"
    "v_mfma_f32_16x16x32_bf16 v[156:159], a[160:163], a[220:223], v[156:159]\n"
    "buffer_load_dwordx4 a[80:83], v14, s[36:39], s68 offen offset:0 sc1\n"
    "v_mfma_f32_16x16x32_bf16 v[156:159], a[156:159], a[216:219], v[156:159]\n"
    "v_add_f32 v217, v217, v133\n"
    "v_add_f32 v218, v218, v134\n"
    "v_mfma_f32_16x16x32_bf16 v[156:159], a[152:155], a[212:215], v[156:159]\n"
    "v_add_f32 v219, v219, v135\n"
    "v_mfma_f32_16x16x32_bf16 v[156:159], a[148:151], a[208:211], v[156:159]\n"
    "v_mfma_f32_16x16x32_bf16 v[156:159], a[144:147], a[204:207], v[156:159]\n"
    "v_mfma_f32_16x16x32_bf16 v[156:159], a[140:143], a[200:203], v[156:159]\n"
    "s_add_u32 m0, s69, 16384\n"
    "ds_read_b128 a[200:203], v13 offset:10240\n"
    "v_mfma_f32_16x16x32_bf16 v[168:171], a[140:143], a[204:207], v[168:171]\n"
    "buffer_load_dwordx4 v22, s[20:23], s60 offen lds\n"
    "v_mfma_f32_16x16x32_bf16 v[168:171], a[144:147], a[208:211], v[168:171]\n"
    "ds_read_b128 a[204:207], v13 offset:11264\n"
    "v_add_f32 v228, v228, v144\n"
    "v_mfma_f32_16x16x32_bf16 v[168:171], a[148:151], a[212:215], v[168:171]\n"
    "ds_read_b128 a[208:211], v13 offset:12288\n"
    "v_add_f32 v229, v229, v145\n"
    "v_mfma_f32_16x16x32_bf16 v[168:171], a[152:155], a[216:219], v[168:171]\n"
    "v_add_f32 v230, v230, v146\n"
    "v_add_f32 v231, v231, v147\n"
    "v_mfma_f32_16x16x32_bf16 v[168:171], a[156:159], a[220:223], v[168:171]\n"
    "v_mfma_f32_16x16x32_bf16 v[168:171], a[160:163], a[224:227], v[168:171]\n"
    "v_mfma_f32_16x16x32_bf16 v[168:171], a[164:167], a[228:231], v[168:171]\n"
    "s_waitcnt lgkmcnt(3)\n"
    "s_waitcnt vmcnt(17)\n"
    "v_mfma_f32_16x16x32_bf16 v[88:91], a[0:3], a[232:235], v[88:91]\n"
    "s_add_u32 m0, s69, 17408\n"
    "v_add_f32 v240, v240, v156\n"
    "v_add_f32 v241, v241, v157\n"
    "v_mfma_f32_16x16x32_bf16 v[88:91], a[4:7], a[236:239], v[88:91]\n"
    "buffer_load_dwordx4 v23, s[20:23], s60 offen lds\n"
    "v_mfma_f32_16x16x32_bf16 v[88:91], a[8:11], a[168:171], v[88:91]\n"
    "v_add_f32 v242, v242, v158\n"
    "v_add_f32 v243, v243, v159\n"
    "v_mfma_f32_16x16x32_bf16 v[88:91], a[12:15], a[172:175], v[88:91]\n"
    "v_mfma_f32_16x16x32_bf16 v[88:91], a[16:19], a[176:179], v[88:91]\n"
    "v_mfma_f32_16x16x32_bf16 v[88:91], a[20:23], a[180:183], v[88:91]\n"
    "v_mfma_f32_16x16x32_bf16 v[88:91], a[24:27], a[184:187], v[88:91]\n"
    "v_mfma_f32_16x16x32_bf16 v[100:103], a[24:27], a[188:191], v[100:103]\n"
    "v_add_f32 v252, v252, v168\n"
    "v_add_f32 v253, v253, v169\n"
    "v_mfma_f32_16x16x32_bf16 v[100:103], a[20:23], a[184:187], v[100:103]\n"
    "global_load_dwordx4 v[56:59], v14, %[tab] offset:3072\n"
    "v_mfma_f32_16x16x32_bf16 v[100:103], a[16:19], a[180:183], v[100:103]\n"
    "ds_read_b64_tr_b16 a[240:241], v18 offset:0\n"
    "v_add_f32 v254, v254, v170\n"
    "v_mfma_f32_16x16x32_bf16 v[100:103], a[12:15], a[176:179], v[100:103]\n"
    "ds_read_b64_tr_b16 a[242:243], v18 offset:512\n"
    "v_add_f32 v255, v255, v171\n"
    "v_mfma_f32_16x16x32_bf16 v[100:103], a[8:11], a[172:175], v[100:103]\n"
    "ds_read_b64_tr_b16 a[244:245], v19 offset:0\n"
    "v_mfma_f32_16x16x32_bf16 v[100:103], a[4:7], a[168:171], v[100:103]\n"
    "ds_read_b64_tr_b16 a[246:247], v19 offset:512\n"
    "v_mfma_f32_16x16x32_bf16 v[100:103], a[0:3], a[236:239], v[100:103]\n"
    "s_add_u32 s68, s63, 258048\n"
    "v_mfma_f32_16x16x32_bf16 v[112:115], a[0:3], a[168:171], v[112:115]\n"
    "buffer_load_dwordx4 a[84:87], v14, s[36:39], s68 offen offset:0 sc1\n"
    "v_mfma_f32_16x16x32_bf16 v[112:115], a[4:7], a[172:175], v[112:115]\n"
    "v_mfma_f32_16x16x32_bf16 v[112:115], a[8:11], a[176:179], v[112:115]\n"
    "v_mfma_f32_16x16x32_bf16 v[112:115], a[12:15], a[180:183], v[112:115]\n"
    "v_mfma_f32_16x16x32_bf16 v[112:115], a[16:19], a[184:187], v[112:115]\n"
    "v_mfma_f32_16x16x32_bf16 v[112:115], a[20:23], a[188:191], v[112:115]\n"
    "buffer_load_dwordx4 a[88:91], v14, s[36:39], s68 offen offset:1024 sc1\n"
    "v_mfma_f32_16x16x32_bf16 v[112:115], a[24:27], a[192:195], v[112:115]\n"
    "s_waitcnt lgkmcnt(0)\n"
    "v_mfma_f32_16x16x32_bf16 v[64:67], a[244:247], v[52:55], 0\n"
    ".Lpad2:\n"
    "s_cbranch_execz .Lpad6\n"
    "v_mfma_f32_16x16x32_bf16 v[60:63], a[240:243], v[52:55], 0\n"
    "v_mfma_f32_16x16x32_bf16 v[124:127], a[24:27], a[196:199], v[124:127]\n"
    "v_mfma_f32_16x16x32_bf16 v[124:127], a[20:23], a[192:195], v[124:127]\n"
    "ds_read_b64_tr_b16 a[248:249], v18 offset:2816\n"
    "v_mfma_f32_16x16x32_bf16 v[124:127], a[16:19], a[188:191], v[124:127]\n"
    "ds_read_b64_tr_b16 a[250:251], v18 offset:3328\n"
    "v_mfma_f32_16x16x32_bf16 v[124:127], a[12:15], a[184:187], v[124:127]\n"
    "buffer_load_dwordx4 a[92:95], v14, s[36:39], s68 offen offset:2048 sc1\n"
    "v_mfma_f32_16x16x32_bf16 v[124:127], a[8:11], a[180:183], v[124:127]\n"
    "ds_read_b64_tr_b16 a[252:253], v19 offset:2816\n"
    "v_mfma_f32_16x16x32_bf16 v[124:127], a[4:7], a[176:179], v[124:127]\n"
    "ds_read_b64_tr_b16 a[254:255], v19 offset:3328\n"
    "v_mfma_f32_16x16x32_bf16 v[124:127], a[0:3], a[172:175], v[124:127]\n"
    "v_cvt_pk_bf16_f32 v78, v64, v65\n"
    "v_cvt_pk_bf16_f32 v79, v66, v67\n"
    "v_mfma_f32_16x16x32_bf16 v[136:139], a[0:3], a[176:179], v[136:139]\n"
    "v_cvt_pk_bf16_f32 v76, v60, v61\n"
    "v_cvt_pk_bf16_f32 v77, v62, v63\n"
    "v_mfma_f32_16x16x32_bf16 v[136:139], a[4:7], a[180:183], v[136:139]\n"
    "buffer_load_dwordx4 a[96:99], v14, s[36:39], s68 offen offset:3072 sc1\n"
    "v_mfma_f32_16x16x32_bf16 v[136:139], a[8:11], a[184:187], v[136:139]\n"
    "ds_write_b32 v12, v78 offset:20488\n"
    "v_mfma_f32_16x16x32_bf16 v[136:139], a[12:15], a[188:191], v[136:139]\n"
    "v_mfma_f32_16x16x32_bf16 v[136:139], a[16:19], a[192:195], v[136:139]\n"
    "ds_write_b32 v12, v79 offset:20492\n"
    "v_mfma_f32_16x16x32_bf16 v[136:139], a[20:23], a[196:199], v[136:139]\n"
    "s_waitcnt lgkmcnt(2)\n"
    "v_mfma_f32_16x16x32_bf16 v[136:139], a[24:27], a[200:203], v[136:139]\n"
    "s_addk_i32 s68, 0x1000\n"
    "ds_write_b32 v12, v76 offset:20480\n"
    "v_mfma_f32_16x16x32_bf16 v[72:75], a[252:255], v[52:55], 0\n"
    "v_mfma_f32_16x16x32_bf16 v[68:71], a[248:251], v[52:55], 0\n"
    "v_mfma_f32_16x16x32_bf16 v[148:151], a[24:27], a[204:207], v[148:151]\n"
    "buffer_load_dwordx4 a[100:103], v14, s[36:39], s68 offen offset:0 sc1\n"
    "v_mfma_f32_16x16x32_bf16 v[148:151], a[20:23], a[200:203], v[148:151]\n"
    "ds_write_b32 v12, v77 offset:20484\n"
    "v_mfma_f32_16x16x32_bf16 v[148:151], a[16:19], a[196:199], v[148:151]\n"
    "ds_read_b64_tr_b16 a[240:241], v18 offset:5632\n"
    "v_mfma_f32_16x16x32_bf16 v[148:151], a[12:15], a[192:195], v[148:151]\n"
    "ds_read_b64_tr_b16 a[242:243], v18 offset:6144\n"
    "v_mfma_f32_16x16x32_bf16 v[148:151], a[8:11], a[188:191], v[148:151]\n"
    "ds_read_b64_tr_b16 a[244:245], v19 offset:5632\n"
    "v_mfma_f32_16x16x32_bf16 v[148:151], a[4:7], a[184:187], v[148:151]\n"
    "buffer_load_dwordx4 a[104:107], v14, s[36:39], s68 offen offset:1024 sc1\n"
    "v_mfma_f32_16x16x32_bf16 v[148:151], a[0:3], a[180:183], v[148:151]\n"
    "v_cvt_pk_bf16_f32 v82, v72, v73\n"
    "v_cvt_pk_bf16_f32 v83, v74, v75\n"
    "v_mfma_f32_16x16x32_bf16 v[160:163], a[0:3], a[184:187], v[160:163]\n"
    "v_cvt_pk_bf16_f32 v80, v68, v69\n"
    "v_cvt_pk_bf16_f32 v81, v70, v71\n"
    "v_mfma_f32_16x16x32_bf16 v[160:163], a[4:7], a[188:191], v[160:163]\n"
    "ds_write_b32 v12, v82 offset:24584\n"
    "v_mfma_f32_16x16x32_bf16 v[160:163], a[8:11], a[192:195], v[160:163]\n"
    "ds_read_b64_tr_b16 a[246:247], v19 offset:6144\n"
    "v_mfma_f32_16x16x32_bf16 v[160:163], a[12:15], a[196:199], v[160:163]\n"
    "buffer_load_dwordx4 a[108:111], v14, s[36:39], s68 offen offset:2048 sc1\n"
    "v_mfma_f32_16x16x32_bf16 v[160:163], a[16:19], a[200:203], v[160:163]\n"
    "ds_write_b32 v12, v83 offset:24588\n"
    "v_mfma_f32_16x16x32_bf16 v[160:163], a[20:23], a[204:207], v[160:163]\n"
    "s_waitcnt vmcnt(19)\n"
    "v_mfma_f32_16x16x32_bf16 v[160:163], a[24:27], a[208:211], v[160:163]\n"
    "ds_write_b32 v12, v80 offset:24576\n"
    "v_mfma_f32_16x16x32_bf16 v[92:95], a[28:31], a[232:235], v[92:95]\n"
    "v_mfma_f32_16x16x32_bf16 v[92:95], a[32:35], a[236:239], v[92:95]\n"
    "s_add_u32 m0, s69, 18432\n"
    "ds_write_b32 v12, v81 offset:24580\n"
    "v_mfma_f32_16x16x32_bf16 v[92:95], a[36:39], a[168:171], v[92:95]\n"
    "buffer_load_dwordx4 v24, s[20:23], s60 offen lds\n"
    "v_mfma_f32_16x16x32_bf16 v[92:95], a[40:43], a[172:175], v[92:95]\n"
    "ds_read_b64_tr_b16 a[248:249], v18 offset:8448\n"
    "v_mfma_f32_16x16x32_bf16 v[92:95], a[44:47], a[176:179], v[92:95]\n"
    "ds_read_b64_tr_b16 a[250:251], v18 offset:8960\n"
    "v_mfma_f32_16x16x32_bf16 v[92:95], a[48:51], a[180:183], v[92:95]\n"
    "ds_read_b64_tr_b16 a[252:253], v19 offset:8448\n"
    "v_mfma_f32_16x16x32_bf16 v[92:95], a[52:55], a[184:187], v[92:95]\n"
    "s_waitcnt lgkmcnt(5)\n"
    "ds_read_b64_tr_b16 a[254:255], v19 offset:8960\n"
    "v_mfma_f32_16x16x32_bf16 v[64:67], a[244:247], v[52:55], 0\n"
    "v_mfma_f32_16x16x32_bf16 v[60:63], a[240:243], v[52:55], 0\n"
    "v_mfma_f32_16x16x32_bf16 v[104:107], a[52:55], a[188:191], v[104:107]\n"
    "v_mfma_f32_16x16x32_bf16 v[104:107], a[48:51], a[184:187], v[104:107]\n"
    "v_mfma_f32_16x16x32_bf16 v[104:107], a[44:47], a[180:183], v[104:107]\n"
    "s_add_u32 m0, s69, 19456\n"
    "v_mfma_f32_16x16x32_bf16 v[104:107], a[40:43], a[176:179], v[104:107]\n"
    "buffer_load_dwordx4 v25, s[20:23], s60 offen lds\n"
    "v_mfma_f32_16x16x32_bf16 v[104:107], a[36:39], a[172:175], v[104:107]\n"
    "v_mfma_f32_16x16x32_bf16 v[104:107], a[32:35], a[168:171], v[104:107]\n"
    "v_mfma_f32_16x16x32_bf16 v[104:107], a[28:31], a[236:239], v[104:107]\n"
    "v_cvt_pk_bf16_f32 v78, v64, v65\n"
    "v_cvt_pk_bf16_f32 v79, v66, v67\n"
    "v_mfma_f32_16x16x32_bf16 v[116:119], a[28:31], a[168:171], v[116:119]\n"
    "v_cvt_pk_bf16_f32 v76, v60, v61\n"
    "v_cvt_pk_bf16_f32 v77, v62, v63\n"
    "v_mfma_f32_16x16x32_bf16 v[116:119], a[32:35], a[172:175], v[116:119]\n"
    "ds_write_b32 v12, v78 offset:28680\n"
    "v_mfma_f32_16x16x32_bf16 v[116:119], a[36:39], a[176:179], v[116:119]\n"
    "ds_read_b64_tr_b16 a[240:241], v18 offset:11264\n"
    "v_mfma_f32_16x16x32_bf16 v[116:119], a[40:43], a[180:183], v[116:119]\n"
    "buffer_load_dwordx4 a[112:115], v14, s[36:39], s68 offen offset:3072 sc1\n"
    "v_mfma_f32_16x16x32_bf16 v[116:119], a[44:47], a[184:187], v[116:119]\n"
    "ds_write_b32 v12, v79 offset:28684\n"
    "v_mfma_f32_16x16x32_bf16 v[116:119], a[48:51], a[188:191], v[116:119]\n"
    "s_waitcnt lgkmcnt(3)\n"
    "ds_read_b64_tr_b16 a[242:243], v18 offset:11776\n"
    "v_mfma_f32_16x16x32_bf16 v[116:119], a[52:55], a[192:195], v[116:119]\n"
    "ds_write_b32 v12, v76 offset:28672\n"
    "v_mfma_f32_16x16x32_bf16 v[72:75], a[252:255], v[52:55], 0\n"
    "v_mfma_f32_16x16x32_bf16 v[68:71], a[248:251], v[52:55], 0\n"
    "v_mfma_f32_16x16x32_bf16 v[128:131], a[52:55], a[196:199], v[128:131]\n"
    "ds_read_b64_tr_b16 a[244:245], v19 offset:11264\n"
    "v_mfma_f32_16x16x32_bf16 v[128:131], a[48:51], a[192:195], v[128:131]\n"
    "s_addk_i32 s68, 0x1000\n"
    "ds_write_b32 v12, v77 offset:28676\n"
    "v_mfma_f32_16x16x32_bf16 v[128:131], a[44:47], a[188:191], v[128:131]\n"
    "buffer_load_dwordx4 a[116:119], v14, s[36:39], s68 offen offset:0 sc1\n"
    "v_mfma_f32_16x16x32_bf16 v[128:131], a[40:43], a[184:187], v[128:131]\n"
    "ds_read_b64_tr_b16 a[246:247], v19 offset:11776\n"
    "v_mfma_f32_16x16x32_bf16 v[128:131], a[36:39], a[180:183], v[128:131]\n"
    "v_mfma_f32_16x16x32_bf16 v[128:131], a[32:35], a[176:179], v[128:131]\n"
    "v_mfma_f32_16x16x32_bf16 v[128:131], a[28:31], a[172:175], v[128:131]\n"
    "v_cvt_pk_bf16_f32 v82, v72, v73\n"
    "v_cvt_pk_bf16_f32 v83, v74, v75\n"
    "v_mfma_f32_16x16x32_bf16 v[140:143], a[28:31], a[176:179], v[140:143]\n"
    "buffer_load_dwordx4 a[120:123], v14, s[36:39], s68 offen offset:1024 sc1\n"
    "v_mfma_f32_16x16x32_bf16 v[140:143], a[32:35], a[180:183], v[140:143]\n"
    "v_cvt_pk_bf16_f32 v80, v68, v69\n"
    "v_cvt_pk_bf16_f32 v81, v70, v71\n"
    "v_mfma_f32_16x16x32_bf16 v[140:143], a[36:39], a[184:187], v[140:143]\n"
    "ds_write_b32 v12, v82 offset:32776\n"
    "v_mfma_f32_16x16x32_bf16 v[140:143], a[40:43], a[188:191], v[140:143]\n"
    "s_waitcnt lgkmcnt(1)\n"
    "v_mfma_f32_16x16x32_bf16 v[140:143], a[44:47], a[192:195], v[140:143]\n"
    "ds_write_b32 v12, v83 offset:32780\n"
    "v_mfma_f32_16x16x32_bf16 v[140:143], a[48:51], a[196:199], v[140:143]\n"
    "buffer_load_dwordx4 a[124:127], v14, s[36:39], s68 offen offset:2048 sc1\n"
    "v_mfma_f32_16x16x32_bf16 v[140:143], a[52:55], a[200:203], v[140:143]\n"
    "ds_write_b32 v12, v80 offset:32768\n"
    "v_mfma_f32_16x16x32_bf16 v[64:67], a[244:247], v[52:55], 0\n"
    "v_mfma_f32_16x16x32_bf16 v[60:63], a[240:243], v[52:55], 0\n"
    "v_mfma_f32_16x16x32_bf16 v[152:155], a[52:55], a[204:207], v[152:155]\n"
    "v_add_u32 v37, 1, v37\n"
    "v_mfma_f32_16x16x32_bf16 v[152:155], a[48:51], a[200:203], v[152:155]\n"
    "ds_write_b32 v12, v81 offset:32772\n"
    "v_mfma_f32_16x16x32_bf16 v[152:155], a[44:47], a[196:199], v[152:155]\n"
    "v_mfma_f32_16x16x32_bf16 v[152:155], a[40:43], a[192:195], v[152:155]\n"
    "buffer_load_dwordx4 a[128:131], v14, s[36:39], s68 offen offset:3072 sc1\n"
    "v_mfma_f32_16x16x32_bf16 v[152:155], a[36:39], a[188:191], v[152:155]\n"
    "v_mfma_f32_16x16x32_bf16 v[152:155], a[32:35], a[184:187], v[152:155]\n"
    "v_mfma_f32_16x16x32_bf16 v[152:155], a[28:31], a[180:183], v[152:155]\n"
    "v_cvt_pk_bf16_f32 v78, v64, v65\n"
    "v_cvt_pk_bf16_f32 v79, v66, v67\n"
    "v_mfma_f32_16x16x32_bf16 v[164:167], a[28:31], a[184:187], v[164:167]\n"
    "v_cvt_pk_bf16_f32 v76, v60, v61\n"
    "v_cvt_pk_bf16_f32 v77, v62, v63\n"
    "v_mfma_f32_16x16x32_bf16 v[164:167], a[32:35], a[188:191], v[164:167]\n"
    "s_addk_i32 s68, 0x1000\n"
    "ds_write_b32 v12, v78 offset:36872\n"
    "v_mfma_f32_16x16x32_bf16 v[164:167], a[36:39], a[192:195], v[164:167]\n"
    "buffer_load_dwordx4 a[132:135], v14, s[36:39], s68 offen offset:0 sc1\n"
    "v_mfma_f32_16x16x32_bf16 v[164:167], a[40:43], a[196:199], v[164:167]\n"
    "ds_write_b32 v12, v79 offset:36876\n"
    "v_mfma_f32_16x16x32_bf16 v[164:167], a[44:47], a[200:203], v[164:167]\n"
    "v_mfma_f32_16x16x32_bf16 v[164:167], a[48:51], a[204:207], v[164:167]\n"
    "ds_write_b32 v12, v76 offset:36864\n"
    "v_mfma_f32_16x16x32_bf16 v[164:167], a[52:55], a[208:211], v[164:167]\n"
    "s_waitcnt vmcnt(18)\n"
    "v_mfma_f32_16x16x32_bf16 v[96:99], a[56:59], a[232:235], v[96:99]\n"
    "buffer_load_dwordx4 a[136:139], v14, s[36:39], s68 offen offset:1024 sc1\n"
    "v_mfma_f32_16x16x32_bf16 v[96:99], a[60:63], a[236:239], v[96:99]\n"
    "ds_write_b32 v12, v77 offset:36868\n"
    "v_mfma_f32_16x16x32_bf16 v[96:99], a[64:67], a[168:171], v[96:99]\n"
    "ds_write_b32 v35, v37\n"
    "v_mfma_f32_16x16x32_bf16 v[96:99], a[68:71], a[172:175], v[96:99]\n"
    "v_mfma_f32_16x16x32_bf16 v[96:99], a[72:75], a[176:179], v[96:99]\n"
    "v_mfma_f32_16x16x32_bf16 v[96:99], a[76:79], a[180:183], v[96:99]\n"
    "s_add_u32 m0, s69, 20480\n"
    "v_mfma_f32_16x16x32_bf16 v[96:99], a[80:83], a[184:187], v[96:99]\n"
    "buffer_load_dwordx4 v26, s[20:23], s60 offen lds\n"
    "v_mfma_f32_16x16x32_bf16 v[108:111], a[80:83], a[188:191], v[108:111]\n"
    "v_mfma_f32_16x16x32_bf16 v[108:111], a[76:79], a[184:187], v[108:111]\n"
    "v_mfma_f32_16x16x32_bf16 v[108:111], a[72:75], a[180:183], v[108:111]\n"
    "v_mfma_f32_16x16x32_bf16 v[108:111], a[68:71], a[176:179], v[108:111]\n"
    "ds_read_b32 v38, v36 offset:30592\n"
    "v_mfma_f32_16x16x32_bf16 v[108:111], a[64:67], a[172:175], v[108:111]\n"
    "v_mfma_f32_16x16x32_bf16 v[108:111], a[60:63], a[168:171], v[108:111]\n"
    "v_mfma_f32_16x16x32_bf16 v[108:111], a[56:59], a[236:239], v[108:111]\n"
    "buffer_load_dwordx4 a[140:143], v14, s[36:39], s68 offen offset:2048 sc1\n"
    "v_mfma_f32_16x16x32_bf16 v[120:123], a[56:59], a[168:171], v[120:123]\n"
    "v_mfma_f32_16x16x32_bf16 v[120:123], a[60:63], a[172:175], v[120:123]\n"
    "s_waitcnt lgkmcnt(0)\n"
    "v_cmp_gt_u32 vcc, 5, v38\n"
    "v_mfma_f32_16x16x32_bf16 v[120:123], a[64:67], a[176:179], v[120:123]\n"
    "v_mfma_f32_16x16x32_bf16 v[120:123], a[68:71], a[180:183], v[120:123]\n"
    "s_cbranch_vccnz .Lsl5\n"
    ".Lbk5:\n"
    "v_mfma_f32_16x16x32_bf16 v[120:123], a[72:75], a[184:187], v[120:123]\n"
    "buffer_load_dwordx4 a[144:147], v14, s[36:39], s68 offen offset:3072 sc1\n"
    "v_mfma_f32_16x16x32_bf16 v[120:123], a[76:79], a[188:191], v[120:123]\n"
    "ds_read_b128 a[212:215], v13 offset:20480\n"
    "v_mfma_f32_16x16x32_bf16 v[120:123], a[80:83], a[192:195], v[120:123]\n"
    "ds_read_b128 a[216:219], v13 offset:21504\n"
    "v_mfma_f32_16x16x32_bf16 v[132:135], a[80:83], a[196:199], v[132:135]\n"
    "ds_read_b128 a[220:223], v13 offset:22528\n"
    "v_mfma_f32_16x16x32_bf16 v[132:135], a[76:79], a[192:195], v[132:135]\n"
    "ds_read_b128 a[224:227], v13 offset:23552\n"
    "v_mfma_f32_16x16x32_bf16 v[132:135], a[72:75], a[188:191], v[132:135]\n"
    "s_addk_i32 s68, 0x1000\n"
    "ds_read_b128 a[228:231], v13 offset:24576\n"
    "v_mfma_f32_16x16x32_bf16 v[132:135], a[68:71], a[184:187], v[132:135]\n"
    "buffer_load_dwordx4 a[148:151], v14, s[36:39], s68 offen offset:0 sc1\n"
    "v_mfma_f32_16x16x32_bf16 v[132:135], a[64:67], a[180:183], v[132:135]\n"
    "ds_read_b128 a[232:235], v13 offset:25600\n"
    "v_mfma_f32_16x16x32_bf16 v[132:135], a[60:63], a[176:179], v[132:135]\n"
    "ds_read_b128 a[236:239], v13 offset:26624\n"
    "v_mfma_f32_16x16x32_bf16 v[132:135], a[56:59], a[172:175], v[132:135]\n"
    "ds_read_b128 a[168:171], v13 offset:27648\n"
    "v_mfma_f32_16x16x32_bf16 v[144:147], a[56:59], a[176:179], v[144:147]\n"
    "ds_read_b128 a[172:175], v13 offset:28672\n"
    "v_mfma_f32_16x16x32_bf16 v[144:147], a[60:63], a[180:183], v[144:147]\n"
    "buffer_load_dwordx4 a[152:155], v14, s[36:39], s68 offen offset:1024 sc1\n"
    "v_mfma_f32_16x16x32_bf16 v[144:147], a[64:67], a[184:187], v[144:147]\n"
    "ds_read_b128 a[176:179], v13 offset:29696\n"
    "v_mfma_f32_16x16x32_bf16 v[144:147], a[68:71], a[188:191], v[144:147]\n"
    "v_mfma_f32_16x16x32_bf16 v[144:147], a[72:75], a[192:195], v[144:147]\n"
    "v_mfma_f32_16x16x32_bf16 v[144:147], a[76:79], a[196:199], v[144:147]\n"
    "v_mfma_f32_16x16x32_bf16 v[144:147], a[80:83], a[200:203], v[144:147]\n"
    "buffer_load_dwordx4 a[156:159], v14, s[36:39], s68 offen offset:2048 sc1\n"
    "v_mfma_f32_16x16x32_bf16 v[156:159], a[80:83], a[204:207], v[156:159]\n"
    "v_mfma_f32_16x16x32_bf16 v[156:159], a[76:79], a[200:203], v[156:159]\n"
    "v_mfma_f32_16x16x32_bf16 v[156:159], a[72:75], a[196:199], v[156:159]\n"
    "v_mfma_f32_16x16x32_bf16 v[156:159], a[68:71], a[192:195], v[156:159]\n"
    "v_mfma_f32_16x16x32_bf16 v[156:159], a[64:67], a[188:191], v[156:159]\n"
    "buffer_load_dwordx4 a[160:163], v14, s[36:39], s68 offen offset:3072 sc1\n"
    "v_mfma_f32_16x16x32_bf16 v[156:159], a[60:63], a[184:187], v[156:159]\n"
    "v_mfma_f32_16x16x32_bf16 v[156:159], a[56:59], a[180:183], v[156:159]\n"
    "ds_read_b128 a[180:183], v13 offset:30720\n"
    "v_mfma_f32_16x16x32_bf16 v[168:171], a[56:59], a[184:187], v[168:171]\n"
    "ds_read_b128 a[184:187], v13 offset:31744\n"
    "v_mfma_f32_16x16x32_bf16 v[168:171], a[60:63], a[188:191], v[168:171]\n"
    "ds_read_b128 a[188:191], v13 offset:32768\n"
    "v_mfma_f32_16x16x32_bf16 v[168:171], a[64:67], a[192:195], v[168:171]\n"
    "s_addk_i32 s68, 0x1000\n"
    "v_mfma_f32_16x16x32_bf16 v[168:171], a[68:71], a[196:199], v[168:171]\n"
    "buffer_load_dwordx4 a[164:167], v14, s[36:39], s68 offen offset:0 sc1\n"
    "v_mfma_f32_16x16x32_bf16 v[168:171], a[72:75], a[200:203], v[168:171]\n"
    "v_mfma_f32_16x16x32_bf16 v[168:171], a[76:79], a[204:207], v[168:171]\n"
    "v_mfma_f32_16x16x32_bf16 v[168:171], a[80:83], a[208:211], v[168:171]\n"
    "s_waitcnt lgkmcnt(3)\n"
    "s_waitcnt vmcnt(17)\n"
    "v_mfma_f32_16x16x32_bf16 v[88:91], a[84:87], a[212:215], v[88:91]\n"
    "v_mfma_f32_16x16x32_bf16 v[88:91], a[88:91], a[216:219], v[88:91]\n"
    "s_add_u32 s68, s64, 258048\n"
    "v_mfma_f32_16x16x32_bf16 v[88:91], a[92:95], a[220:223], v[88:91]\n"
    "buffer_load_dwordx4 a[0:3], v14, s[36:39], s68 offen offset:0 sc1\n"
    "v_mfma_f32_16x16x32_bf16 v[88:91], a[96:99], a[224:227], v[88:91]\n"
    "v_mfma_f32_16x16x32_bf16 v[88:91], a[100:103], a[228:231], v[88:91]\n"
    "v_mfma_f32_16x16x32_bf16 v[88:91], a[104:107], a[232:235], v[88:91]\n"
    "v_mfma_f32_16x16x32_bf16 v[88:91], a[108:111], a[236:239], v[88:91]\n"
    "v_mfma_f32_16x16x32_bf16 v[100:103], a[108:111], a[168:171], v[100:103]\n"
    "buffer_load_dwordx4 a[4:7], v14, s[36:39], s68 offen offset:1024 sc1\n"
    "v_mfma_f32_16x16x32_bf16 v[100:103], a[104:107], a[236:239], v[100:103]\n"
    "ds_read_b64_tr_b16 a[240:241], v28 offset:14336\n"
    "v_mfma_f32_16x16x32_bf16 v[100:103], a[100:103], a[232:235], v[100:103]\n"
    "ds_read_b64_tr_b16 a[242:243], v28 offset:14592\n"
    "v_mfma_f32_16x16x32_bf16 v[100:103], a[96:99], a[228:231], v[100:103]\n"
    "ds_read_b64_tr_b16 a[244:245], v29 offset:14336\n"
    "v_mfma_f32_16x16x32_bf16 v[100:103], a[92:95], a[224:227], v[100:103]\n"
    "ds_read_b64_tr_b16 a[246:247], v29 offset:14592\n"
    "v_mfma_f32_16x16x32_bf16 v[100:103], a[88:91], a[220:223], v[100:103]\n"
    "buffer_load_dwordx4 a[8:11], v14, s[36:39], s68 offen offset:2048 sc1\n"
    "v_mfma_f32_16x16x32_bf16 v[100:103], a[84:87], a[216:219], v[100:103]\n"
    "v_mfma_f32_16x16x32_bf16 v[112:115], a[84:87], a[220:223], v[112:115]\n"
    "v_mfma_f32_16x16x32_bf16 v[112:115], a[88:91], a[224:227], v[112:115]\n"
    "v_mfma_f32_16x16x32_bf16 v[112:115], a[92:95], a[228:231], v[112:115]\n"
    "v_mfma_f32_16x16x32_bf16 v[112:115], a[96:99], a[232:235], v[112:115]\n"
    "buffer_load_dwordx4 a[12:15], v14, s[36:39], s68 offen offset:3072 sc1\n"
    "v_mfma_f32_16x16x32_bf16 v[112:115], a[100:103], a[236:239], v[112:115]\n"
    "v_mfma_f32_16x16x32_bf16 v[112:115], a[104:107], a[168:171], v[112:115]\n"
    "v_mfma_f32_16x16x32_bf16 v[112:115], a[108:111], a[172:175], v[112:115]\n"
    "s_waitcnt lgkmcnt(0)\n"
    "v_mfma_f32_16x16x32_bf16 v[64:67], a[244:247], v[52:55], 0\n"
    "v_mfma_f32_16x16x32_bf16 v[60:63], a[240:243], v[52:55], 0\n"
    "v_mfma_f32_16x16x32_bf16 v[124:127], a[108:111], a[176:179], v[124:127]\n"
    "v_mfma_f32_16x16x32_bf16 v[124:127], a[104:107], a[172:175], v[124:127]\n"
    "s_addk_i32 s68, 0x1000\n"
    "ds_read_b64_tr_b16 a[248:249], v28 offset:15744\n"
    "v_mfma_f32_16x16x32_bf16 v[124:127], a[100:103], a[168:171], v[124:127]\n"
    "buffer_load_dwordx4 a[16:19], v14, s[36:39], s68 offen offset:0 sc1\n"
    "v_mfma_f32_16x16x32_bf16 v[124:127], a[96:99], a[236:239], v[124:127]\n"
    "ds_read_b64_tr_b16 a[250:251], v28 offset:16000\n"
    "v_mfma_f32_16x16x32_bf16 v[124:127], a[92:95], a[232:235], v[124:127]\n"
    "ds_read_b64_tr_b16 a[252:253], v29 offset:15744\n"
    "v_mfma_f32_16x16x32_bf16 v[124:127], a[88:91], a[228:231], v[124:127]\n"
    "ds_read_b64_tr_b16 a[254:255], v29 offset:16000\n"
    "v_mfma_f32_16x16x32_bf16 v[124:127], a[84:87], a[224:227], v[124:127]\n"
    "v_cvt_pk_bf16_f32 v78, v64, v65\n"
    "v_cvt_pk_bf16_f32 v79, v66, v67\n"
    "v_mfma_f32_16x16x32_bf16 v[136:139], a[84:87], a[228:231], v[136:139]\n"
    "buffer_load_dwordx4 a[20:23], v14, s[36:39], s68 offen offset:1024 sc1\n"
    "v_mfma_f32_16x16x32_bf16 v[136:139], a[88:91], a[232:235], v[136:139]\n"
    "v_cvt_pk_bf16_f32 v76, v60, v61\n"
    "v_cvt_pk_bf16_f32 v77, v62, v63\n"
    "v_mfma_f32_16x16x32_bf16 v[136:139], a[92:95], a[236:239], v[136:139]\n"
    "ds_write_b32 v12, v78 offset:8\n"
    "v_mfma_f32_16x16x32_bf16 v[136:139], a[96:99], a[168:171], v[136:139]\n"
    "s_waitcnt lgkmcnt(1)\n"
    "v_mfma_f32_16x16x32_bf16 v[136:139], a[100:103], a[172:175], v[136:139]\n"
    "ds_write_b32 v12, v79 offset:12\n"
    "v_mfma_f32_16x16x32_bf16 v[136:139], a[104:107], a[176:179], v[136:139]\n"
    "buffer_load_dwordx4 a[24:27], v14, s[36:39], s68 offen offset:2048 sc1\n"
    "v_mfma_f32_16x16x32_bf16 v[136:139], a[108:111], a[180:183], v[136:139]\n"
    "ds_write_b32 v12, v76 offset:0\n"
    "v_mfma_f32_16x16x32_bf16 v[72:75], a[252:255], v[52:55], 0\n"
    "v_mfma_f32_16x16x32_bf16 v[68:71], a[248:251], v[52:55], 0\n"
    "v_mfma_f32_16x16x32_bf16 v[148:151], a[108:111], a[184:187], v[148:151]\n"
    "v_mfma_f32_16x16x32_bf16 v[148:151], a[104:107], a[180:183], v[148:151]\n"
    "ds_write_b32 v12, v77 offset:4\n"
    "v_mfma_f32_16x16x32_bf16 v[148:151], a[100:103], a[176:179], v[148:151]\n"
    "s_waitcnt vmcnt(23)\n"
    "ds_read_b64_tr_b16 a[240:241], v28 offset:17152\n"
    "v_mfma_f32_16x16x32_bf16 v[148:151], a[96:99], a[172:175], v[148:151]\n"
    "buffer_load_dwordx4 a[28:31], v14, s[36:39], s68 offen offset:3072 sc1\n"
    "v_mfma_f32_16x16x32_bf16 v[148:151], a[92:95], a[168:171], v[148:151]\n"
    "ds_read_b64_tr_b16 a[242:243], v28 offset:17408\n"
    "v_mfma_f32_16x16x32_bf16 v[148:151], a[88:91], a[236:239], v[148:151]\n"
    "ds_read_b64_tr_b16 a[244:245], v29 offset:17152\n"
    "v_mfma_f32_16x16x32_bf16 v[148:151], a[84:87], a[232:235], v[148:151]\n"
    "v_cvt_pk_bf16_f32 v82, v72, v73\n"
    "v_cvt_pk_bf16_f32 v83, v74, v75\n"
    "v_mfma_f32_16x16x32_bf16 v[160:163], a[84:87], a[236:239], v[160:163]\n"
    "v_cvt_pk_bf16_f32 v80, v68, v69\n"
    "v_cvt_pk_bf16_f32 v81, v70, v71\n"
    "v_mfma_f32_16x16x32_bf16 v[160:163], a[88:91], a[168:171], v[160:163]\n"
    "s_addk_i32 s68, 0x1000\n"
    "ds_write_b32 v12, v82 offset:4104\n"
    "v_mfma_f32_16x16x32_bf16 v[160:163], a[92:95], a[172:175], v[160:163]\n"
    "buffer_load_dwordx4 a[32:35], v14, s[36:39], s68 offen offset:0 sc1\n"
    "v_mfma_f32_16x16x32_bf16 v[160:163], a[96:99], a[176:179], v[160:163]\n"
    "ds_write_b32 v12, v83 offset:4108\n"
    "v_mfma_f32_16x16x32_bf16 v[160:163], a[100:103], a[180:183], v[160:163]\n"
    "ds_read_b64_tr_b16 a[246:247], v29 offset:17408\n"
    "v_mfma_f32_16x16x32_bf16 v[160:163], a[104:107], a[184:187], v[160:163]\n"
    "ds_write_b32 v12, v80 offset:4096\n"
    "v_mfma_f32_16x16x32_bf16 v[160:163], a[108:111], a[188:191], v[160:163]\n"
    "s_waitcnt vmcnt(17)\n"
    "v_mfma_f32_16x16x32_bf16 v[92:95], a[112:115], a[212:215], v[92:95]\n"
    "buffer_load_dwordx4 a[36:39], v14, s[36:39], s68 offen offset:1024 sc1\n"
    "v_mfma_f32_16x16x32_bf16 v[92:95], a[116:119], a[216:219], v[92:95]\n"
    "ds_write_b32 v12, v81 offset:4100\n"
    "v_mfma_f32_16x16x32_bf16 v[92:95], a[120:123], a[220:223], v[92:95]\n"
    "ds_read_b64_tr_b16 a[248:249], v28 offset:18560\n"
    "v_mfma_f32_16x16x32_bf16 v[92:95], a[124:127], a[224:227], v[92:95]\n"
    "ds_read_b64_tr_b16 a[250:251], v28 offset:18816\n"
    "v_mfma_f32_16x16x32_bf16 v[92:95], a[128:131], a[228:231], v[92:95]\n"
    "ds_read_b64_tr_b16 a[252:253], v29 offset:18560\n"
    "v_mfma_f32_16x16x32_bf16 v[92:95], a[132:135], a[232:235], v[92:95]\n"
    "buffer_load_dwordx4 a[40:43], v14, s[36:39], s68 offen offset:2048 sc1\n"
    "v_mfma_f32_16x16x32_bf16 v[92:95], a[136:139], a[236:239], v[92:95]\n"
    "s_waitcnt lgkmcnt(5)\n"
    "ds_read_b64_tr_b16 a[254:255], v29 offset:18816\n"
    "v_mfma_f32_16x16x32_bf16 v[64:67], a[244:247], v[52:55], 0\n"
    "v_mfma_f32_16x16x32_bf16 v[60:63], a[240:243], v[52:55], 0\n"
    "v_mfma_f32_16x16x32_bf16 v[104:107], a[136:139], a[168:171], v[104:107]\n"
    "v_mfma_f32_16x16x32_bf16 v[104:107], a[132:135], a[236:239], v[104:107]\n"
    "v_mfma_f32_16x16x32_bf16 v[104:107], a[128:131], a[232:235], v[104:107]\n"
    "v_mfma_f32_16x16x32_bf16 v[104:107], a[124:127], a[228:231], v[104:107]\n"
    "buffer_load_dwordx4 a[44:47], v14, s[36:39], s68 offen offset:3072 sc1\n"
    "v_mfma_f32_16x16x32_bf16 v[104:107], a[120:123], a[224:227], v[104:107]\n"
    "v_mfma_f32_16x16x32_bf16 v[104:107], a[116:119], a[220:223], v[104:107]\n"
    "v_mfma_f32_16x16x32_bf16 v[104:107], a[112:115], a[216:219], v[104:107]\n"
    "v_cvt_pk_bf16_f32 v78, v64, v65\n"
    "v_cvt_pk_bf16_f32 v79, v66, v67\n"
    "v_mfma_f32_16x16x32_bf16 v[116:119], a[112:115], a[220:223], v[116:119]\n"
    "v_cvt_pk_bf16_f32 v76, v60, v61\n"
    "v_cvt_pk_bf16_f32 v77, v62, v63\n"
    "v_mfma_f32_16x16x32_bf16 v[116:119], a[116:119], a[224:227], v[116:119]\n"
    "s_addk_i32 s68, 0x1000\n"
    "ds_write_b32 v12, v78 offset:8200\n"
    "v_mfma_f32_16x16x32_bf16 v[116:119], a[120:123], a[228:231], v[116:119]\n"
    "buffer_load_dwordx4 a[48:51], v14, s[36:39], s68 offen offset:0 sc1\n"
    "v_mfma_f32_16x16x32_bf16 v[116:119], a[124:127], a[232:235], v[116:119]\n"
    "ds_write_b32 v12, v79 offset:8204\n"
    "v_mfma_f32_16x16x32_bf16 v[116:119], a[128:131], a[236:239], v[116:119]\n"
    "s_waitcnt vmcnt(20)\n"
    "ds_read_b64_tr_b16 a[240:241], v28 offset:19968\n"
    "v_mfma_f32_16x16x32_bf16 v[116:119], a[132:135], a[168:171], v[116:119]\n"
    "ds_write_b32 v12, v76 offset:8192\n"
    "v_mfma_f32_16x16x32_bf16 v[116:119], a[136:139], a[172:175], v[116:119]\n"
    "s_waitcnt lgkmcnt(4)\n"
    "ds_read_b64_tr_b16 a[242:243], v28 offset:20224\n"
    "v_mfma_f32_16x16x32_bf16 v[72:75], a[252:255], v[52:55], 0\n"
    "v_mfma_f32_16x16x32_bf16 v[68:71], a[248:251], v[52:55], 0\n"
    "v_mfma_f32_16x16x32_bf16 v[128:131], a[136:139], a[176:179], v[128:131]\n"
    "buffer_load_dwordx4 a[52:55], v14, s[36:39], s68 offen offset:1024 sc1\n"
    "v_mfma_f32_16x16x32_bf16 v[128:131], a[132:135], a[172:175], v[128:131]\n"
    "ds_write_b32 v12, v77 offset:8196\n"
    "v_mfma_f32_16x16x32_bf16 v[128:131], a[128:131], a[168:171], v[128:131]\n"
    "ds_read_b64_tr_b16 a[244:245], v29 offset:19968\n"
    "v_mfma_f32_16x16x32_bf16 v[128:131], a[124:127], a[236:239], v[128:131]\n"
    "ds_read_b64_tr_b16 a[246:247], v29 offset:20224\n"
    "v_mfma_f32_16x16x32_bf16 v[128:131], a[120:123], a[232:235], v[128:131]\n"
    "v_mfma_f32_16x16x32_bf16 v[128:131], a[116:119], a[228:231], v[128:131]\n"
    "buffer_load_dwordx4 a[56:59], v14, s[36:39], s68 offen offset:2048 sc1\n"
    "v_mfma_f32_16x16x32_bf16 v[128:131], a[112:115], a[224:227], v[128:131]\n"
    "v_cvt_pk_bf16_f32 v82, v72, v73\n"
    "v_cvt_pk_bf16_f32 v83, v74, v75\n"
    "v_mfma_f32_16x16x32_bf16 v[140:143], a[112:115], a[228:231], v[140:143]\n"
    "v_cvt_pk_bf16_f32 v80, v68, v69\n"
    "v_cvt_pk_bf16_f32 v81, v70, v71\n"
    "v_mfma_f32_16x16x32_bf16 v[140:143], a[116:119], a[232:235], v[140:143]\n"
    "ds_write_b32 v12, v82 offset:12296\n"
    "v_mfma_f32_16x16x32_bf16 v[140:143], a[120:123], a[236:239], v[140:143]\n"
    "v_mfma_f32_16x16x32_bf16 v[140:143], a[124:127], a[168:171], v[140:143]\n"
    "buffer_load_dwordx4 a[60:63], v14, s[36:39], s68 offen offset:3072 sc1\n"
    "v_mfma_f32_16x16x32_bf16 v[140:143], a[128:131], a[172:175], v[140:143]\n"
    "ds_write_b32 v12, v83 offset:12300\n"
    "v_mfma_f32_16x16x32_bf16 v[140:143], a[132:135], a[176:179], v[140:143]\n"
    "s_waitcnt lgkmcnt(2)\n"
    "v_mfma_f32_16x16x32_bf16 v[140:143], a[136:139], a[180:183], v[140:143]\n"
    "ds_write_b32 v12, v80 offset:12288\n"
    "v_mfma_f32_16x16x32_bf16 v[64:67], a[244:247], v[52:55], 0\n"
    "v_mfma_f32_16x16x32_bf16 v[60:63], a[240:243], v[52:55], 0\n"
    "v_mfma_f32_16x16x32_bf16 v[152:155], a[136:139], a[184:187], v[152:155]\n"
    "v_add_u32 v37, 1, v37\n"
    "v_mfma_f32_16x16x32_bf16 v[152:155], a[132:135], a[180:183], v[152:155]\n"
    "s_addk_i32 s68, 0x1000\n"
    "ds_write_b32 v12, v81 offset:12292\n"
    "v_mfma_f32_16x16x32_bf16 v[152:155], a[128:131], a[176:179], v[152:155]\n"
    "buffer_load_dwordx4 a[64:67], v14, s[36:39], s68 offen offset:0 sc1\n"
    "v_mfma_f32_16x16x32_bf16 v[152:155], a[124:127], a[172:175], v[152:155]\n"
    "v_mfma_f32_16x16x32_bf16 v[152:155], a[120:123], a[168:171], v[152:155]\n"
    "v_mfma_f32_16x16x32_bf16 v[152:155], a[116:119], a[236:239], v[152:155]\n"
    "v_mfma_f32_16x16x32_bf16 v[152:155], a[112:115], a[232:235], v[152:155]\n"
    "v_cvt_pk_bf16_f32 v78, v64, v65\n"
    "v_cvt_pk_bf16_f32 v79, v66, v67\n"
    "v_mfma_f32_16x16x32_bf16 v[164:167], a[112:115], a[236:239], v[164:167]\n"
    "buffer_load_dwordx4 a[68:71], v14, s[36:39], s68 offen offset:1024 sc1\n"
    "v_mfma_f32_16x16x32_bf16 v[164:167], a[116:119], a[168:171], v[164:167]\n"
    "v_cvt_pk_bf16_f32 v76, v60, v61\n"
    "v_cvt_pk_bf16_f32 v77, v62, v63\n"
    "v_mfma_f32_16x16x32_bf16 v[164:167], a[120:123], a[172:175], v[164:167]\n"
    "ds_write_b32 v12, v78 offset:16392\n"
    "v_mfma_f32_16x16x32_bf16 v[164:167], a[124:127], a[176:179], v[164:167]\n"
    "s_waitcnt vmcnt(18)\n"
    "v_mfma_f32_16x16x32_bf16 v[164:167], a[128:131], a[180:183], v[164:167]\n"
    "ds_write_b32 v12, v79 offset:16396\n"
    "v_mfma_f32_16x16x32_bf16 v[164:167], a[132:135], a[184:187], v[164:167]\n"
    "buffer_load_dwordx4 a[72:75], v14, s[36:39], s68 offen offset:2048 sc1\n"
    "v_mfma_f32_16x16x32_bf16 v[164:167], a[136:139], a[188:191], v[164:167]\n"
    "ds_write_b32 v12, v76 offset:16384\n"
    "v_mfma_f32_16x16x32_bf16 v[96:99], a[140:143], a[212:215], v[96:99]\n"
    "v_mfma_f32_16x16x32_bf16 v[96:99], a[144:147], a[216:219], v[96:99]\n"
    "ds_write_b32 v12, v77 offset:16388\n"
    "v_mfma_f32_16x16x32_bf16 v[96:99], a[148:151], a[220:223], v[96:99]\n"
    "ds_write_b32 v35, v37\n"
    "v_mfma_f32_16x16x32_bf16 v[96:99], a[152:155], a[224:227], v[96:99]\n"
    "buffer_load_dwordx4 a[76:79], v14, s[36:39], s68 offen offset:3072 sc1\n"
    "v_mfma_f32_16x16x32_bf16 v[96:99], a[156:159], a[228:231], v[96:99]\n"
    "v_mfma_f32_16x16x32_bf16 v[96:99], a[160:163], a[232:235], v[96:99]\n"
    "v_mfma_f32_16x16x32_bf16 v[96:99], a[164:167], a[236:239], v[96:99]\n"
    "v_mfma_f32_16x16x32_bf16 v[108:111], a[164:167], a[168:171], v[108:111]\n"
    "v_mfma_f32_16x16x32_bf16 v[108:111], a[160:163], a[236:239], v[108:111]\n"
    "s_addk_i32 s68, 0x1000\n"
    "v_mfma_f32_16x16x32_bf16 v[108:111], a[156:159], a[232:235], v[108:111]\n"
    "buffer_load_dwordx4 a[80:83], v14, s[36:39], s68 offen offset:0 sc1\n"
    "v_mfma_f32_16x16x32_bf16 v[108:111], a[152:155], a[228:231], v[108:111]\n"
    "ds_read_b32 v38, v36 offset:30592\n"
    "v_mfma_f32_16x16x32_bf16 v[108:111], a[148:151], a[224:227], v[108:111]\n"
    "v_mfma_f32_16x16x32_bf16 v[108:111], a[144:147], a[220:223], v[108:111]\n"
    "v_mfma_f32_16x16x32_bf16 v[108:111], a[140:143], a[216:219], v[108:111]\n"
    "v_mfma_f32_16x16x32_bf16 v[120:123], a[140:143], a[220:223], v[120:123]\n"
    "s_add_u32 s68, s64, 0\n"
    "v_mfma_f32_16x16x32_bf16 v[120:123], a[144:147], a[224:227], v[120:123]\n"
    "buffer_load_dwordx4 a[84:87], v14, s[36:39], s68 offen offset:0 sc1\n"
    "v_mfma_f32_16x16x32_bf16 v[120:123], a[148:151], a[228:231], v[120:123]\n"
    "s_waitcnt lgkmcnt(0)\n"
    "v_cmp_gt_u32 vcc, 6, v38\n"
    "v_mfma_f32_16x16x32_bf16 v[120:123], a[152:155], a[232:235], v[120:123]\n"
    "v_mfma_f32_16x16x32_bf16 v[120:123], a[156:159], a[236:239], v[120:123]\n"
    "s_cbranch_vccnz .Lsl6\n"
    ".Lbk6:\n"
    "v_mfma_f32_16x16x32_bf16 v[120:123], a[160:163], a[168:171], v[120:123]\n"
    "ds_read_b128 a[192:195], v13 offset:0\n"
    "v_mfma_f32_16x16x32_bf16 v[120:123], a[164:167], a[172:175], v[120:123]\n"
    "buffer_load_dwordx4 a[88:91], v14, s[36:39], s68 offen offset:1024 sc1\n"
    "v_mfma_f32_16x16x32_bf16 v[132:135], a[164:167], a[176:179], v[132:135]\n"
    "ds_read_b128 a[196:199], v13 offset:1024\n"
    "v_mfma_f32_16x16x32_bf16 v[132:135], a[160:163], a[172:175], v[132:135]\n"
    "ds_read_b128 a[200:203], v13 offset:2048\n"
    "v_mfma_f32_16x16x32_bf16 v[132:135], a[156:159], a[168:171], v[132:135]\n"
    "ds_read_b128 a[204:207], v13 offset:3072\n"
    "v_mfma_f32_16x16x32_bf16 v[132:135], a[152:155], a[236:239], v[132:135]\n"
    "ds_read_b128 a[208:211], v13 offset:4096\n"
    "v_mfma_f32_16x16x32_bf16 v[132:135], a[148:151], a[232:235], v[132:135]\n"
    "buffer_load_dwordx4 a[92:95], v14, s[36:39], s68 offen offset:2048 sc1\n"
    "v_mfma_f32_16x16x32_bf16 v[132:135], a[144:147], a[228:231], v[132:135]\n"
    "ds_read_b128 a[212:215], v13 offset:5120\n"
    "v_mfma_f32_16x16x32_bf16 v[132:135], a[140:143], a[224:227], v[132:135]\n"
    "ds_read_b128 a[216:219], v13 offset:6144\n"
    "v_mfma_f32_16x16x32_bf16 v[144:147], a[140:143], a[228:231], v[144:147]\n"
    "ds_read_b128 a[220:223], v13 offset:7168\n"
    "v_mfma_f32_16x16x32_bf16 v[144:147], a[144:147], a[232:235], v[144:147]\n"
    "ds_read_b128 a[224:227], v13 offset:8192\n"
    "v_mfma_f32_16x16x32_bf16 v[144:147], a[148:151], a[236:239], v[144:147]\n"
    "buffer_load_dwordx4 a[96:99], v14, s[36:39], s68 offen offset:3072 sc1\n"
    "v_mfma_f32_16x16x32_bf16 v[144:147], a[152:155], a[168:171], v[144:147]\n"
    "ds_read_b128 a[228:231], v13 offset:9216\n"
    "v_mfma_f32_16x16x32_bf16 v[144:147], a[156:159], a[172:175], v[144:147]\n"
    "v_mfma_f32_16x16x32_bf16 v[144:147], a[160:163], a[176:179], v[144:147]\n"
    "v_mfma_f32_16x16x32_bf16 v[144:147], a[164:167], a[180:183], v[144:147]\n"
    "v_mfma_f32_16x16x32_bf16 v[156:159], a[164:167], a[184:187], v[156:159]\n"
    "s_addk_i32 s68, 0x1000\n"
    "v_mfma_f32_16x16x32_bf16 v[156:159], a[160:163], a[180:183], v[156:159]\n"
    "buffer_load_dwordx4 a[100:103], v14, s[36:39], s68 offen offset:0 sc1\n"
    "v_mfma_f32_16x16x32_bf16 v[156:159], a[156:159], a[176:179], v[156:159]\n"
    "v_mfma_f32_16x16x32_bf16 v[156:159], a[152:155], a[172:175], v[156:159]\n"
    "v_mfma_f32_16x16x32_bf16 v[156:159], a[148:151], a[168:171], v[156:159]\n"
    "v_mfma_f32_16x16x32_bf16 v[156:159], a[144:147], a[236:239], v[156:159]\n"
    "v_mfma_f32_16x16x32_bf16 v[156:159], a[140:143], a[232:235], v[156:159]\n"
    "buffer_load_dwordx4 a[104:107], v14, s[36:39], s68 offen offset:1024 sc1\n"
    "v_mfma_f32_16x16x32_bf16 v[168:171], a[140:143], a[236:239], v[168:171]\n"
    "ds_read_b128 a[232:235], v13 offset:10240\n"
    "v_mfma_f32_16x16x32_bf16 v[168:171], a[144:147], a[168:171], v[168:171]\n"
    "ds_read_b128 a[236:239], v13 offset:11264\n"
    "v_mfma_f32_16x16x32_bf16 v[168:171], a[148:151], a[172:175], v[168:171]\n"
    "ds_read_b128 a[168:171], v13 offset:12288\n"
    "v_mfma_f32_16x16x32_bf16 v[168:171], a[152:155], a[176:179], v[168:171]\n"
    "v_mfma_f32_16x16x32_bf16 v[168:171], a[156:159], a[180:183], v[168:171]\n"
    "buffer_load_dwordx4 a[108:111], v14, s[36:39], s68 offen offset:2048 sc1\n"
    "v_mfma_f32_16x16x32_bf16 v[168:171], a[160:163], a[184:187], v[168:171]\n"
    ".Lpad3:\n"
    "s_cbranch_execz .Lhop_done\n"
    "v_mfma_f32_16x16x32_bf16 v[168:171], a[164:167], a[188:191], v[168:171]\n"
    "s_waitcnt lgkmcnt(3)\n"
    "s_waitcnt vmcnt(21)\n"
    "v_mfma_f32_16x16x32_bf16 v[88:91], a[0:3], a[192:195], v[88:91]\n"
    "v_mfma_f32_16x16x32_bf16 v[88:91], a[4:7], a[196:199], v[88:91]\n"
    "v_mfma_f32_16x16x32_bf16 v[88:91], a[8:11], a[200:203], v[88:91]\n"
    "buffer_load_dwordx4 a[112:115], v14, s[36:39], s68 offen offset:3072 sc1\n"
    "v_mfma_f32_16x16x32_bf16 v[88:91], a[12:15], a[204:207], v[88:91]\n"
    "v_mfma_f32_16x16x32_bf16 v[88:91], a[16:19], a[208:211], v[88:91]\n"
    "v_mfma_f32_16x16x32_bf16 v[88:91], a[20:23], a[212:215], v[88:91]\n"
    "v_mfma_f32_16x16x32_bf16 v[88:91], a[24:27], a[216:219], v[88:91]\n"
    "v_mfma_f32_16x16x32_bf16 v[100:103], a[24:27], a[220:223], v[100:103]\n"
    "s_addk_i32 s68, 0x1000\n"
    "v_mfma_f32_16x16x32_bf16 v[100:103], a[20:23], a[216:219], v[100:103]\n"
    "buffer_load_dwordx4 a[116:119], v14, s[36:39], s68 offen offset:0 sc1\n"
    "v_mfma_f32_16x16x32_bf16 v[100:103], a[16:19], a[212:215], v[100:103]\n"
    "ds_read_b64_tr_b16 a[240:241], v28 offset:14336\n"
    "v_mfma_f32_16x16x32_bf16 v[100:103], a[12:15], a[208:211], v[100:103]\n"
    "ds_read_b64_tr_b16 a[242:243], v28 offset:14592\n"
    "v_mfma_f32_16x16x32_bf16 v[100:103], a[8:11], a[204:207], v[100:103]\n"
    "ds_read_b64_tr_b16 a[244:245], v29 offset:14336\n"
    "v_mfma_f32_16x16x32_bf16 v[100:103], a[4:7], a[200:203], v[100:103]\n"
    "ds_read_b64_tr_b16 a[246:247], v29 offset:14592\n"
    "v_mfma_f32_16x16x32_bf16 v[100:103], a[0:3], a[196:199], v[100:103]\n"
    "buffer_load_dwordx4 a[120:123], v14, s[36:39], s68 offen offset:1024 sc1\n"
    "v_mfma_f32_16x16x32_bf16 v[112:115], a[0:3], a[200:203], v[112:115]\n"
    "v_sub_f32 v172, v172, v88\n"
    "v_sub_f32 v173, v173, v89\n"
    "v_mfma_f32_16x16x32_bf16 v[112:115], a[4:7], a[204:207], v[112:115]\n"
    "v_sub_f32 v174, v174, v90\n"
    "v_sub_f32 v175, v175, v91\n"
    "v_mfma_f32_16x16x32_bf16 v[112:115], a[8:11], a[208:211], v[112:115]\n"
    "v_mfma_f32_16x16x32_bf16 v[112:115], a[12:15], a[212:215], v[112:115]\n"
    "v_mfma_f32_16x16x32_bf16 v[112:115], a[16:19], a[216:219], v[112:115]\n"
    "buffer_load_dwordx4 a[124:127], v14, s[36:39], s68 offen offset:2048 sc1\n"
    "v_mfma_f32_16x16x32_bf16 v[112:115], a[20:23], a[220:223], v[112:115]\n"
    "v_mfma_f32_16x16x32_bf16 v[112:115], a[24:27], a[224:227], v[112:115]\n"
    "s_waitcnt lgkmcnt(0)\n"
    "v_mfma_f32_16x16x32_bf16 v[64:67], a[244:247], v[48:51], 0\n"
    "v_mfma_f32_16x16x32_bf16 v[60:63], a[240:243], v[48:51], 0\n"
    "v_mfma_f32_16x16x32_bf16 v[124:127], a[24:27], a[228:231], v[124:127]\n"
    "v_sub_f32 v184, v184, v100\n"
    "v_sub_f32 v185, v185, v101\n"
    "v_mfma_f32_16x16x32_bf16 v[124:127], a[20:23], a[224:227], v[124:127]\n"
    "ds_read_b64_tr_b16 a[248:249], v28 offset:15744\n"
    "v_sub_f32 v186, v186, v102\n"
    "v_mfma_f32_16x16x32_bf16 v[124:127], a[16:19], a[220:223], v[124:127]\n"
    "buffer_load_dwordx4 a[128:131], v14, s[36:39], s68 offen offset:3072 sc1\n"
    "v_mfma_f32_16x16x32_bf16 v[124:127], a[12:15], a[216:219], v[124:127]\n"
    "ds_read_b64_tr_b16 a[250:251], v28 offset:16000\n"
    "v_sub_f32 v187, v187, v103\n"
    "v_mfma_f32_16x16x32_bf16 v[124:127], a[8:11], a[212:215], v[124:127]\n"
    "ds_read_b64_tr_b16 a[252:253], v29 offset:15744\n"
    "v_mfma_f32_16x16x32_bf16 v[124:127], a[4:7], a[208:211], v[124:127]\n"
    "ds_read_b64_tr_b16 a[254:255], v29 offset:16000\n"
    "v_mfma_f32_16x16x32_bf16 v[124:127], a[0:3], a[204:207], v[124:127]\n"
    "v_cvt_pk_bf16_f32 v78, v64, v65\n"
    "v_cvt_pk_bf16_f32 v79, v66, v67\n"
    "v_mfma_f32_16x16x32_bf16 v[136:139], a[0:3], a[208:211], v[136:139]\n"
    "s_addk_i32 s68, 0x1000\n"
    "v_cvt_pk_bf16_f32 v76, v60, v61\n"
    "v_cvt_pk_bf16_f32 v77, v62, v63\n"
    "v_mfma_f32_16x16x32_bf16 v[136:139], a[4:7], a[212:215], v[136:139]\n"
    "buffer_load_dwordx4 a[132:135], v14, s[36:39], s68 offen offset:0 sc1\n"
    "v_mfma_f32_16x16x32_bf16 v[136:139], a[8:11], a[216:219], v[136:139]\n"
    "ds_write_b32 v12, v78 offset:20488\n"
    "v_mfma_f32_16x16x32_bf16 v[136:139], a[12:15], a[220:223], v[136:139]\n"
    "v_sub_f32 v196, v196, v112\n"
    "v_sub_f32 v197, v197, v113\n"
    "v_mfma_f32_16x16x32_bf16 v[136:139], a[16:19], a[224:227], v[136:139]\n"
    "ds_write_b32 v12, v79 offset:20492\n"
    "v_mfma_f32_16x16x32_bf16 v[136:139], a[20:23], a[228:231], v[136:139]\n"
    "s_waitcnt lgkmcnt(2)\n"
    "v_sub_f32 v198, v198, v114\n"
    "v_sub_f32 v199, v199, v115\n"
    "v_mfma_f32_16x16x32_bf16 v[136:139], a[24:27], a[232:235], v[136:139]\n"
    "buffer_load_dwordx4 a[136:139], v14, s[36:39], s68 offen offset:1024 sc1\n"
    "v_mfma_f32_16x16x32_bf16 v[148:151], a[24:27], a[236:239], v[148:151]\n"
    "ds_write_b32 v12, v76 offset:20480\n"
    "v_mfma_f32_16x16x32_bf16 v[72:75], a[252:255], v[48:51], 0\n"
    "v_mfma_f32_16x16x32_bf16 v[68:71], a[248:251], v[48:51], 0\n"
    "v_mfma_f32_16x16x32_bf16 v[148:151], a[20:23], a[232:235], v[148:151]\n"
    "ds_read_b64_tr_b16 a[240:241], v28 offset:17152\n"
    "v_sub_f32 v208, v208, v124\n"
    "v_mfma_f32_16x16x32_bf16 v[148:151], a[16:19], a[228:231], v[148:151]\n"
    "ds_write_b32 v12, v77 offset:20484\n"
    "v_mfma_f32_16x16x32_bf16 v[148:151], a[12:15], a[224:227], v[148:151]\n"
    "ds_read_b64_tr_b16 a[242:243], v28 offset:17408\n"
    "v_sub_f32 v209, v209, v125\n"
    "v_mfma_f32_16x16x32_bf16 v[148:151], a[8:11], a[220:223], v[148:151]\n"
    "buffer_load_dwordx4 a[140:143], v14, s[36:39], s68 offen offset:2048 sc1\n"
    "v_mfma_f32_16x16x32_bf16 v[148:151], a[4:7], a[216:219], v[148:151]\n"
    "ds_read_b64_tr_b16 a[244:245], v29 offset:17152\n"
    "v_sub_f32 v210, v210, v126\n"
    "v_mfma_f32_16x16x32_bf16 v[148:151], a[0:3], a[212:215], v[148:151]\n"
    "ds_read_b64_tr_b16 a[246:247], v29 offset:17408\n"
    "v_sub_f32 v211, v211, v127\n"
    "v_mfma_f32_16x16x32_bf16 v[160:163], a[0:3], a[216:219], v[160:163]\n"
    "v_cvt_pk_bf16_f32 v82, v72, v73\n"
    "v_cvt_pk_bf16_f32 v83, v74, v75\n"
    "v_mfma_f32_16x16x32_bf16 v[160:163], a[4:7], a[220:223], v[160:163]\n"
    "v_cvt_pk_bf16_f32 v80, v68, v69\n"
    "v_cvt_pk_bf16_f32 v81, v70, v71\n"
    "v_mfma_f32_16x16x32_bf16 v[160:163], a[8:11], a[224:227], v[160:163]\n"
    "buffer_load_dwordx4 a[144:147], v14, s[36:39], s68 offen offset:3072 sc1\n"
    "v_mfma_f32_16x16x32_bf16 v[160:163], a[12:15], a[228:231], v[160:163]\n"
    "ds_write_b32 v12, v82 offset:24584\n"
    "v_mfma_f32_16x16x32_bf16 v[160:163], a[16:19], a[232:235], v[160:163]\n"
    "s_waitcnt vmcnt(23)\n"
    "v_sub_f32 v220, v220, v136\n"
    "v_sub_f32 v221, v221, v137\n"
    "v_mfma_f32_16x16x32_bf16 v[160:163], a[20:23], a[236:239], v[160:163]\n"
    "ds_write_b32 v12, v83 offset:24588\n"
    "v_mfma_f32_16x16x32_bf16 v[160:163], a[24:27], a[168:171], v[160:163]\n"
    "v_sub_f32 v222, v222, v138\n"
    "v_sub_f32 v223, v223, v139\n"
    "v_mfma_f32_16x16x32_bf16 v[92:95], a[28:31], a[192:195], v[92:95]\n"
    "s_addk_i32 s68, 0x1000\n"
    "ds_write_b32 v12, v80 offset:24576\n"
    "v_mfma_f32_16x16x32_bf16 v[92:95], a[32:35], a[196:199], v[92:95]\n"
    "buffer_load_dwordx4 a[148:151], v14, s[36:39], s68 offen offset:0 sc1\n"
    "v_mfma_f32_16x16x32_bf16 v[92:95], a[36:39], a[200:203], v[92:95]\n"
    "ds_write_b32 v12, v81 offset:24580\n"
    "v_mfma_f32_16x16x32_bf16 v[92:95], a[40:43], a[204:207], v[92:95]\n"
    "ds_read_b64_tr_b16 a[248:249], v28 offset:18560\n"
    "v_sub_f32 v232, v232, v148\n"
    "v_mfma_f32_16x16x32_bf16 v[92:95], a[44:47], a[208:211], v[92:95]\n"
    "ds_read_b64_tr_b16 a[250:251], v28 offset:18816\n"
    "v_sub_f32 v233, v233, v149\n"
    "v_mfma_f32_16x16x32_bf16 v[92:95], a[48:51], a[212:215], v[92:95]\n"
    "ds_read_b64_tr_b16 a[252:253], v29 offset:18560\n"
    "v_sub_f32 v234, v234, v150\n"
    "v_mfma_f32_16x16x32_bf16 v[92:95], a[52:55], a[216:219], v[92:95]\n"
    "buffer_load_dwordx4 a[152:155], v14, s[36:39], s68 offen offset:1024 sc1\n"
    "v_mfma_f32_16x16x32_bf16 v[104:107], a[52:55], a[220:223], v[104:107]\n"
    "s_waitcnt lgkmcnt(5)\n"
    "ds_read_b64_tr_b16 a[254:255], v29 offset:18816\n"
    "v_sub_f32 v235, v235, v151\n"
    "v_mfma_f32_16x16x32_bf16 v[64:67], a[244:247], v[48:51], 0\n"
    "v_mfma_f32_16x16x32_bf16 v[60:63], a[240:243], v[48:51], 0\n"
    "v_mfma_f32_16x16x32_bf16 v[104:107], a[48:51], a[216:219], v[104:107]\n"
    "v_sub_f32 v244, v244, v160\n"
    "v_sub_f32 v245, v245, v161\n"
    "v_mfma_f32_16x16x32_bf16 v[104:107], a[44:47], a[212:215], v[104:107]\n"
    "v_sub_f32 v246, v246, v162\n"
    "v_sub_f32 v247, v247, v163\n"
    "v_mfma_f32_16x16x32_bf16 v[104:107], a[40:43], a[208:211], v[104:107]\n"
    "v_mfma_f32_16x16x32_bf16 v[104:107], a[36:39], a[204:207], v[104:107]\n"
    "buffer_load_dwordx4 a[156:159], v14, s[36:39], s68 offen offset:2048 sc1\n"
    "v_mfma_f32_16x16x32_bf16 v[104:107], a[32:35], a[200:203], v[104:107]\n"
    "v_mfma_f32_16x16x32_bf16 v[104:107], a[28:31], a[196:199], v[104:107]\n"
    "v_mfma_f32_16x16x32_bf16 v[116:119], a[28:31], a[200:203], v[116:119]\n"
    "v_cvt_pk_bf16_f32 v78, v64, v65\n"
    "v_cvt_pk_bf16_f32 v79, v66, v67\n"
    "v_mfma_f32_16x16x32_bf16 v[116:119], a[32:35], a[204:207], v[116:119]\n"
    "v_cvt_pk_bf16_f32 v76, v60, v61\n"
    "v_cvt_pk_bf16_f32 v77, v62, v63\n"
    "v_mfma_f32_16x16x32_bf16 v[116:119], a[36:39], a[208:211], v[116:119]\n"
    "buffer_load_dwordx4 a[160:163], v14, s[36:39], s68 offen offset:3072 sc1\n"
    "v_mfma_f32_16x16x32_bf16 v[116:119], a[40:43], a[212:215], v[116:119]\n"
    "ds_write_b32 v12, v78 offset:28680\n"
    "v_mfma_f32_16x16x32_bf16 v[116:119], a[44:47], a[216:219], v[116:119]\n"
    "ds_read_b64_tr_b16 a[240:241], v28 offset:19968\n"
    "v_sub_f32 v176, v176, v92\n"
    "v_mfma_f32_16x16x32_bf16 v[116:119], a[48:51], a[220:223], v[116:119]\n"
    "ds_write_b32 v12, v79 offset:28684\n"
    "v_mfma_f32_16x16x32_bf16 v[116:119], a[52:55], a[224:227], v[116:119]\n"
    "s_waitcnt lgkmcnt(3)\n"
    "ds_read_b64_tr_b16 a[242:243], v28 offset:20224\n"
    "v_sub_f32 v177, v177, v93\n"
    "v_mfma_f32_16x16x32_bf16 v[72:75], a[252:255], v[48:51], 0\n"
    "v_mfma_f32_16x16x32_bf16 v[68:71], a[248:251], v[48:51], 0\n"
    "v_mfma_f32_16x16x32_bf16 v[128:131], a[52:55], a[228:231], v[128:131]\n"
    "s_addk_i32 s68, 0x1000\n"
    "ds_write_b32 v12, v76 offset:28672\n"
    "v_mfma_f32_16x16x32_bf16 v[128:131], a[48:51], a[224:227], v[128:131]\n"
    "buffer_load_dwordx4 a[164:167], v14, s[36:39], s68 offen offset:0 sc1\n"
    "v_mfma_f32_16x16x32_bf16 v[128:131], a[44:47], a[220:223], v[128:131]\n"
    "ds_write_b32 v12, v77 offset:28676\n"
    "v_mfma_f32_16x16x32_bf16 v[128:131], a[40:43], a[216:219], v[128:131]\n"
    "ds_read_b64_tr_b16 a[244:245], v29 offset:19968\n"
    "v_sub_f32 v178, v178, v94\n"
    "v_mfma_f32_16x16x32_bf16 v[128:131], a[36:39], a[212:215], v[128:131]\n"
    "ds_read_b64_tr_b16 a[246:247], v29 offset:20224\n"
    "v_sub_f32 v179, v179, v95\n"
    "v_mfma_f32_16x16x32_bf16 v[128:131], a[32:35], a[208:211], v[128:131]\n"
    "v_sub_f32 v188, v188, v104\n"
    "v_sub_f32 v189, v189, v105\n"
    "v_mfma_f32_16x16x32_bf16 v[128:131], a[28:31], a[204:207], v[128:131]\n"
    "s_add_u32 s68, s62, 516096\n"
    "v_cvt_pk_bf16_f32 v82, v72, v73\n"
    "v_cvt_pk_bf16_f32 v83, v74, v75\n"
    "v_mfma_f32_16x16x32_bf16 v[140:143], a[28:31], a[208:211], v[140:143]\n"
    "buffer_load_dwordx4 a[0:3], v14, s[36:39], s68 offen offset:0 sc1\n"
    "v_mfma_f32_16x16x32_bf16 v[140:143], a[32:35], a[212:215], v[140:143]\n"
    "v_cvt_pk_bf16_f32 v80, v68, v69\n"
    "v_cvt_pk_bf16_f32 v81, v70, v71\n"
    "v_mfma_f32_16x16x32_bf16 v[140:143], a[36:39], a[216:219], v[140:143]\n"
    "ds_write_b32 v12, v82 offset:32776\n"
    "v_mfma_f32_16x16x32_bf16 v[140:143], a[40:43], a[220:223], v[140:143]\n"
    "s_waitcnt lgkmcnt(1)\n"
    "v_sub_f32 v190, v190, v106\n"
    "v_sub_f32 v191, v191, v107\n"
    "v_mfma_f32_16x16x32_bf16 v[140:143], a[44:47], a[224:227], v[140:143]\n"
    "ds_write_b32 v12, v83 offset:32780\n"
    "v_mfma_f32_16x16x32_bf16 v[140:143], a[48:51], a[228:231], v[140:143]\n"
    "buffer_load_dwordx4 a[4:7], v14, s[36:39], s68 offen offset:1024 sc1\n"
    "v_mfma_f32_16x16x32_bf16 v[140:143], a[52:55], a[232:235], v[140:143]\n"
    "ds_write_b32 v12, v80 offset:32768\n"
    "v_mfma_f32_16x16x32_bf16 v[64:67], a[244:247], v[48:51], 0\n"
    "v_mfma_f32_16x16x32_bf16 v[60:63], a[240:243], v[48:51], 0\n"
    "v_mfma_f32_16x16x32_bf16 v[152:155], a[52:55], a[236:239], v[152:155]\n"
    "v_add_u32 v37, 1, v37\n"
    "v_sub_f32 v200, v200, v116\n"
    "v_mfma_f32_16x16x32_bf16 v[152:155], a[48:51], a[232:235], v[152:155]\n"
    "ds_write_b32 v12, v81 offset:32772\n"
    "v_mfma_f32_16x16x32_bf16 v[152:155], a[44:47], a[228:231], v[152:155]\n"
    "v_sub_f32 v201, v201, v117\n"
    "v_sub_f32 v202, v202, v118\n"
    "v_mfma_f32_16x16x32_bf16 v[152:155], a[40:43], a[224:227], v[152:155]\n"
    "buffer_load_dwordx4 a[8:11], v14, s[36:39], s68 offen offset:2048 sc1\n"
    "v_mfma_f32_16x16x32_bf16 v[152:155], a[36:39], a[220:223], v[152:155]\n"
    "v_sub_f32 v203, v203, v119\n"
    "v_sub_f32 v212, v212, v128\n"
    "v_mfma_f32_16x16x32_bf16 v[152:155], a[32:35], a[216:219], v[152:155]\n"
    "v_sub_f32 v213, v213, v129\n"
    "v_sub_f32 v214, v214, v130\n"
    "v_mfma_f32_16x16x32_bf16 v[152:155], a[28:31], a[212:215], v[152:155]\n"
    "v_cvt_pk_bf16_f32 v78, v64, v65\n"
    "v_cvt_pk_bf16_f32 v79, v66, v67\n"
    "v_mfma_f32_16x16x32_bf16 v[164:167], a[28:31], a[216:219], v[164:167]\n"
    "v_cvt_pk_bf16_f32 v76, v60, v61\n"
    "v_cvt_pk_bf16_f32 v77, v62, v63\n"
    "v_mfma_f32_16x16x32_bf16 v[164:167], a[32:35], a[220:223], v[164:167]\n"
    "buffer_load_dwordx4 a[12:15], v14, s[36:39], s68 offen offset:3072 sc1\n"
    "v_mfma_f32_16x16x32_bf16 v[164:167], a[36:39], a[224:227], v[164:167]\n"
    "ds_write_b32 v12, v78 offset:36872\n"
    "v_mfma_f32_16x16x32_bf16 v[164:167], a[40:43], a[228:231], v[164:167]\n"
    "v_sub_f32 v215, v215, v131\n"
    "v_sub_f32 v224, v224, v140\n"
    "v_mfma_f32_16x16x32_bf16 v[164:167], a[44:47], a[232:235], v[164:167]\n"
    "ds_write_b32 v12, v79 offset:36876\n"
    "v_mfma_f32_16x16x32_bf16 v[164:167], a[48:51], a[236:239], v[164:167]\n"
    "s_waitcnt vmcnt(25)\n"
    "v_sub_f32 v225, v225, v141\n"
    "v_sub_f32 v226, v226, v142\n"
    "v_mfma_f32_16x16x32_bf16 v[164:167], a[52:55], a[168:171], v[164:167]\n"
    "s_addk_i32 s68, 0x1000\n"
    "ds_write_b32 v12, v76 offset:36864\n"
    "v_mfma_f32_16x16x32_bf16 v[96:99], a[56:59], a[192:195], v[96:99]\n"
    "buffer_load_dwordx4 a[16:19], v14, s[36:39], s68 offen offset:0 sc1\n"
    "v_mfma_f32_16x16x32_bf16 v[96:99], a[60:63], a[196:199], v[96:99]\n"
    "ds_write_b32 v12, v77 offset:36868\n"
    "v_mfma_f32_16x16x32_bf16 v[96:99], a[64:67], a[200:203], v[96:99]\n"
    "ds_write_b32 v35, v37\n"
    "v_mfma_f32_16x16x32_bf16 v[96:99], a[68:71], a[204:207], v[96:99]\n"
    "v_sub_f32 v227, v227, v143\n"
    "v_sub_f32 v236, v236, v152\n"
    "v_mfma_f32_16x16x32_bf16 v[96:99], a[72:75], a[208:211], v[96:99]\n"
    "v_sub_f32 v237, v237, v153\n"
    "v_sub_f32 v238, v238, v154\n"
    "v_mfma_f32_16x16x32_bf16 v[96:99], a[76:79], a[212:215], v[96:99]\n"
    "buffer_load_dwordx4 a[20:23], v14, s[36:39], s68 offen offset:1024 sc1\n"
    "v_mfma_f32_16x16x32_bf16 v[96:99], a[80:83], a[216:219], v[96:99]\n"
    "v_sub_f32 v239, v239, v155\n"
    "v_mfma_f32_16x16x32_bf16 v[108:111], a[80:83], a[220:223], v[108:111]\n"
    "v_sub_f32 v248, v248, v164\n"
    "v_sub_f32 v249, v249, v165\n"
    "v_mfma_f32_16x16x32_bf16 v[108:111], a[76:79], a[216:219], v[108:111]\n"
    "v_sub_f32 v250, v250, v166\n"
    "v_sub_f32 v251, v251, v167\n"
    "v_mfma_f32_16x16x32_bf16 v[108:111], a[72:75], a[212:215], v[108:111]\n"
    "v_mfma_f32_16x16x32_bf16 v[108:111], a[68:71], a[208:211], v[108:111]\n"
    "buffer_load_dwordx4 a[24:27], v14, s[36:39], s68 offen offset:2048 sc1\n"
    "v_mfma_f32_16x16x32_bf16 v[108:111], a[64:67], a[204:207], v[108:111]\n"
    "ds_read_b32 v38, v36 offset:30592\n"
    "v_mfma_f32_16x16x32_bf16 v[108:111], a[60:63], a[200:203], v[108:111]\n"
    "v_mfma_f32_16x16x32_bf16 v[108:111], a[56:59], a[196:199], v[108:111]\n"
    "v_mfma_f32_16x16x32_bf16 v[120:123], a[56:59], a[200:203], v[120:123]\n"
    "v_sub_f32 v180, v180, v96\n"
    "v_sub_f32 v181, v181, v97\n"
    "v_mfma_f32_16x16x32_bf16 v[120:123], a[60:63], a[204:207], v[120:123]\n"
    "buffer_load_dwordx4 a[28:31], v14, s[36:39], s68 offen offset:3072 sc1\n"
    "v_mfma_f32_16x16x32_bf16 v[120:123], a[64:67], a[208:211], v[120:123]\n"
    "s_waitcnt lgkmcnt(0)\n"
    "v_cmp_gt_u32 vcc, 7, v38\n"
    "v_sub_f32 v182, v182, v98\n"
    "v_mfma_f32_16x16x32_bf16 v[120:123], a[68:71], a[212:215], v[120:123]\n"
    "v_sub_f32 v183, v183, v99\n"
    "v_mfma_f32_16x16x32_bf16 v[120:123], a[72:75], a[216:219], v[120:123]\n"
    "s_cbranch_vccnz .Lsl7\n"
    ".Lbk7:\n"
    "v_mfma_f32_16x16x32_bf16 v[120:123], a[76:79], a[220:223], v[120:123]\n"
    "ds_read_b128 a[172:175], v13 offset:20480\n"
    "v_mfma_f32_16x16x32_bf16 v[120:123], a[80:83], a[224:227], v[120:123]\n"
    "s_addk_i32 s68, 0x1000\n"
    "ds_read_b128 a[176:179], v13 offset:21504\n"
    "v_mfma_f32_16x16x32_bf16 v[132:135], a[80:83], a[228:231], v[132:135]\n"
    "buffer_load_dwordx4 a[32:35], v14, s[36:39], s68 offen offset:0 sc1\n"
    "v_mfma_f32_16x16x32_bf16 v[132:135], a[76:79], a[224:227], v[132:135]\n"
    "ds_read_b128 a[180:183], v13 offset:22528\n"
    "v_sub_f32 v192, v192, v108\n"
    "v_mfma_f32_16x16x32_bf16 v[132:135], a[72:75], a[220:223], v[132:135]\n"
    "ds_read_b128 a[184:187], v13 offset:23552\n"
    "v_sub_f32 v193, v193, v109\n"
    "v_mfma_f32_16x16x32_bf16 v[132:135], a[68:71], a[216:219], v[132:135]\n"
    "ds_read_b128 a[188:191], v13 offset:24576\n"
    "v_sub_f32 v194, v194, v110\n"
    "v_mfma_f32_16x16x32_bf16 v[132:135], a[64:67], a[212:215], v[132:135]\n"
    "ds_read_b128 a[192:195], v13 offset:25600\n"
    "v_sub_f32 v195, v195, v111\n"
    "v_mfma_f32_16x16x32_bf16 v[132:135], a[60:63], a[208:211], v[132:135]\n"
    "buffer_load_dwordx4 a[36:39], v14, s[36:39], s68 offen offset:1024 sc1\n"
    "v_mfma_f32_16x16x32_bf16 v[132:135], a[56:59], a[204:207], v[132:135]\n"
    "ds_read_b128 a[196:199], v13 offset:26624\n"
    "v_mfma_f32_16x16x32_bf16 v[144:147], a[56:59], a[208:211], v[144:147]\n"
    "ds_read_b128 a[200:203], v13 offset:27648\n"
    "v_sub_f32 v204, v204, v120\n"
    "v_mfma_f32_16x16x32_bf16 v[144:147], a[60:63], a[212:215], v[144:147]\n"
    "ds_read_b128 a[204:207], v13 offset:28672\n"
    "v_sub_f32 v205, v205, v121\n"
    "v_mfma_f32_16x16x32_bf16 v[144:147], a[64:67], a[216:219], v[144:147]\n"
    "ds_read_b128 a[208:211], v13 offset:29696\n"
    "v_sub_f32 v206, v206, v122\n"
    "v_mfma_f32_16x16x32_bf16 v[144:147], a[68:71], a[220:223], v[144:147]\n"
    "buffer_load_dwordx4 a[40:43], v14, s[36:39], s68 offen offset:2048 sc1\n"
    "v_mfma_f32_16x16x32_bf16 v[144:147], a[72:75], a[224:227], v[144:147]\n"
    "v_sub_f32 v207, v207, v123\n"
    "v_mfma_f32_16x16x32_bf16 v[144:147], a[76:79], a[228:231], v[144:147]\n"
    "v_mfma_f32_16x16x32_bf16 v[144:147], a[80:83], a[232:235], v[144:147]\n"
    "v_mfma_f32_16x16x32_bf16 v[156:159], a[80:83], a[236:239], v[156:159]\n"
    "v_sub_f32 v216, v216, v132\n"
    "v_sub_f32 v217, v217, v133\n"
    "v_mfma_f32_16x16x32_bf16 v[156:159], a[76:79], a[232:235], v[156:159]\n"
    "buffer_load_dwordx4 a[44:47], v14, s[36:39], s68 offen offset:3072 sc1\n"
    "v_mfma_f32_16x16x32_bf16 v[156:159], a[72:75], a[228:231], v[156:159]\n"
    "v_sub_f32 v218, v218, v134\n"
    "v_sub_f32 v219, v219, v135\n"
    "v_mfma_f32_16x16x32_bf16 v[156:159], a[68:71], a[224:227], v[156:159]\n"
    "v_mfma_f32_16x16x32_bf16 v[156:159], a[64:67], a[220:223], v[156:159]\n"
    "v_mfma_f32_16x16x32_bf16 v[156:159], a[60:63], a[216:219], v[156:159]\n"
    "v_mfma_f32_16x16x32_bf16 v[156:159], a[56:59], a[212:215], v[156:159]\n"
    "s_addk_i32 s68, 0x1000\n"
    "ds_read_b128 a[212:215], v13 offset:30720\n"
    "v_mfma_f32_16x16x32_bf16 v[168:171], a[56:59], a[216:219], v[168:171]\n"
    "buffer_load_dwordx4 a[48:51], v14, s[36:39], s68 offen offset:0 sc1\n"
    "v_mfma_f32_16x16x32_bf16 v[168:171], a[60:63], a[220:223], v[168:171]\n"
    "ds_read_b128 a[216:219], v13 offset:31744\n"
    "v_sub_f32 v228, v228, v144\n"
    "v_mfma_f32_16x16x32_bf16 v[168:171], a[64:67], a[224:227], v[168:171]\n"
    "ds_read_b128 a[220:223], v13 offset:32768\n"
    "v_sub_f32 v229, v229, v145\n"
    "v_mfma_f32_16x16x32_bf16 v[168:171], a[68:71], a[228:231], v[168:171]\n"
    "v_sub_f32 v230, v230, v146\n"
    "v_sub_f32 v231, v231, v147\n"
    "v_mfma_f32_16x16x32_bf16 v[168:171], a[72:75], a[232:235], v[168:171]\n"
    "v_mfma_f32_16x16x32_bf16 v[168:171], a[76:79], a[236:239], v[168:171]\n"
    "buffer_load_dwordx4 a[52:55], v14, s[36:39], s68 offen offset:1024 sc1\n"
    "v_mfma_f32_16x16x32_bf16 v[168:171], a[80:83], a[168:171], v[168:171]\n"
    "s_waitcnt lgkmcnt(3)\n"
    "s_waitcnt vmcnt(28)\n"
    "v_mfma_f32_16x16x32_bf16 v[88:91], a[84:87], a[172:175], v[88:91]\n"
    "s_mov_b32 s28, s44\n"
    "v_sub_f32 v240, v240, v156\n"
    "v_sub_f32 v241, v241, v157\n"
    "v_mfma_f32_16x16x32_bf16 v[88:91], a[88:91], a[176:179], v[88:91]\n"
    "s_and_b32 s29, s45, 0xffff\n"
    "v_sub_f32 v242, v242, v158\n"
    "v_sub_f32 v243, v243, v159\n"
    "v_mfma_f32_16x16x32_bf16 v[88:91], a[92:95], a[180:183], v[88:91]\n"
    "s_mov_b32 s30, 9633792\n"
    "s_mov_b32 s31, 0x20000\n"
    "s_mov_b32 s73, 0\n"
    "v_mfma_f32_16x16x32_bf16 v[88:91], a[96:99], a[184:187], v[88:91]\n"
    "buffer_load_dwordx4 a[56:59], v14, s[36:39], s68 offen offset:2048 sc1\n"
    "v_mfma_f32_16x16x32_bf16 v[88:91], a[100:103], a[188:191], v[88:91]\n"
    "s_xor_b32 s54, s48, 2\n"
    "s_mul_i32 s54, s54, 30720\n"
    "v_add_u32 v20, s69, v14\n"
    "v_mfma_f32_16x16x32_bf16 v[88:91], a[104:107], a[192:195], v[88:91]\n"
    "v_add_u32 v21, s54, v14\n"
    "v_and_b32 v5, 3, v4\n"
    "v_mfma_f32_16x16x32_bf16 v[88:91], a[108:111], a[196:199], v[88:91]\n"
    "v_and_b32 v6, 60, v4\n"
    "v_lshl_add_u32 v22, v5, 6, v6\n"
    "v_mfma_f32_16x16x32_bf16 v[100:103], a[108:111], a[200:203], v[100:103]\n"
    "v_sub_f32 v252, v252, v168\n"
    "v_sub_f32 v253, v253, v169\n"
    "v_mfma_f32_16x16x32_bf16 v[100:103], a[104:107], a[196:199], v[100:103]\n"
    "buffer_load_dwordx4 a[60:63], v14, s[36:39], s68 offen offset:3072 sc1\n"
    "v_mfma_f32_16x16x32_bf16 v[100:103], a[100:103], a[192:195], v[100:103]\n"
    "ds_read_b64_tr_b16 a[240:241], v16 offset:0\n"
    "v_sub_f32 v254, v254, v170\n"
    "v_mfma_f32_16x16x32_bf16 v[100:103], a[96:99], a[188:191], v[100:103]\n"
    "ds_read_b64_tr_b16 a[242:243], v16 offset:512\n"
    "v_sub_f32 v255, v255, v171\n"
    "v_mfma_f32_16x16x32_bf16 v[100:103], a[92:95], a[184:187], v[100:103]\n"
    "ds_read_b64_tr_b16 a[244:245], v17 offset:0\n"
    "v_lshrrev_b32 v5, 4, v4\n"
    "v_mfma_f32_16x16x32_bf16 v[100:103], a[88:91], a[180:183], v[100:103]\n"
    "ds_read_b64_tr_b16 a[246:247], v17 offset:512\n"
    "v_bfe_u32 v6, v4, 2, 2\n"
    "v_mfma_f32_16x16x32_bf16 v[100:103], a[84:87], a[176:179], v[100:103]\n"
    "v_xor_b32 v5, v5, v6\n"
    "v_and_b32 v6, 15, v4\n"
    "v_mfma_f32_16x16x32_bf16 v[112:115], a[84:87], a[180:183], v[112:115]\n"
    "s_addk_i32 s68, 0x1000\n"
    "v_fmac_f32 v172, s70, v88\n"
    "v_fmac_f32 v173, s70, v89\n"
    "v_mfma_f32_16x16x32_bf16 v[112:115], a[88:91], a[184:187], v[112:115]\n"
    "buffer_load_dwordx4 a[64:67], v14, s[36:39], s68 offen offset:0 sc1\n"
    "v_mfma_f32_16x16x32_bf16 v[112:115], a[92:95], a[188:191], v[112:115]\n"
    "v_fmac_f32 v174, s70, v90\n"
    "v_fmac_f32 v175, s70, v91\n"
    "v_mfma_f32_16x16x32_bf16 v[112:115], a[96:99], a[192:195], v[112:115]\n"
    "v_lshl_add_u32 v5, v6, 2, v5\n"
    "v_lshl_add_u32 v23, v5, 3, s69\n"
    "v_mfma_f32_16x16x32_bf16 v[112:115], a[100:103], a[196:199], v[112:115]\n"
    "s_lshl_b32 s55, s50, 3\n"
    "s_mul_i32 s54, s49, 61440\n"
    "v_lshrrev_b32 v7, 3, v4\n"
    "v_mfma_f32_16x16x32_bf16 v[112:115], a[104:107], a[200:203], v[112:115]\n"
    "v_add_u32 v8, s55, v7\n"
    "v_lshrrev_b32 v5, 2, v8\n"
    "v_mfma_f32_16x16x32_bf16 v[112:115], a[108:111], a[204:207], v[112:115]\n"
    "s_waitcnt lgkmcnt(0)\n"
    "v_and_b32 v6, 3, v4\n"
    "v_xor_b32 v5, v5, v6\n"
    "v_mfma_f32_16x16x32_bf16 v[64:67], a[244:247], v[56:59], 0\n"
    "v_mfma_f32_16x16x32_bf16 v[60:63], a[240:243], v[56:59], 0\n"
    "v_mfma_f32_16x16x32_bf16 v[124:127], a[108:111], a[208:211], v[124:127]\n"
    "buffer_load_dwordx4 a[68:71], v14, s[36:39], s68 offen offset:1024 sc1\n"
    "v_mfma_f32_16x16x32_bf16 v[124:127], a[104:107], a[204:207], v[124:127]\n"
    "ds_read_b64_tr_b16 a[248:249], v16 offset:2816\n"
    "v_fmac_f32 v184, s70, v100\n"
    "v_mfma_f32_16x16x32_bf16 v[124:127], a[100:103], a[200:203], v[124:127]\n"
    "ds_read_b64_tr_b16 a[250:251], v16 offset:3328\n"
    "v_fmac_f32 v185, s70, v101\n"
    "v_mfma_f32_16x16x32_bf16 v[124:127], a[96:99], a[196:199], v[124:127]\n"
    "ds_read_b64_tr_b16 a[252:253], v17 offset:2816\n"
    "v_fmac_f32 v186, s70, v102\n"
    "v_mfma_f32_16x16x32_bf16 v[124:127], a[92:95], a[192:195], v[124:127]\n"
    "ds_read_b64_tr_b16 a[254:255], v17 offset:3328\n"
    "v_fmac_f32 v187, s70, v103\n"
    "v_mfma_f32_16x16x32_bf16 v[124:127], a[88:91], a[188:191], v[124:127]\n"
    "v_lshl_add_u32 v5, v8, 2, v5\n"
    "v_lshlrev_b32 v5, 3, v5\n"
    "v_mfma_f32_16x16x32_bf16 v[124:127], a[84:87], a[184:187], v[124:127]\n"
    "v_cvt_pk_bf16_f32 v78, v64, v65\n"
    "v_cvt_pk_bf16_f32 v79, v66, v67\n"
    "v_mfma_f32_16x16x32_bf16 v[136:139], a[84:87], a[188:191], v[136:139]\n"
    "buffer_load_dwordx4 a[72:75], v14, s[36:39], s68 offen offset:2048 sc1\n"
    "v_mfma_f32_16x16x32_bf16 v[136:139], a[88:91], a[192:195], v[136:139]\n"
    "v_cvt_pk_bf16_f32 v76, v60, v61\n"
    "v_cvt_pk_bf16_f32 v77, v62, v63\n"
    "v_mfma_f32_16x16x32_bf16 v[136:139], a[92:95], a[196:199], v[136:139]\n"
    "ds_write_b32 v12, v78 offset:8\n"
    "v_mfma_f32_16x16x32_bf16 v[136:139], a[96:99], a[200:203], v[136:139]\n"
    "v_fmac_f32 v196, s70, v112\n"
    "v_fmac_f32 v197, s70, v113\n"
    "v_mfma_f32_16x16x32_bf16 v[136:139], a[100:103], a[204:207], v[136:139]\n"
    "ds_write_b32 v12, v79 offset:12\n"
    "v_mfma_f32_16x16x32_bf16 v[136:139], a[104:107], a[208:211], v[136:139]\n"
    "s_waitcnt lgkmcnt(2)\n"
    "v_fmac_f32 v198, s70, v114\n"
    "v_fmac_f32 v199, s70, v115\n"
    "v_mfma_f32_16x16x32_bf16 v[136:139], a[108:111], a[212:215], v[136:139]\n"
    "ds_write_b32 v12, v76 offset:0\n"
    "v_mfma_f32_16x16x32_bf16 v[72:75], a[252:255], v[56:59], 0\n"
    "v_mfma_f32_16x16x32_bf16 v[68:71], a[248:251], v[56:59], 0\n"
    "v_mfma_f32_16x16x32_bf16 v[148:151], a[108:111], a[216:219], v[148:151]\n"
    "buffer_load_dwordx4 a[76:79], v14, s[36:39], s68 offen offset:3072 sc1\n"
    "v_mfma_f32_16x16x32_bf16 v[148:151], a[104:107], a[212:215], v[148:151]\n"
    "ds_write_b32 v12, v77 offset:4\n"
    "v_mfma_f32_16x16x32_bf16 v[148:151], a[100:103], a[208:211], v[148:151]\n"
    "ds_read_b64_tr_b16 a[240:241], v16 offset:5632\n"
    "v_fmac_f32 v208, s70, v124\n"
    "v_mfma_f32_16x16x32_bf16 v[148:151], a[96:99], a[204:207], v[148:151]\n"
    "ds_read_b64_tr_b16 a[242:243], v16 offset:6144\n"
    "v_fmac_f32 v209, s70, v125\n"
    "v_mfma_f32_16x16x32_bf16 v[148:151], a[92:95], a[200:203], v[148:151]\n"
    "ds_read_b64_tr_b16 a[244:245], v17 offset:5632\n"
    "v_fmac_f32 v210, s70, v126\n"
    "v_mfma_f32_16x16x32_bf16 v[148:151], a[88:91], a[196:199], v[148:151]\n"
    "ds_read_b64_tr_b16 a[246:247], v17 offset:6144\n"
    "v_fmac_f32 v211, s70, v127\n"
    "v_mfma_f32_16x16x32_bf16 v[148:151], a[84:87], a[192:195], v[148:151]\n"
    "v_cvt_pk_bf16_f32 v82, v72, v73\n"
    "v_cvt_pk_bf16_f32 v83, v74, v75\n"
    "v_mfma_f32_16x16x32_bf16 v[160:163], a[84:87], a[196:199], v[160:163]\n"
    "s_addk_i32 s68, 0x1000\n"
    "v_cvt_pk_bf16_f32 v80, v68, v69\n"
    "v_cvt_pk_bf16_f32 v81, v70, v71\n"
    "v_mfma_f32_16x16x32_bf16 v[160:163], a[88:91], a[200:203], v[160:163]\n"
    "buffer_load_dwordx4 a[80:83], v14, s[36:39], s68 offen offset:0 sc1\n"
    "v_mfma_f32_16x16x32_bf16 v[160:163], a[92:95], a[204:207], v[160:163]\n"
    "ds_write_b32 v12, v82 offset:4104\n"
    "v_mfma_f32_16x16x32_bf16 v[160:163], a[96:99], a[208:211], v[160:163]\n"
    "v_fmac_f32 v220, s70, v136\n"
    "v_fmac_f32 v221, s70, v137\n"
    "v_mfma_f32_16x16x32_bf16 v[160:163], a[100:103], a[212:215], v[160:163]\n"
    "ds_write_b32 v12, v83 offset:4108\n"
    "v_mfma_f32_16x16x32_bf16 v[160:163], a[104:107], a[216:219], v[160:163]\n"
    "s_waitcnt lgkmcnt(2)\n"
    "v_fmac_f32 v222, s70, v138\n"
    "v_fmac_f32 v223, s70, v139\n"
    "v_mfma_f32_16x16x32_bf16 v[160:163], a[108:111], a[220:223], v[160:163]\n"
    "ds_write_b32 v12, v80 offset:4096\n"
    "v_mfma_f32_16x16x32_bf16 v[64:67], a[244:247], v[56:59], 0\n"
    "v_mfma_f32_16x16x32_bf16 v[60:63], a[240:243], v[56:59], 0\n"
    "s_waitcnt vmcnt(28)\n"
    "v_mfma_f32_16x16x32_bf16 v[92:95], a[112:115], a[172:175], v[92:95]\n"
    "s_add_u32 s68, s63, 516096\n"
    "v_fmac_f32 v232, s70, v148\n"
    "v_fmac_f32 v233, s70, v149\n"
    "v_mfma_f32_16x16x32_bf16 v[92:95], a[116:119], a[176:179], v[92:95]\n"
    "buffer_load_dwordx4 a[84:87], v14, s[36:39], s68 offen offset:0 sc1\n"
    "v_mfma_f32_16x16x32_bf16 v[92:95], a[120:123], a[180:183], v[92:95]\n"
    "ds_write_b32 v12, v81 offset:4100\n"
    "v_mfma_f32_16x16x32_bf16 v[92:95], a[124:127], a[184:187], v[92:95]\n"
    "ds_read_b64_tr_b16 a[248:249], v16 offset:8448\n"
    "v_fmac_f32 v234, s70, v150\n"
    "v_mfma_f32_16x16x32_bf16 v[92:95], a[128:131], a[188:191], v[92:95]\n"
    "ds_read_b64_tr_b16 a[250:251], v16 offset:8960\n"
    "v_fmac_f32 v235, s70, v151\n"
    "v_mfma_f32_16x16x32_bf16 v[92:95], a[132:135], a[192:195], v[92:95]\n"
    "ds_read_b64_tr_b16 a[252:253], v17 offset:8448\n"
    "v_bfe_u32 v6, v4, 2, 1\n"
    "v_mfma_f32_16x16x32_bf16 v[92:95], a[136:139], a[196:199], v[92:95]\n"
    "v_cvt_pk_bf16_f32 v78, v64, v65\n"
    "v_cvt_pk_bf16_f32 v79, v66, v67\n"
    "v_mfma_f32_16x16x32_bf16 v[104:107], a[136:139], a[200:203], v[104:107]\n"
    "buffer_load_dwordx4 a[88:91], v14, s[36:39], s68 offen offset:1024 sc1\n"
    "v_mfma_f32_16x16x32_bf16 v[104:107], a[132:135], a[196:199], v[104:107]\n"
    "v_cvt_pk_bf16_f32 v76, v60, v61\n"
    "v_cvt_pk_bf16_f32 v77, v62, v63\n"
    "v_mfma_f32_16x16x32_bf16 v[104:107], a[128:131], a[192:195], v[104:107]\n"
    "ds_write_b32 v12, v78 offset:8200\n"
    "v_mfma_f32_16x16x32_bf16 v[104:107], a[124:127], a[188:191], v[104:107]\n"
    "ds_read_b64_tr_b16 a[254:255], v17 offset:8960\n"
    "v_fmac_f32 v244, s70, v160\n"
    "v_mfma_f32_16x16x32_bf16 v[104:107], a[120:123], a[184:187], v[104:107]\n"
    "ds_write_b32 v12, v79 offset:8204\n"
    "v_mfma_f32_16x16x32_bf16 v[104:107], a[116:119], a[180:183], v[104:107]\n"
    "v_fmac_f32 v245, s70, v161\n"
    "v_fmac_f32 v246, s70, v162\n"
    "v_mfma_f32_16x16x32_bf16 v[104:107], a[112:115], a[176:179], v[104:107]\n"
    "ds_write_b32 v12, v76 offset:8192\n"
    "v_mfma_f32_16x16x32_bf16 v[116:119], a[112:115], a[180:183], v[116:119]\n"
    "buffer_load_dwordx4 a[92:95], v14, s[36:39], s68 offen offset:2048 sc1\n"
    "v_mfma_f32_16x16x32_bf16 v[116:119], a[116:119], a[184:187], v[116:119]\n"
    "ds_write_b32 v12, v77 offset:8196\n"
    "v_mfma_f32_16x16x32_bf16 v[116:119], a[120:123], a[188:191], v[116:119]\n"
    "ds_read_b64_tr_b16 a[240:241], v16 offset:11264\n"
    "v_fmac_f32 v247, s70, v163\n"
    "v_mfma_f32_16x16x32_bf16 v[116:119], a[124:127], a[192:195], v[116:119]\n"
    "ds_read_b64_tr_b16 a[242:243], v16 offset:11776\n"
    "v_fmac_f32 v176, s70, v92\n"
    "v_mfma_f32_16x16x32_bf16 v[116:119], a[128:131], a[196:199], v[116:119]\n"
    "ds_read_b64_tr_b16 a[244:245], v17 offset:11264\n"
    "v_fmac_f32 v177, s70, v93\n"
    "v_mfma_f32_16x16x32_bf16 v[116:119], a[132:135], a[200:203], v[116:119]\n"
    "ds_read_b64_tr_b16 a[246:247], v17 offset:11776\n"
    "v_fmac_f32 v178, s70, v94\n"
    "v_mfma_f32_16x16x32_bf16 v[116:119], a[136:139], a[204:207], v[116:119]\n"
    "s_waitcnt lgkmcnt(6)\n"
    "v_fmac_f32 v179, s70, v95\n"
    "v_mul_u32_u24 v6, 30720, v6\n"
    "v_mfma_f32_16x16x32_bf16 v[72:75], a[252:255], v[56:59], 0\n"
    "v_mfma_f32_16x16x32_bf16 v[68:71], a[248:251], v[56:59], 0\n"
    "v_mfma_f32_16x16x32_bf16 v[128:131], a[136:139], a[208:211], v[128:131]\n"
    "buffer_load_dwordx4 a[96:99], v14, s[36:39], s68 offen offset:3072 sc1\n"
    "v_mfma_f32_16x16x32_bf16 v[128:131], a[132:135], a[204:207], v[128:131]\n"
    "v_fmac_f32 v188, s70, v104\n"
    "v_fmac_f32 v189, s70, v105\n"
    "v_mfma_f32_16x16x32_bf16 v[128:131], a[128:131], a[200:203], v[128:131]\n"
    "v_fmac_f32 v190, s70, v106\n"
    "v_fmac_f32 v191, s70, v107\n"
    "v_mfma_f32_16x16x32_bf16 v[128:131], a[124:127], a[196:199], v[128:131]\n"
    "s_waitcnt lgkmcnt(0)\n"
    "v_add3_u32 v24, v5, v6, s54\n"
    "v_mfma_f32_16x16x32_bf16 v[128:131], a[120:123], a[192:195], v[128:131]\n"
    "s_mul_i32 s54, s52, 86016\n"
    "s_add_u32 s55, s53, s49\n"
    "s_add_u32 s55, s55, 3\n"
    "v_mfma_f32_16x16x32_bf16 v[128:131], a[116:119], a[188:191], v[128:131]\n"
    "s_mul_i32 s55, s55, 384\n"
    "s_add_u32 s54, s54, s55\n"
    "s_add_u32 s54, s54, s58\n"
    "v_mfma_f32_16x16x32_bf16 v[128:131], a[112:115], a[184:187], v[128:131]\n"
    "s_lshl_b32 s55, s50, 7\n"
    "v_cvt_pk_bf16_f32 v82, v72, v73\n"
    "v_cvt_pk_bf16_f32 v83, v74, v75\n"
    "v_mfma_f32_16x16x32_bf16 v[64:67], a[244:247], v[56:59], 0\n"
    "v_mfma_f32_16x16x32_bf16 v[60:63], a[240:243], v[56:59], 0\n"
    "v_mfma_f32_16x16x32_bf16 v[140:143], a[112:115], a[188:191], v[140:143]\n"
    "s_addk_i32 s68, 0x1000\n"
    "v_cvt_pk_bf16_f32 v80, v68, v69\n"
    "v_cvt_pk_bf16_f32 v81, v70, v71\n"
    "v_mfma_f32_16x16x32_bf16 v[140:143], a[116:119], a[192:195], v[140:143]\n"
    "buffer_load_dwordx4 a[100:103], v14, s[36:39], s68 offen offset:0 sc1\n"
    "v_mfma_f32_16x16x32_bf16 v[140:143], a[120:123], a[196:199], v[140:143]\n"
    "s_add_u32 s55, s55, s54\n"
    "s_mul_i32 s56, s50, 602112\n"
    "ds_write_b32 v12, v82 offset:12296\n"
    "v_mfma_f32_16x16x32_bf16 v[140:143], a[124:127], a[200:203], v[140:143]\n"
    ".Lpad4:\n"
    "s_cbranch_execz .Lhop_done\n"
    "s_add_u32 s56, s56, s54\n"
    "v_add_u32 v37, 1, v37\n"
    "v_fmac_f32 v200, s70, v116\n"
    "v_mfma_f32_16x16x32_bf16 v[140:143], a[128:131], a[204:207], v[140:143]\n"
    "s_add_u32 s56, s56, 64\n"
    "ds_write_b32 v12, v83 offset:12300\n"
    "v_mfma_f32_16x16x32_bf16 v[140:143], a[132:135], a[208:211], v[140:143]\n"
    "v_fmac_f32 v201, s70, v117\n"
    "v_fmac_f32 v202, s70, v118\n"
    "v_mfma_f32_16x16x32_bf16 v[140:143], a[136:139], a[212:215], v[140:143]\n"
    "ds_write_b32 v12, v80 offset:12288\n"
    "v_mfma_f32_16x16x32_bf16 v[152:155], a[136:139], a[216:219], v[152:155]\n"
    "buffer_load_dwordx4 a[104:107], v14, s[36:39], s68 offen offset:1024 sc1\n"
    "v_mfma_f32_16x16x32_bf16 v[152:155], a[132:135], a[212:215], v[152:155]\n"
    "ds_write_b32 v12, v81 offset:12292\n"
    "v_mfma_f32_16x16x32_bf16 v[152:155], a[128:131], a[208:211], v[152:155]\n"
    "v_cvt_pk_bf16_f32 v78, v64, v65\n"
    "v_cvt_pk_bf16_f32 v79, v66, v67\n"
    "v_mfma_f32_16x16x32_bf16 v[152:155], a[124:127], a[204:207], v[152:155]\n"
    "v_cvt_pk_bf16_f32 v76, v60, v61\n"
    "v_cvt_pk_bf16_f32 v77, v62, v63\n"
    "v_mfma_f32_16x16x32_bf16 v[152:155], a[120:123], a[200:203], v[152:155]\n"
    "ds_write_b32 v12, v78 offset:16392\n"
    "v_mfma_f32_16x16x32_bf16 v[152:155], a[116:119], a[196:199], v[152:155]\n"
    "v_fmac_f32 v203, s70, v119\n"
    "v_fmac_f32 v212, s70, v128\n"
    "v_mfma_f32_16x16x32_bf16 v[152:155], a[112:115], a[192:195], v[152:155]\n"
    "ds_write_b32 v12, v79 offset:16396\n"
    "v_mfma_f32_16x16x32_bf16 v[164:167], a[112:115], a[196:199], v[164:167]\n"
    "buffer_load_dwordx4 a[108:111], v14, s[36:39], s68 offen offset:2048 sc1\n"
    "v_mfma_f32_16x16x32_bf16 v[164:167], a[116:119], a[200:203], v[164:167]\n"
    "ds_write_b32 v12, v76 offset:16384\n"
    "v_mfma_f32_16x16x32_bf16 v[164:167], a[120:123], a[204:207], v[164:167]\n"
    "v_fmac_f32 v213, s70, v129\n"
    "v_fmac_f32 v214, s70, v130\n"
    "v_mfma_f32_16x16x32_bf16 v[164:167], a[124:127], a[208:211], v[164:167]\n"
    "ds_write_b32 v12, v77 offset:16388\n"
    "v_mfma_f32_16x16x32_bf16 v[164:167], a[128:131], a[212:215], v[164:167]\n"
    "ds_write_b32 v35, v37\n"
    "v_mfma_f32_16x16x32_bf16 v[164:167], a[132:135], a[216:219], v[164:167]\n"
    "s_waitcnt vmcnt(28)\n"
    "v_fmac_f32 v215, s70, v131\n"
    "v_fmac_f32 v224, s70, v140\n"
    "v_mfma_f32_16x16x32_bf16 v[164:167], a[136:139], a[220:223], v[164:167]\n"
    "v_fmac_f32 v225, s70, v141\n"
    "v_fmac_f32 v226, s70, v142\n"
    "v_mfma_f32_16x16x32_bf16 v[96:99], a[140:143], a[172:175], v[96:99]\n"
    "buffer_load_dwordx4 a[112:115], v14, s[36:39], s68 offen offset:3072 sc1\n"
    "v_mfma_f32_16x16x32_bf16 v[96:99], a[144:147], a[176:179], v[96:99]\n"
    "v_fmac_f32 v227, s70, v143\n"
    "v_fmac_f32 v236, s70, v152\n"
    "v_mfma_f32_16x16x32_bf16 v[96:99], a[148:151], a[180:183], v[96:99]\n"
    "v_fmac_f32 v237, s70, v153\n"
    "v_fmac_f32 v238, s70, v154\n"
    "v_mfma_f32_16x16x32_bf16 v[96:99], a[152:155], a[184:187], v[96:99]\n"
    "v_fmac_f32 v239, s70, v155\n"
    "v_lshrrev_b32 v5, 5, v4\n"
    "v_mfma_f32_16x16x32_bf16 v[96:99], a[156:159], a[188:191], v[96:99]\n"
    "v_mul_u32_u24 v5, 602112, v5\n"
    "v_bfe_u32 v6, v4, 2, 3\n"
    "v_mfma_f32_16x16x32_bf16 v[96:99], a[160:163], a[192:195], v[96:99]\n"
    "ds_read_b32 v38, v36 offset:30592\n"
    "v_mul_u32_u24 v6, 768, v6\n"
    "v_mfma_f32_16x16x32_bf16 v[96:99], a[164:167], a[196:199], v[96:99]\n"
    "v_add3_u32 v5, v5, v6, s55\n"
    "v_and_b32 v6, 3, v4\n"
    "v_mfma_f32_16x16x32_bf16 v[108:111], a[164:167], a[200:203], v[108:111]\n"
    "s_addk_i32 s68, 0x1000\n"
    "v_fmac_f32 v248, s70, v164\n"
    "v_fmac_f32 v249, s70, v165\n"
    "v_mfma_f32_16x16x32_bf16 v[108:111], a[160:163], a[196:199], v[108:111]\n"
    "buffer_load_dwordx4 a[116:119], v14, s[36:39], s68 offen offset:0 sc1\n"
    "v_mfma_f32_16x16x32_bf16 v[108:111], a[156:159], a[192:195], v[108:111]\n"
    "v_fmac_f32 v250, s70, v166\n"
    "v_fmac_f32 v251, s70, v167\n"
    "v_mfma_f32_16x16x32_bf16 v[108:111], a[152:155], a[188:191], v[108:111]\n"
    "s_waitcnt lgkmcnt(0)\n"
    "v_cmp_gt_u32 vcc, 8, v38\n"
    "v_lshl_add_u32 v25, v6, 4, v5\n"
    "v_mfma_f32_16x16x32_bf16 v[108:111], a[148:151], a[184:187], v[108:111]\n"
    "v_mul_u32_u24 v7, 768, v7\n"
    "v_and_b32 v6, 7, v4\n"
    "v_mfma_f32_16x16x32_bf16 v[108:111], a[144:147], a[180:183], v[108:111]\n"
    "s_cbranch_vccnz .Lsl8\n"
    ".Lbk8:\n"
    "v_mfma_f32_16x16x32_bf16 v[108:111], a[140:143], a[176:179], v[108:111]\n"
    "ds_read_b128 a[224:227], v13 offset:0\n"
    "v_lshlrev_b32 v6, 3, v6\n"
    "v_mfma_f32_16x16x32_bf16 v[120:123], a[140:143], a[180:183], v[120:123]\n"
    "buffer_load_dwordx4 a[120:123], v14, s[36:39], s68 offen offset:1024 sc1\n"
    "v_mfma_f32_16x16x32_bf16 v[120:123], a[144:147], a[184:187], v[120:123]\n"
    "ds_read_b128 a[228:231], v13 offset:1024\n"
    "v_fmac_f32 v180, s70, v96\n"
    "v_mfma_f32_16x16x32_bf16 v[120:123], a[148:151], a[188:191], v[120:123]\n"
    "ds_read_b128 a[232:235], v13 offset:2048\n"
    "v_fmac_f32 v181, s70, v97\n"
    "v_mfma_f32_16x16x32_bf16 v[120:123], a[152:155], a[192:195], v[120:123]\n"
    "ds_read_b128 a[236:239], v13 offset:3072\n"
    "v_fmac_f32 v182, s70, v98\n"
    "v_mfma_f32_16x16x32_bf16 v[120:123], a[156:159], a[196:199], v[120:123]\n"
    "ds_read_b128 a[168:171], v13 offset:4096\n"
    "v_fmac_f32 v183, s70, v99\n"
    "v_mfma_f32_16x16x32_bf16 v[120:123], a[160:163], a[200:203], v[120:123]\n"
    "ds_read_b128 a[172:175], v13 offset:5120\n"
    "v_add3_u32 v26, v7, v6, s56\n"
    "v_mfma_f32_16x16x32_bf16 v[120:123], a[164:167], a[204:207], v[120:123]\n"
    "s_lshl_b32 s54, %[bid], 7\n"
    "s_add_u32 s54, s54, 4096\n"
    "ds_read_b128 a[176:179], v13 offset:6144\n"
    "v_mfma_f32_16x16x32_bf16 v[132:135], a[164:167], a[208:211], v[132:135]\n"
    "buffer_load_dwordx4 a[124:127], v14, s[36:39], s68 offen offset:2048 sc1\n"
    "v_mfma_f32_16x16x32_bf16 v[132:135], a[160:163], a[204:207], v[132:135]\n"
    "ds_read_b128 a[180:183], v13 offset:7168\n"
    "v_fmac_f32 v192, s70, v108\n"
    "v_mfma_f32_16x16x32_bf16 v[132:135], a[156:159], a[200:203], v[132:135]\n"
    "v_fmac_f32 v193, s70, v109\n"
    "v_fmac_f32 v194, s70, v110\n"
    "v_mfma_f32_16x16x32_bf16 v[132:135], a[152:155], a[196:199], v[132:135]\n"
    "v_fmac_f32 v195, s70, v111\n"
    "v_mov_b32 v5, s54\n"
    "v_mfma_f32_16x16x32_bf16 v[132:135], a[148:151], a[192:195], v[132:135]\n"
    "v_mov_b32 v6, 3221225472\n"
    "v_cmp_eq_u32 s[76:77], 0, %[tid]\n"
    "v_mfma_f32_16x16x32_bf16 v[132:135], a[144:147], a[188:191], v[132:135]\n"
    "v_mfma_f32_16x16x32_bf16 v[132:135], a[140:143], a[184:187], v[132:135]\n"
    "ds_read_b128 a[184:187], v13 offset:8192\n"
    "v_cndmask_b32 v15, v6, v5, s[76:77]\n"
    "v_mfma_f32_16x16x32_bf16 v[144:147], a[140:143], a[188:191], v[144:147]\n"
    "buffer_load_dwordx4 a[128:131], v14, s[36:39], s68 offen offset:3072 sc1\n"
    "v_mfma_f32_16x16x32_bf16 v[144:147], a[144:147], a[192:195], v[144:147]\n"
    "ds_read_b128 a[188:191], v13 offset:9216\n"
    "v_fmac_f32 v204, s70, v120\n"
    "v_mfma_f32_16x16x32_bf16 v[144:147], a[148:151], a[196:199], v[144:147]\n"
    "v_fmac_f32 v205, s70, v121\n"
    "v_fmac_f32 v206, s70, v122\n"
    "v_mfma_f32_16x16x32_bf16 v[144:147], a[152:155], a[200:203], v[144:147]\n"
    "v_fmac_f32 v207, s70, v123\n"
    "v_mfma_f32_16x16x32_bf16 v[144:147], a[156:159], a[204:207], v[144:147]\n"
    "v_mfma_f32_16x16x32_bf16 v[144:147], a[160:163], a[208:211], v[144:147]\n"
    "v_mfma_f32_16x16x32_bf16 v[144:147], a[164:167], a[212:215], v[144:147]\n"
    "v_mfma_f32_16x16x32_bf16 v[156:159], a[164:167], a[216:219], v[156:159]\n"
    "s_addk_i32 s68, 0x1000\n"
    "v_fmac_f32 v216, s70, v132\n"
    "v_fmac_f32 v217, s70, v133\n"
    "v_mfma_f32_16x16x32_bf16 v[156:159], a[160:163], a[212:215], v[156:159]\n"
    "buffer_load_dwordx4 a[132:135], v14, s[36:39], s68 offen offset:0 sc1\n"
    "v_mfma_f32_16x16x32_bf16 v[156:159], a[156:159], a[208:211], v[156:159]\n"
    "v_fmac_f32 v218, s70, v134\n"
    "v_fmac_f32 v219, s70, v135\n"
    "v_mfma_f32_16x16x32_bf16 v[156:159], a[152:155], a[204:207], v[156:159]\n"
    "v_mfma_f32_16x16x32_bf16 v[156:159], a[148:151], a[200:203], v[156:159]\n"
    "v_mfma_f32_16x16x32_bf16 v[156:159], a[144:147], a[196:199], v[156:159]\n"
    "v_mfma_f32_16x16x32_bf16 v[156:159], a[140:143], a[192:195], v[156:159]\n"
    "ds_read_b128 a[192:195], v13 offset:10240\n"
    "v_mfma_f32_16x16x32_bf16 v[168:171], a[140:143], a[196:199], v[168:171]\n"
    "buffer_load_dwordx4 a[136:139], v14, s[36:39], s68 offen offset:1024 sc1\n"
    "v_mfma_f32_16x16x32_bf16 v[168:171], a[144:147], a[200:203], v[168:171]\n"
    "ds_read_b128 a[196:199], v13 offset:11264\n"
    "v_fmac_f32 v228, s70, v144\n"
    "v_mfma_f32_16x16x32_bf16 v[168:171], a[148:151], a[204:207], v[168:171]\n"
    "ds_read_b128 a[200:203], v13 offset:12288\n"
    "v_fmac_f32 v229, s70, v145\n"
    "v_mfma_f32_16x16x32_bf16 v[168:171], a[152:155], a[208:211], v[168:171]\n"
    "v_fmac_f32 v230, s70, v146\n"
    "v_fmac_f32 v231, s70, v147\n"
    "v_mfma_f32_16x16x32_bf16 v[168:171], a[156:159], a[212:215], v[168:171]\n"
    "v_mfma_f32_16x16x32_bf16 v[168:171], a[160:163], a[216:219], v[168:171]\n"
    "v_mfma_f32_16x16x32_bf16 v[168:171], a[164:167], a[220:223], v[168:171]\n"
    "s_waitcnt lgkmcnt(3)\n"
    "s_waitcnt vmcnt(28)\n"
    "v_mfma_f32_16x16x32_bf16 v[88:91], a[0:3], a[224:227], v[88:91]\n"
    "buffer_load_dwordx4 a[140:143], v14, s[36:39], s68 offen offset:2048 sc1\n"
    "v_mfma_f32_16x16x32_bf16 v[88:91], a[4:7], a[228:231], v[88:91]\n"
    "v_fmac_f32 v240, s70, v156\n"
    "v_fmac_f32 v241, s70, v157\n"
    "v_mfma_f32_16x16x32_bf16 v[88:91], a[8:11], a[232:235], v[88:91]\n"
    "v_fmac_f32 v242, s70, v158\n"
    "v_fmac_f32 v243, s70, v159\n"
    "v_mfma_f32_16x16x32_bf16 v[88:91], a[12:15], a[236:239], v[88:91]\n"
    "v_mfma_f32_16x16x32_bf16 v[88:91], a[16:19], a[168:171], v[88:91]\n"
    "v_mfma_f32_16x16x32_bf16 v[88:91], a[20:23], a[172:175], v[88:91]\n"
    "v_mfma_f32_16x16x32_bf16 v[88:91], a[24:27], a[176:179], v[88:91]\n"
    "v_mfma_f32_16x16x32_bf16 v[100:103], a[24:27], a[180:183], v[100:103]\n"
    "buffer_load_dwordx4 a[144:147], v14, s[36:39], s68 offen offset:3072 sc1\n"
    "v_mfma_f32_16x16x32_bf16 v[100:103], a[20:23], a[176:179], v[100:103]\n"
    "ds_read_b64_tr_b16 a[240:241], v18 offset:0\n"
    "v_fmac_f32 v252, s70, v168\n"
    "v_mfma_f32_16x16x32_bf16 v[100:103], a[16:19], a[172:175], v[100:103]\n"
    "ds_read_b64_tr_b16 a[242:243], v18 offset:512\n"
    "v_fmac_f32 v253, s70, v169\n"
    "v_mfma_f32_16x16x32_bf16 v[100:103], a[12:15], a[168:171], v[100:103]\n"
    "ds_read_b64_tr_b16 a[244:245], v19 offset:0\n"
    "v_fmac_f32 v254, s70, v170\n"
    "v_mfma_f32_16x16x32_bf16 v[100:103], a[8:11], a[236:239], v[100:103]\n"
    "ds_read_b64_tr_b16 a[246:247], v19 offset:512\n"
    "v_fmac_f32 v255, s70, v171\n"
    "v_mfma_f32_16x16x32_bf16 v[100:103], a[4:7], a[232:235], v[100:103]\n"
    "v_mfma_f32_16x16x32_bf16 v[100:103], a[0:3], a[228:231], v[100:103]\n"
    "v_mfma_f32_16x16x32_bf16 v[112:115], a[0:3], a[232:235], v[112:115]\n"
    "s_addk_i32 s68, 0x1000\n"
    "v_mfma_f32_16x16x32_bf16 v[112:115], a[4:7], a[236:239], v[112:115]\n"
    "buffer_load_dwordx4 a[148:151], v14, s[36:39], s68 offen offset:0 sc1\n"
    "v_mfma_f32_16x16x32_bf16 v[112:115], a[8:11], a[168:171], v[112:115]\n"
    "v_mfma_f32_16x16x32_bf16 v[112:115], a[12:15], a[172:175], v[112:115]\n"
    "v_mfma_f32_16x16x32_bf16 v[112:115], a[16:19], a[176:179], v[112:115]\n"
    "v_mfma_f32_16x16x32_bf16 v[112:115], a[20:23], a[180:183], v[112:115]\n"
    "v_mfma_f32_16x16x32_bf16 v[112:115], a[24:27], a[184:187], v[112:115]\n"
    "s_waitcnt lgkmcnt(0)\n"
    "v_mfma_f32_16x16x32_bf16 v[64:67], a[244:247], v[56:59], 0\n"
    "v_mfma_f32_16x16x32_bf16 v[60:63], a[240:243], v[56:59], 0\n"
    "v_mfma_f32_16x16x32_bf16 v[124:127], a[24:27], a[188:191], v[124:127]\n"
    "buffer_load_dwordx4 a[152:155], v14, s[36:39], s68 offen offset:1024 sc1\n"
    "v_mfma_f32_16x16x32_bf16 v[124:127], a[20:23], a[184:187], v[124:127]\n"
    "ds_read_b64_tr_b16 a[248:249], v18 offset:2816\n"
    "v_mfma_f32_16x16x32_bf16 v[124:127], a[16:19], a[180:183], v[124:127]\n"
    "ds_read_b64_tr_b16 a[250:251], v18 offset:3328\n"
    "v_mfma_f32_16x16x32_bf16 v[124:127], a[12:15], a[176:179], v[124:127]\n"
    "ds_read_b64_tr_b16 a[252:253], v19 offset:2816\n"
    "v_mfma_f32_16x16x32_bf16 v[124:127], a[8:11], a[172:175], v[124:127]\n"
    "ds_read_b64_tr_b16 a[254:255], v19 offset:3328\n"
    "v_mfma_f32_16x16x32_bf16 v[124:127], a[4:7], a[168:171], v[124:127]\n"
    "v_mfma_f32_16x16x32_bf16 v[124:127], a[0:3], a[236:239], v[124:127]\n"
    "v_cvt_pk_bf16_f32 v78, v64, v65\n"
    "v_cvt_pk_bf16_f32 v79, v66, v67\n"
    "v_mfma_f32_16x16x32_bf16 v[136:139], a[0:3], a[168:171], v[136:139]\n"
    "buffer_load_dwordx4 a[156:159], v14, s[36:39], s68 offen offset:2048 sc1\n"
    "v_mfma_f32_16x16x32_bf16 v[136:139], a[4:7], a[172:175], v[136:139]\n"
    "v_cvt_pk_bf16_f32 v76, v60, v61\n"
    "v_cvt_pk_bf16_f32 v77, v62, v63\n"
    "v_mfma_f32_16x16x32_bf16 v[136:139], a[8:11], a[176:179], v[136:139]\n"
    "ds_write_b32 v12, v78 offset:20488\n"
    "v_mfma_f32_16x16x32_bf16 v[136:139], a[12:15], a[180:183], v[136:139]\n"
    "v_mfma_f32_16x16x32_bf16 v[136:139], a[16:19], a[184:187], v[136:139]\n"
    "ds_write_b32 v12, v79 offset:20492\n"
    "v_mfma_f32_16x16x32_bf16 v[136:139], a[20:23], a[188:191], v[136:139]\n"
    "s_waitcnt lgkmcnt(2)\n"
    "v_mfma_f32_16x16x32_bf16 v[136:139], a[24:27], a[192:195], v[136:139]\n"
    "ds_write_b32 v12, v76 offset:20480\n"
    "v_mfma_f32_16x16x32_bf16 v[72:75], a[252:255], v[56:59], 0\n"
    "v_mfma_f32_16x16x32_bf16 v[68:71], a[248:251], v[56:59], 0\n"
    "v_mfma_f32_16x16x32_bf16 v[148:151], a[24:27], a[196:199], v[148:151]\n"
    "buffer_load_dwordx4 a[160:163], v14, s[36:39], s68 offen offset:3072 sc1\n"
    "v_mfma_f32_16x16x32_bf16 v[148:151], a[20:23], a[192:195], v[148:151]\n"
    "ds_write_b32 v12, v77 offset:20484\n"
    "v_mfma_f32_16x16x32_bf16 v[148:151], a[16:19], a[188:191], v[148:151]\n"
    "ds_read_b64_tr_b16 a[240:241], v18 offset:5632\n"
    "v_mfma_f32_16x16x32_bf16 v[148:151], a[12:15], a[184:187], v[148:151]\n"
    "ds_read_b64_tr_b16 a[242:243], v18 offset:6144\n"
    "v_mfma_f32_16x16x32_bf16 v[148:151], a[8:11], a[180:183], v[148:151]\n"
    "ds_read_b64_tr_b16 a[244:245], v19 offset:5632\n"
    "v_mfma_f32_16x16x32_bf16 v[148:151], a[4:7], a[176:179], v[148:151]\n"
    "ds_read_b64_tr_b16 a[246:247], v19 offset:6144\n"
    "v_mfma_f32_16x16x32_bf16 v[148:151], a[0:3], a[172:175], v[148:151]\n"
    "v_cvt_pk_bf16_f32 v82, v72, v73\n"
    "v_cvt_pk_bf16_f32 v83, v74, v75\n"
    "v_mfma_f32_16x16x32_bf16 v[160:163], a[0:3], a[176:179], v[160:163]\n"
    "s_addk_i32 s68, 0x1000\n"
    "v_cvt_pk_bf16_f32 v80, v68, v69\n"
    "v_cvt_pk_bf16_f32 v81, v70, v71\n"
    "v_mfma_f32_16x16x32_bf16 v[160:163], a[4:7], a[180:183], v[160:163]\n"
    "buffer_load_dwordx4 a[164:167], v14, s[36:39], s68 offen offset:0 sc1\n"
    "v_mfma_f32_16x16x32_bf16 v[160:163], a[8:11], a[184:187], v[160:163]\n"
    "ds_write_b32 v12, v82 offset:24584\n"
    "v_mfma_f32_16x16x32_bf16 v[160:163], a[12:15], a[188:191], v[160:163]\n"
    "v_mfma_f32_16x16x32_bf16 v[160:163], a[16:19], a[192:195], v[160:163]\n"
    "ds_write_b32 v12, v83 offset:24588\n"
    "v_mfma_f32_16x16x32_bf16 v[160:163], a[20:23], a[196:199], v[160:163]\n"
    "s_waitcnt lgkmcnt(2)\n"
    "v_mfma_f32_16x16x32_bf16 v[160:163], a[24:27], a[200:203], v[160:163]\n"
    "ds_write_b32 v12, v80 offset:24576\n"
    "v_mfma_f32_16x16x32_bf16 v[64:67], a[244:247], v[56:59], 0\n"
    "v_mfma_f32_16x16x32_bf16 v[60:63], a[240:243], v[56:59], 0\n"
    "s_waitcnt vmcnt(28)\n"
    "v_mfma_f32_16x16x32_bf16 v[92:95], a[28:31], a[224:227], v[92:95]\n"
    "s_add_u32 s68, s64, 516096\n"
    "v_mfma_f32_16x16x32_bf16 v[92:95], a[32:35], a[228:231], v[92:95]\n"
    "buffer_load_dwordx4 a[0:3], v14, s[36:39], s68 offen offset:0 sc1\n"
    "v_mfma_f32_16x16x32_bf16 v[92:95], a[36:39], a[232:235], v[92:95]\n"
    "ds_write_b32 v12, v81 offset:24580\n"
    "v_mfma_f32_16x16x32_bf16 v[92:95], a[40:43], a[236:239], v[92:95]\n"
    "ds_read_b64_tr_b16 a[248:249], v18 offset:8448\n"
    "v_mfma_f32_16x16x32_bf16 v[92:95], a[44:47], a[168:171], v[92:95]\n"
    "ds_read_b64_tr_b16 a[250:251], v18 offset:8960\n"
    "v_mfma_f32_16x16x32_bf16 v[92:95], a[48:51], a[172:175], v[92:95]\n"
    "ds_read_b64_tr_b16 a[252:253], v19 offset:8448\n"
    "v_mfma_f32_16x16x32_bf16 v[92:95], a[52:55], a[176:179], v[92:95]\n"
    "v_cvt_pk_bf16_f32 v78, v64, v65\n"
    "v_cvt_pk_bf16_f32 v79, v66, v67\n"
    "v_mfma_f32_16x16x32_bf16 v[104:107], a[52:55], a[180:183], v[104:107]\n"
    "buffer_load_dwordx4 a[4:7], v14, s[36:39], s68 offen offset:1024 sc1\n"
    "v_mfma_f32_16x16x32_bf16 v[104:107], a[48:51], a[176:179], v[104:107]\n"
    "v_cvt_pk_bf16_f32 v76, v60, v61\n"
    "v_cvt_pk_bf16_f32 v77, v62, v63\n"
    "v_mfma_f32_16x16x32_bf16 v[104:107], a[44:47], a[172:175], v[104:107]\n"
    "ds_write_b32 v12, v78 offset:28680\n"
    "v_mfma_f32_16x16x32_bf16 v[104:107], a[40:43], a[168:171], v[104:107]\n"
    "ds_read_b64_tr_b16 a[254:255], v19 offset:8960\n"
    "v_mfma_f32_16x16x32_bf16 v[104:107], a[36:39], a[236:239], v[104:107]\n"
    "ds_write_b32 v12, v79 offset:28684\n"
    "v_mfma_f32_16x16x32_bf16 v[104:107], a[32:35], a[232:235], v[104:107]\n"
    "v_mfma_f32_16x16x32_bf16 v[104:107], a[28:31], a[228:231], v[104:107]\n"
    "ds_write_b32 v12, v76 offset:28672\n"
    "v_mfma_f32_16x16x32_bf16 v[116:119], a[28:31], a[232:235], v[116:119]\n"
    "buffer_load_dwordx4 a[8:11], v14, s[36:39], s68 offen offset:2048 sc1\n"
    "v_mfma_f32_16x16x32_bf16 v[116:119], a[32:35], a[236:239], v[116:119]\n"
    "ds_write_b32 v12, v77 offset:28676\n"
    "v_mfma_f32_16x16x32_bf16 v[116:119], a[36:39], a[168:171], v[116:119]\n"
    "ds_read_b64_tr_b16 a[240:241], v18 offset:11264\n"
    "v_mfma_f32_16x16x32_bf16 v[116:119], a[40:43], a[172:175], v[116:119]\n"
    "ds_read_b64_tr_b16 a[242:243], v18 offset:11776\n"
    "v_mfma_f32_16x16x32_bf16 v[116:119], a[44:47], a[176:179], v[116:119]\n"
    "ds_read_b64_tr_b16 a[244:245], v19 offset:11264\n"
    "v_mfma_f32_16x16x32_bf16 v[116:119], a[48:51], a[180:183], v[116:119]\n"
    "ds_read_b64_tr_b16 a[246:247], v19 offset:11776\n"
    "v_mfma_f32_16x16x32_bf16 v[116:119], a[52:55], a[184:187], v[116:119]\n"
    "s_waitcnt lgkmcnt(6)\n"
    "v_mfma_f32_16x16x32_bf16 v[72:75], a[252:255], v[56:59], 0\n"
    "v_mfma_f32_16x16x32_bf16 v[68:71], a[248:251], v[56:59], 0\n"
    "v_mfma_f32_16x16x32_bf16 v[128:131], a[52:55], a[188:191], v[128:131]\n"
    "buffer_load_dwordx4 a[12:15], v14, s[36:39], s68 offen offset:3072 sc1\n"
    "v_mfma_f32_16x16x32_bf16 v[128:131], a[48:51], a[184:187], v[128:131]\n"
    "v_mfma_f32_16x16x32_bf16 v[128:131], a[44:47], a[180:183], v[128:131]\n"
    "v_mfma_f32_16x16x32_bf16 v[128:131], a[40:43], a[176:179], v[128:131]\n"
    "v_mfma_f32_16x16x32_bf16 v[128:131], a[36:39], a[172:175], v[128:131]\n"
    "buffer_store_dwordx2 v[40:41], v15, s[36:39], 0 offen sc1\n"
    "v_mfma_f32_16x16x32_bf16 v[128:131], a[32:35], a[168:171], v[128:131]\n"
    "v_mfma_f32_16x16x32_bf16 v[128:131], a[28:31], a[236:239], v[128:131]\n"
    "s_waitcnt lgkmcnt(0)\n"
    "v_cvt_pk_bf16_f32 v82, v72, v73\n"
    "v_cvt_pk_bf16_f32 v83, v74, v75\n"
    "v_mfma_f32_16x16x32_bf16 v[64:67], a[244:247], v[56:59], 0\n"
    "v_mfma_f32_16x16x32_bf16 v[60:63], a[240:243], v[56:59], 0\n"
    "v_mfma_f32_16x16x32_bf16 v[140:143], a[28:31], a[168:171], v[140:143]\n"
    "s_addk_i32 s68, 0x1000\n"
    "v_cvt_pk_bf16_f32 v80, v68, v69\n"
    "v_cvt_pk_bf16_f32 v81, v70, v71\n"
    "v_mfma_f32_16x16x32_bf16 v[140:143], a[32:35], a[172:175], v[140:143]\n"
    "buffer_load_dwordx4 a[16:19], v14, s[36:39], s68 offen offset:0 sc1\n"
    "v_mfma_f32_16x16x32_bf16 v[140:143], a[36:39], a[176:179], v[140:143]\n"
    "ds_write_b32 v12, v82 offset:32776\n"
    "v_mfma_f32_16x16x32_bf16 v[140:143], a[40:43], a[180:183], v[140:143]\n"
    "v_add_u32 v37, 1, v37\n"
    "v_mfma_f32_16x16x32_bf16 v[140:143], a[44:47], a[184:187], v[140:143]\n"
    "ds_write_b32 v12, v83 offset:32780\n"
    "v_mfma_f32_16x16x32_bf16 v[140:143], a[48:51], a[188:191], v[140:143]\n"
    "v_mfma_f32_16x16x32_bf16 v[140:143], a[52:55], a[192:195], v[140:143]\n"
    "ds_write_b32 v12, v80 offset:32768\n"
    "v_mfma_f32_16x16x32_bf16 v[152:155], a[52:55], a[196:199], v[152:155]\n"
    "buffer_load_dwordx4 a[20:23], v14, s[36:39], s68 offen offset:1024 sc1\n"
    "v_mfma_f32_16x16x32_bf16 v[152:155], a[48:51], a[192:195], v[152:155]\n"
    "ds_write_b32 v12, v81 offset:32772\n"
    "v_mfma_f32_16x16x32_bf16 v[152:155], a[44:47], a[188:191], v[152:155]\n"
    "v_cvt_pk_bf16_f32 v78, v64, v65\n"
    "v_cvt_pk_bf16_f32 v79, v66, v67\n"
    "v_mfma_f32_16x16x32_bf16 v[152:155], a[40:43], a[184:187], v[152:155]\n"
    "v_cvt_pk_bf16_f32 v76, v60, v61\n"
    "v_cvt_pk_bf16_f32 v77, v62, v63\n"
    "v_mfma_f32_16x16x32_bf16 v[152:155], a[36:39], a[180:183], v[152:155]\n"
    "ds_write_b32 v12, v78 offset:36872\n"
    "v_mfma_f32_16x16x32_bf16 v[152:155], a[32:35], a[176:179], v[152:155]\n"
    "v_mfma_f32_16x16x32_bf16 v[152:155], a[28:31], a[172:175], v[152:155]\n"
    "ds_write_b32 v12, v79 offset:36876\n"
    "v_mfma_f32_16x16x32_bf16 v[164:167], a[28:31], a[176:179], v[164:167]\n"
    "buffer_load_dwordx4 a[24:27], v14, s[36:39], s68 offen offset:2048 sc1\n"
    "v_mfma_f32_16x16x32_bf16 v[164:167], a[32:35], a[180:183], v[164:167]\n"
    "ds_write_b32 v12, v76 offset:36864\n"
    "v_mfma_f32_16x16x32_bf16 v[164:167], a[36:39], a[184:187], v[164:167]\n"
    "v_mfma_f32_16x16x32_bf16 v[164:167], a[40:43], a[188:191], v[164:167]\n"
    "ds_write_b32 v12, v77 offset:36868\n"
    "v_mfma_f32_16x16x32_bf16 v[164:167], a[44:47], a[192:195], v[164:167]\n"
    "ds_write_b32 v35, v37\n"
    "v_mfma_f32_16x16x32_bf16 v[164:167], a[48:51], a[196:199], v[164:167]\n"
    "v_mfma_f32_16x16x32_bf16 v[164:167], a[52:55], a[200:203], v[164:167]\n"
    "s_waitcnt vmcnt(29)\n"
    "v_mfma_f32_16x16x32_bf16 v[96:99], a[56:59], a[224:227], v[96:99]\n"
    "buffer_load_dwordx4 a[28:31], v14, s[36:39], s68 offen offset:3072 sc1\n"
    "v_mfma_f32_16x16x32_bf16 v[96:99], a[60:63], a[228:231], v[96:99]\n"
    "v_mfma_f32_16x16x32_bf16 v[96:99], a[64:67], a[232:235], v[96:99]\n"
    "v_mfma_f32_16x16x32_bf16 v[96:99], a[68:71], a[236:239], v[96:99]\n"
    "v_mfma_f32_16x16x32_bf16 v[96:99], a[72:75], a[168:171], v[96:99]\n"
    "v_mfma_f32_16x16x32_bf16 v[96:99], a[76:79], a[172:175], v[96:99]\n"
    "ds_read_b32 v38, v36 offset:30592\n"
    "v_mfma_f32_16x16x32_bf16 v[96:99], a[80:83], a[176:179], v[96:99]\n"
    "v_mfma_f32_16x16x32_bf16 v[108:111], a[80:83], a[180:183], v[108:111]\n"
    "s_addk_i32 s68, 0x1000\n"
    "v_mfma_f32_16x16x32_bf16 v[108:111], a[76:79], a[176:179], v[108:111]\n"
    "buffer_load_dwordx4 a[32:35], v14, s[36:39], s68 offen offset:0 sc1\n"
    "v_mfma_f32_16x16x32_bf16 v[108:111], a[72:75], a[172:175], v[108:111]\n"
    "v_mfma_f32_16x16x32_bf16 v[108:111], a[68:71], a[168:171], v[108:111]\n"
    "s_waitcnt lgkmcnt(0)\n"
    "v_cmp_gt_u32 vcc, 9, v38\n"
    "v_mfma_f32_16x16x32_bf16 v[108:111], a[64:67], a[236:239], v[108:111]\n"
    "v_mfma_f32_16x16x32_bf16 v[108:111], a[60:63], a[232:235], v[108:111]\n"
    "s_cbranch_vccnz .Lsl9\n"
    ".Lbk9:\n"
    "v_mfma_f32_16x16x32_bf16 v[108:111], a[56:59], a[228:231], v[108:111]\n"
    "ds_read_b128 a[204:207], v13 offset:20480\n"
    "v_mfma_f32_16x16x32_bf16 v[120:123], a[56:59], a[232:235], v[120:123]\n"
    "buffer_load_dwordx4 a[36:39], v14, s[36:39], s68 offen offset:1024 sc1\n"
    "v_mfma_f32_16x16x32_bf16 v[120:123], a[60:63], a[236:239], v[120:123]\n"
    "ds_read_b128 a[208:211], v13 offset:21504\n"
    "v_mfma_f32_16x16x32_bf16 v[120:123], a[64:67], a[168:171], v[120:123]\n"
    "ds_read_b128 a[212:215], v13 offset:22528\n"
    "v_mfma_f32_16x16x32_bf16 v[120:123], a[68:71], a[172:175], v[120:123]\n"
    "ds_read_b128 a[216:219], v13 offset:23552\n"
    "v_mfma_f32_16x16x32_bf16 v[120:123], a[72:75], a[176:179], v[120:123]\n"
    "ds_read_b128 a[220:223], v13 offset:24576\n"
    "v_mfma_f32_16x16x32_bf16 v[120:123], a[76:79], a[180:183], v[120:123]\n"
    "ds_read_b128 a[224:227], v13 offset:25600\n"
    "v_mfma_f32_16x16x32_bf16 v[120:123], a[80:83], a[184:187], v[120:123]\n"
    "ds_read_b128 a[228:231], v13 offset:26624\n"
    "v_mfma_f32_16x16x32_bf16 v[132:135], a[80:83], a[188:191], v[132:135]\n"
    "buffer_load_dwordx4 a[40:43], v14, s[36:39], s68 offen offset:2048 sc1\n"
    "v_mfma_f32_16x16x32_bf16 v[132:135], a[76:79], a[184:187], v[132:135]\n"
    "ds_read_b128 a[232:235], v13 offset:27648\n"
    "v_mfma_f32_16x16x32_bf16 v[132:135], a[72:75], a[180:183], v[132:135]\n"
    "v_mfma_f32_16x16x32_bf16 v[132:135], a[68:71], a[176:179], v[132:135]\n"
    "v_mfma_f32_16x16x32_bf16 v[132:135], a[64:67], a[172:175], v[132:135]\n"
    "v_mfma_f32_16x16x32_bf16 v[132:135], a[60:63], a[168:171], v[132:135]\n"
    "v_mfma_f32_16x16x32_bf16 v[132:135], a[56:59], a[236:239], v[132:135]\n"
    "ds_read_b128 a[236:239], v13 offset:28672\n"
    "v_mfma_f32_16x16x32_bf16 v[144:147], a[56:59], a[168:171], v[144:147]\n"
    "buffer_load_dwordx4 a[44:47], v14, s[36:39], s68 offen offset:3072 sc1\n"
    "v_mfma_f32_16x16x32_bf16 v[144:147], a[60:63], a[172:175], v[144:147]\n"
    "ds_read_b128 a[168:171], v13 offset:29696\n"
    "v_mfma_f32_16x16x32_bf16 v[144:147], a[64:67], a[176:179], v[144:147]\n"
    "v_mfma_f32_16x16x32_bf16 v[144:147], a[68:71], a[180:183], v[144:147]\n"
    "v_mfma_f32_16x16x32_bf16 v[144:147], a[72:75], a[184:187], v[144:147]\n"
    "v_mfma_f32_16x16x32_bf16 v[144:147], a[76:79], a[188:191], v[144:147]\n"
    "v_mfma_f32_16x16x32_bf16 v[144:147], a[80:83], a[192:195], v[144:147]\n"
    "v_mfma_f32_16x16x32_bf16 v[156:159], a[80:83], a[196:199], v[156:159]\n"
    "s_addk_i32 s68, 0x1000\n"
    "v_mfma_f32_16x16x32_bf16 v[156:159], a[76:79], a[192:195], v[156:159]\n"
    "buffer_load_dwordx4 a[48:51], v14, s[36:39], s68 offen offset:0 sc1\n"
    "v_mfma_f32_16x16x32_bf16 v[156:159], a[72:75], a[188:191], v[156:159]\n"
    "v_mfma_f32_16x16x32_bf16 v[156:159], a[68:71], a[184:187], v[156:159]\n"
    "v_mfma_f32_16x16x32_bf16 v[156:159], a[64:67], a[180:183], v[156:159]\n"
    "v_mfma_f32_16x16x32_bf16 v[156:159], a[60:63], a[176:179], v[156:159]\n"
    "v_mfma_f32_16x16x32_bf16 v[156:159], a[56:59], a[172:175], v[156:159]\n"
    "ds_read_b128 a[172:175], v13 offset:30720\n"
    "v_mfma_f32_16x16x32_bf16 v[168:171], a[56:59], a[176:179], v[168:171]\n"
    "buffer_load_dwordx4 a[52:55], v14, s[36:39], s68 offen offset:1024 sc1\n"
    "v_mfma_f32_16x16x32_bf16 v[168:171], a[60:63], a[180:183], v[168:171]\n"
    "ds_read_b128 a[176:179], v13 offset:31744\n"
    "v_mfma_f32_16x16x32_bf16 v[168:171], a[64:67], a[184:187], v[168:171]\n"
    "ds_read_b128 a[180:183], v13 offset:32768\n"
    "v_mfma_f32_16x16x32_bf16 v[168:171], a[68:71], a[188:191], v[168:171]\n"
    "v_mfma_f32_16x16x32_bf16 v[168:171], a[72:75], a[192:195], v[168:171]\n"
    "v_mfma_f32_16x16x32_bf16 v[168:171], a[76:79], a[196:199], v[168:171]\n"
    "v_mfma_f32_16x16x32_bf16 v[168:171], a[80:83], a[200:203], v[168:171]\n"
    "s_waitcnt lgkmcnt(3)\n"
    "s_waitcnt vmcnt(29)\n"
    "v_mfma_f32_16x16x32_bf16 v[88:91], a[84:87], a[204:207], v[88:91]\n"
    "buffer_load_dwordx4 a[56:59], v14, s[36:39], s68 offen offset:2048 sc1\n"
    "v_mfma_f32_16x16x32_bf16 v[88:91], a[88:91], a[208:211], v[88:91]\n"
    "v_mfma_f32_16x16x32_bf16 v[88:91], a[92:95], a[212:215], v[88:91]\n"
    "v_mfma_f32_16x16x32_bf16 v[88:91], a[96:99], a[216:219], v[88:91]\n"
    "v_mfma_f32_16x16x32_bf16 v[88:91], a[100:103], a[220:223], v[88:91]\n"
    "v_mfma_f32_16x16x32_bf16 v[88:91], a[104:107], a[224:227], v[88:91]\n"
    "v_mfma_f32_16x16x32_bf16 v[88:91], a[108:111], a[228:231], v[88:91]\n"
    "v_mfma_f32_16x16x32_bf16 v[100:103], a[108:111], a[232:235], v[100:103]\n"
    "buffer_load_dwordx4 a[60:63], v14, s[36:39], s68 offen offset:3072 sc1\n"
    "v_mfma_f32_16x16x32_bf16 v[100:103], a[104:107], a[228:231], v[100:103]\n"
    "ds_read_b64_tr_b16 a[240:241], v28 offset:14336\n"
    "v_mfma_f32_16x16x32_bf16 v[100:103], a[100:103], a[224:227], v[100:103]\n"
    "ds_read_b64_tr_b16 a[242:243], v28 offset:14592\n"
    "v_mfma_f32_16x16x32_bf16 v[100:103], a[96:99], a[220:223], v[100:103]\n"
    "ds_read_b64_tr_b16 a[244:245], v29 offset:14336\n"
    "v_mfma_f32_16x16x32_bf16 v[100:103], a[92:95], a[216:219], v[100:103]\n"
    "ds_read_b64_tr_b16 a[246:247], v29 offset:14592\n"
    "v_mfma_f32_16x16x32_bf16 v[100:103], a[88:91], a[212:215], v[100:103]\n"
    "v_mfma_f32_16x16x32_bf16 v[100:103], a[84:87], a[208:211], v[100:103]\n"
    "v_mfma_f32_16x16x32_bf16 v[112:115], a[84:87], a[212:215], v[112:115]\n"
    "s_addk_i32 s68, 0x1000\n"
    "v_mfma_f32_16x16x32_bf16 v[112:115], a[88:91], a[216:219], v[112:115]\n"
    "buffer_load_dwordx4 a[64:67], v14, s[36:39], s68 offen offset:0 sc1\n"
    "v_mfma_f32_16x16x32_bf16 v[112:115], a[92:95], a[220:223], v[112:115]\n"
    "v_mfma_f32_16x16x32_bf16 v[112:115], a[96:99], a[224:227], v[112:115]\n"
    "v_mfma_f32_16x16x32_bf16 v[112:115], a[100:103], a[228:231], v[112:115]\n"
    "v_mfma_f32_16x16x32_bf16 v[112:115], a[104:107], a[232:235], v[112:115]\n"
    "v_mfma_f32_16x16x32_bf16 v[112:115], a[108:111], a[236:239], v[112:115]\n"
    "s_waitcnt lgkmcnt(0)\n"
    "v_mfma_f32_16x16x32_bf16 v[64:67], a[244:247], v[56:59], 0\n"
    "v_mfma_f32_16x16x32_bf16 v[60:63], a[240:243], v[56:59], 0\n"
    "v_mfma_f32_16x16x32_bf16 v[124:127], a[108:111], a[168:171], v[124:127]\n"
    "buffer_load_dwordx4 a[68:71], v14, s[36:39], s68 offen offset:1024 sc1\n"
    "v_mfma_f32_16x16x32_bf16 v[124:127], a[104:107], a[236:239], v[124:127]\n"
    "ds_read_b64_tr_b16 a[248:249], v28 offset:15744\n"
    "v_mfma_f32_16x16x32_bf16 v[124:127], a[100:103], a[232:235], v[124:127]\n"
    "ds_read_b64_tr_b16 a[250:251], v28 offset:16000\n"
    "v_mfma_f32_16x16x32_bf16 v[124:127], a[96:99], a[228:231], v[124:127]\n"
    "ds_read_b64_tr_b16 a[252:253], v29 offset:15744\n"
    "v_mfma_f32_16x16x32_bf16 v[124:127], a[92:95], a[224:227], v[124:127]\n"
    "ds_read_b64_tr_b16 a[254:255], v29 offset:16000\n"
    "v_mfma_f32_16x16x32_bf16 v[124:127], a[88:91], a[220:223], v[124:127]\n"
    "v_mfma_f32_16x16x32_bf16 v[124:127], a[84:87], a[216:219], v[124:127]\n"
    "v_cvt_pk_bf16_f32 v78, v64, v65\n"
    "v_cvt_pk_bf16_f32 v79, v66, v67\n"
    "v_mfma_f32_16x16x32_bf16 v[136:139], a[84:87], a[220:223], v[136:139]\n"
    "buffer_load_dwordx4 a[72:75], v14, s[36:39], s68 offen offset:2048 sc1\n"
    "v_mfma_f32_16x16x32_bf16 v[136:139], a[88:91], a[224:227], v[136:139]\n"
    "v_cvt_pk_bf16_f32 v76, v60, v61\n"
    "v_cvt_pk_bf16_f32 v77, v62, v63\n"
    "v_mfma_f32_16x16x32_bf16 v[136:139], a[92:95], a[228:231], v[136:139]\n"
    "ds_write_b32 v12, v78 offset:8\n"
    "v_mfma_f32_16x16x32_bf16 v[136:139], a[96:99], a[232:235], v[136:139]\n"
    "v_mfma_f32_16x16x32_bf16 v[136:139], a[100:103], a[236:239], v[136:139]\n"
    "ds_write_b32 v12, v79 offset:12\n"
    "v_mfma_f32_16x16x32_bf16 v[136:139], a[104:107], a[168:171], v[136:139]\n"
    "s_waitcnt lgkmcnt(2)\n"
    "v_mfma_f32_16x16x32_bf16 v[136:139], a[108:111], a[172:175], v[136:139]\n"
    "ds_write_b32 v12, v76 offset:0\n"
    "v_mfma_f32_16x16x32_bf16 v[72:75], a[252:255], v[56:59], 0\n"
    "v_mfma_f32_16x16x32_bf16 v[68:71], a[248:251], v[56:59], 0\n"
    "v_mfma_f32_16x16x32_bf16 v[148:151], a[108:111], a[176:179], v[148:151]\n"
    "buffer_load_dwordx4 a[76:79], v14, s[36:39], s68 offen offset:3072 sc1\n"
    "v_mfma_f32_16x16x32_bf16 v[148:151], a[104:107], a[172:175], v[148:151]\n"
    "ds_write_b32 v12, v77 offset:4\n"
    "v_mfma_f32_16x16x32_bf16 v[148:151], a[100:103], a[168:171], v[148:151]\n"
    "ds_read_b64_tr_b16 a[240:241], v28 offset:17152\n"
    "v_mfma_f32_16x16x32_bf16 v[148:151], a[96:99], a[236:239], v[148:151]\n"
    "ds_read_b64_tr_b16 a[242:243], v28 offset:17408\n"
    "v_mfma_f32_16x16x32_bf16 v[148:151], a[92:95], a[232:235], v[148:151]\n"
    "ds_read_b64_tr_b16 a[244:245], v29 offset:17152\n"
    "v_mfma_f32_16x16x32_bf16 v[148:151], a[88:91], a[228:231], v[148:151]\n"
    "ds_read_b64_tr_b16 a[246:247], v29 offset:17408\n"
    "v_mfma_f32_16x16x32_bf16 v[148:151], a[84:87], a[224:227], v[148:151]\n"
    "v_cvt_pk_bf16_f32 v82, v72, v73\n"
    "v_cvt_pk_bf16_f32 v83, v74, v75\n"
    "v_mfma_f32_16x16x32_bf16 v[160:163], a[84:87], a[228:231], v[160:163]\n"
    "s_addk_i32 s68, 0x1000\n"
    "v_cvt_pk_bf16_f32 v80, v68, v69\n"
    "v_cvt_pk_bf16_f32 v81, v70, v71\n"
    "v_mfma_f32_16x16x32_bf16 v[160:163], a[88:91], a[232:235], v[160:163]\n"
    "buffer_load_dwordx4 a[80:83], v14, s[36:39], s68 offen offset:0 sc1\n"
    "v_mfma_f32_16x16x32_bf16 v[160:163], a[92:95], a[236:239], v[160:163]\n"
    "ds_write_b32 v12, v82 offset:4104\n"
    "v_mfma_f32_16x16x32_bf16 v[160:163], a[96:99], a[168:171], v[160:163]\n"
    "v_mfma_f32_16x16x32_bf16 v[160:163], a[100:103], a[172:175], v[160:163]\n"
    "ds_write_b32 v12, v83 offset:4108\n"
    "v_mfma_f32_16x16x32_bf16 v[160:163], a[104:107], a[176:179], v[160:163]\n"
    "s_waitcnt lgkmcnt(2)\n"
    "v_mfma_f32_16x16x32_bf16 v[160:163], a[108:111], a[180:183], v[160:163]\n"
    "ds_write_b32 v12, v80 offset:4096\n"
    "v_mfma_f32_16x16x32_bf16 v[64:67], a[244:247], v[56:59], 0\n"
    "v_mfma_f32_16x16x32_bf16 v[60:63], a[240:243], v[56:59], 0\n"
    "s_waitcnt vmcnt(29)\n"
    "v_mfma_f32_16x16x32_bf16 v[92:95], a[112:115], a[204:207], v[92:95]\n"
    "s_add_u32 s68, s64, 774144\n"
    "v_mfma_f32_16x16x32_bf16 v[92:95], a[116:119], a[208:211], v[92:95]\n"
    "buffer_load_dwordx4 a[84:87], v14, s[36:39], s68 offen offset:0 sc1\n"
    "v_mfma_f32_16x16x32_bf16 v[92:95], a[120:123], a[212:215], v[92:95]\n"
    "ds_write_b32 v12, v81 offset:4100\n"
    "v_mfma_f32_16x16x32_bf16 v[92:95], a[124:127], a[216:219], v[92:95]\n"
    ".Lpad5:\n"
    "s_cbranch_execz .Lhop_done\n"
    "ds_read_b64_tr_b16 a[248:249], v28 offset:18560\n"
    "v_mfma_f32_16x16x32_bf16 v[92:95], a[128:131], a[220:223], v[92:95]\n"
    "ds_read_b64_tr_b16 a[250:251], v28 offset:18816\n"
    "v_mfma_f32_16x16x32_bf16 v[92:95], a[132:135], a[224:227], v[92:95]\n"
    "ds_read_b64_tr_b16 a[252:253], v29 offset:18560\n"
    "v_mfma_f32_16x16x32_bf16 v[92:95], a[136:139], a[228:231], v[92:95]\n"
    "v_cvt_pk_bf16_f32 v78, v64, v65\n"
    "v_cvt_pk_bf16_f32 v79, v66, v67\n"
    "v_mfma_f32_16x16x32_bf16 v[104:107], a[136:139], a[232:235], v[104:107]\n"
    "buffer_load_dwordx4 a[88:91], v14, s[36:39], s68 offen offset:1024 sc1\n"
    "v_mfma_f32_16x16x32_bf16 v[104:107], a[132:135], a[228:231], v[104:107]\n"
    "v_cvt_pk_bf16_f32 v76, v60, v61\n"
    "v_cvt_pk_bf16_f32 v77, v62, v63\n"
    "v_mfma_f32_16x16x32_bf16 v[104:107], a[128:131], a[224:227], v[104:107]\n"
    "ds_write_b32 v12, v78 offset:8200\n"
    "v_mfma_f32_16x16x32_bf16 v[104:107], a[124:127], a[220:223], v[104:107]\n"
    "ds_read_b64_tr_b16 a[254:255], v29 offset:18816\n"
    "v_mfma_f32_16x16x32_bf16 v[104:107], a[120:123], a[216:219], v[104:107]\n"
    "ds_write_b32 v12, v79 offset:8204\n"
    "v_mfma_f32_16x16x32_bf16 v[104:107], a[116:119], a[212:215], v[104:107]\n"
    "v_mfma_f32_16x16x32_bf16 v[104:107], a[112:115], a[208:211], v[104:107]\n"
    "ds_write_b32 v12, v76 offset:8192\n"
    "v_mfma_f32_16x16x32_bf16 v[116:119], a[112:115], a[212:215], v[116:119]\n"
    "buffer_load_dwordx4 a[92:95], v14, s[36:39], s68 offen offset:2048 sc1\n"
    "v_mfma_f32_16x16x32_bf16 v[116:119], a[116:119], a[216:219], v[116:119]\n"
    "ds_write_b32 v12, v77 offset:8196\n"
    "v_mfma_f32_16x16x32_bf16 v[116:119], a[120:123], a[220:223], v[116:119]\n"
    "ds_read_b64_tr_b16 a[240:241], v28 offset:19968\n"
    "v_mfma_f32_16x16x32_bf16 v[116:119], a[124:127], a[224:227], v[116:119]\n"
    "ds_read_b64_tr_b16 a[242:243], v28 offset:20224\n"
    "v_mfma_f32_16x16x32_bf16 v[116:119], a[128:131], a[228:231], v[116:119]\n"
    "ds_read_b64_tr_b16 a[244:245], v29 offset:19968\n"
    "v_mfma_f32_16x16x32_bf16 v[116:119], a[132:135], a[232:235], v[116:119]\n"
    "ds_read_b64_tr_b16 a[246:247], v29 offset:20224\n"
    "v_mfma_f32_16x16x32_bf16 v[116:119], a[136:139], a[236:239], v[116:119]\n"
    "s_waitcnt lgkmcnt(6)\n"
    "v_mfma_f32_16x16x32_bf16 v[72:75], a[252:255], v[56:59], 0\n"
    "v_mfma_f32_16x16x32_bf16 v[68:71], a[248:251], v[56:59], 0\n"
    "v_mfma_f32_16x16x32_bf16 v[128:131], a[136:139], a[168:171], v[128:131]\n"
    "buffer_load_dwordx4 a[96:99], v14, s[36:39], s68 offen offset:3072 sc1\n"
    "v_mfma_f32_16x16x32_bf16 v[128:131], a[132:135], a[236:239], v[128:131]\n"
    "v_mfma_f32_16x16x32_bf16 v[128:131], a[128:131], a[232:235], v[128:131]\n"
    "v_mfma_f32_16x16x32_bf16 v[128:131], a[124:127], a[228:231], v[128:131]\n"
    "v_mfma_f32_16x16x32_bf16 v[128:131], a[120:123], a[224:227], v[128:131]\n"
    "v_mfma_f32_16x16x32_bf16 v[128:131], a[116:119], a[220:223], v[128:131]\n"
    "v_mfma_f32_16x16x32_bf16 v[128:131], a[112:115], a[216:219], v[128:131]\n"
    "s_waitcnt lgkmcnt(0)\n"
    "v_cvt_pk_bf16_f32 v82, v72, v73\n"
    "v_cvt_pk_bf16_f32 v83, v74, v75\n"
    "v_mfma_f32_16x16x32_bf16 v[64:67], a[244:247], v[56:59], 0\n"
    "v_mfma_f32_16x16x32_bf16 v[60:63], a[240:243], v[56:59], 0\n"
    "v_mfma_f32_16x16x32_bf16 v[140:143], a[112:115], a[220:223], v[140:143]\n"
    "s_addk_i32 s68, 0x1000\n"
    "v_cvt_pk_bf16_f32 v80, v68, v69\n"
    "v_cvt_pk_bf16_f32 v81, v70, v71\n"
    "v_mfma_f32_16x16x32_bf16 v[140:143], a[116:119], a[224:227], v[140:143]\n"
    "buffer_load_dwordx4 a[100:103], v14, s[36:39], s68 offen offset:0 sc1\n"
    "v_mfma_f32_16x16x32_bf16 v[140:143], a[120:123], a[228:231], v[140:143]\n"
    "ds_write_b32 v12, v82 offset:12296\n"
    "v_mfma_f32_16x16x32_bf16 v[140:143], a[124:127], a[232:235], v[140:143]\n"
    "v_add_u32 v37, 1, v37\n"
    "v_mfma_f32_16x16x32_bf16 v[140:143], a[128:131], a[236:239], v[140:143]\n"
    "ds_write_b32 v12, v83 offset:12300\n"
    "v_mfma_f32_16x16x32_bf16 v[140:143], a[132:135], a[168:171], v[140:143]\n"
    "v_mfma_f32_16x16x32_bf16 v[140:143], a[136:139], a[172:175], v[140:143]\n"
    "ds_write_b32 v12, v80 offset:12288\n"
    "v_mfma_f32_16x16x32_bf16 v[152:155], a[136:139], a[176:179], v[152:155]\n"
    "buffer_load_dwordx4 a[104:107], v14, s[36:39], s68 offen offset:1024 sc1\n"
    "v_mfma_f32_16x16x32_bf16 v[152:155], a[132:135], a[172:175], v[152:155]\n"
    "ds_write_b32 v12, v81 offset:12292\n"
    "v_mfma_f32_16x16x32_bf16 v[152:155], a[128:131], a[168:171], v[152:155]\n"
    "v_cvt_pk_bf16_f32 v78, v64, v65\n"
    "v_cvt_pk_bf16_f32 v79, v66, v67\n"
    "v_mfma_f32_16x16x32_bf16 v[152:155], a[124:127], a[236:239], v[152:155]\n"
    "v_cvt_pk_bf16_f32 v76, v60, v61\n"
    "v_cvt_pk_bf16_f32 v77, v62, v63\n"
    "v_mfma_f32_16x16x32_bf16 v[152:155], a[120:123], a[232:235], v[152:155]\n"
    "ds_write_b32 v12, v78 offset:16392\n"
    "v_mfma_f32_16x16x32_bf16 v[152:155], a[116:119], a[228:231], v[152:155]\n"
    "ds_read_b64_tr_b16 a[240:241], v28 offset:14336\n"
    "v_mfma_f32_16x16x32_bf16 v[152:155], a[112:115], a[224:227], v[152:155]\n"
    "ds_write_b32 v12, v79 offset:16396\n"
    "v_mfma_f32_16x16x32_bf16 v[164:167], a[112:115], a[228:231], v[164:167]\n"
    "buffer_load_dwordx4 a[108:111], v14, s[36:39], s68 offen offset:2048 sc1\n"
    "v_mfma_f32_16x16x32_bf16 v[164:167], a[116:119], a[232:235], v[164:167]\n"
    "ds_write_b32 v12, v76 offset:16384\n"
    "v_mfma_f32_16x16x32_bf16 v[164:167], a[120:123], a[236:239], v[164:167]\n"
    "ds_read_b64_tr_b16 a[242:243], v28 offset:14592\n"
    "v_mfma_f32_16x16x32_bf16 v[164:167], a[124:127], a[168:171], v[164:167]\n"
    "ds_write_b32 v12, v77 offset:16388\n"
    "v_mfma_f32_16x16x32_bf16 v[164:167], a[128:131], a[172:175], v[164:167]\n"
    "ds_write_b32 v35, v37\n"
    "v_mfma_f32_16x16x32_bf16 v[164:167], a[132:135], a[176:179], v[164:167]\n"
    "ds_read_b64_tr_b16 a[244:245], v29 offset:14336\n"
    "v_mfma_f32_16x16x32_bf16 v[164:167], a[136:139], a[180:183], v[164:167]\n"
    "s_waitcnt vmcnt(29)\n"
    "ds_read_b64_tr_b16 a[246:247], v29 offset:14592\n"
    "v_mfma_f32_16x16x32_bf16 v[96:99], a[140:143], a[204:207], v[96:99]\n"
    "buffer_load_dwordx4 a[112:115], v14, s[36:39], s68 offen offset:3072 sc1\n"
    "v_mfma_f32_16x16x32_bf16 v[96:99], a[144:147], a[208:211], v[96:99]\n"
    "ds_read_b64_tr_b16 a[248:249], v28 offset:15744\n"
    "v_mfma_f32_16x16x32_bf16 v[96:99], a[148:151], a[212:215], v[96:99]\n"
    "ds_read_b64_tr_b16 a[250:251], v28 offset:16000\n"
    "v_mfma_f32_16x16x32_bf16 v[96:99], a[152:155], a[216:219], v[96:99]\n"
    "ds_read_b64_tr_b16 a[252:253], v29 offset:15744\n"
    "v_mfma_f32_16x16x32_bf16 v[96:99], a[156:159], a[220:223], v[96:99]\n"
    "ds_read_b64_tr_b16 a[254:255], v29 offset:16000\n"
    "v_mfma_f32_16x16x32_bf16 v[96:99], a[160:163], a[224:227], v[96:99]\n"
    "ds_read_b32 v38, v36 offset:30592\n"
    "v_mfma_f32_16x16x32_bf16 v[96:99], a[164:167], a[228:231], v[96:99]\n"
    "v_mfma_f32_16x16x32_bf16 v[108:111], a[164:167], a[232:235], v[108:111]\n"
    "s_addk_i32 s68, 0x1000\n"
    "v_mfma_f32_16x16x32_bf16 v[108:111], a[160:163], a[228:231], v[108:111]\n"
    "buffer_load_dwordx4 a[116:119], v14, s[36:39], s68 offen offset:0 sc1\n"
    "v_mfma_f32_16x16x32_bf16 v[108:111], a[156:159], a[224:227], v[108:111]\n"
    "v_mfma_f32_16x16x32_bf16 v[108:111], a[152:155], a[220:223], v[108:111]\n"
    "s_waitcnt lgkmcnt(0)\n"
    "v_cmp_gt_u32 vcc, 10, v38\n"
    "v_mfma_f32_16x16x32_bf16 v[108:111], a[148:151], a[216:219], v[108:111]\n"
    "v_mfma_f32_16x16x32_bf16 v[108:111], a[144:147], a[212:215], v[108:111]\n"
    "s_cbranch_vccnz .Lsl10\n"
    ".Lbk10:\n"
    "v_mfma_f32_16x16x32_bf16 v[108:111], a[140:143], a[208:211], v[108:111]\n"
    "ds_read_b128 a[184:187], v13 offset:0\n"
    "v_mfma_f32_16x16x32_bf16 v[64:67], a[244:247], v[44:47], 0\n"
    "v_mfma_f32_16x16x32_bf16 v[60:63], a[240:243], v[44:47], 0\n"
    "v_mfma_f32_16x16x32_bf16 v[72:75], a[252:255], v[44:47], 0\n"
    "v_mfma_f32_16x16x32_bf16 v[68:71], a[248:251], v[44:47], 0\n"
    "v_mfma_f32_16x16x32_bf16 v[120:123], a[140:143], a[212:215], v[120:123]\n"
    "buffer_load_dwordx4 a[120:123], v14, s[36:39], s68 offen offset:1024 sc1\n"
    "v_mfma_f32_16x16x32_bf16 v[120:123], a[144:147], a[216:219], v[120:123]\n"
    "ds_read_b128 a[188:191], v13 offset:1024\n"
    "v_mfma_f32_16x16x32_bf16 v[120:123], a[148:151], a[220:223], v[120:123]\n"
    "ds_read_b128 a[192:195], v13 offset:2048\n"
    "v_mfma_f32_16x16x32_bf16 v[120:123], a[152:155], a[224:227], v[120:123]\n"
    "ds_read_b128 a[196:199], v13 offset:3072\n"
    "v_mfma_f32_16x16x32_bf16 v[120:123], a[156:159], a[228:231], v[120:123]\n"
    "ds_read_b128 a[200:203], v13 offset:4096\n"
    "v_mfma_f32_16x16x32_bf16 v[120:123], a[160:163], a[232:235], v[120:123]\n"
    "ds_read_b128 a[204:207], v13 offset:5120\n"
    "v_mfma_f32_16x16x32_bf16 v[120:123], a[164:167], a[236:239], v[120:123]\n"
    "ds_read_b128 a[208:211], v13 offset:6144\n"
    "v_cvt_pk_bf16_f32 v78, v64, v65\n"
    "v_mfma_f32_16x16x32_bf16 v[132:135], a[164:167], a[168:171], v[132:135]\n"
    "buffer_load_dwordx4 a[124:127], v14, s[36:39], s68 offen offset:2048 sc1\n"
    "v_mfma_f32_16x16x32_bf16 v[132:135], a[160:163], a[236:239], v[132:135]\n"
    "ds_read_b128 a[212:215], v13 offset:7168\n"
    "v_cvt_pk_bf16_f32 v79, v66, v67\n"
    "v_mfma_f32_16x16x32_bf16 v[132:135], a[156:159], a[232:235], v[132:135]\n"
    "v_cvt_pk_bf16_f32 v76, v60, v61\n"
    "v_cvt_pk_bf16_f32 v77, v62, v63\n"
    "v_mfma_f32_16x16x32_bf16 v[132:135], a[152:155], a[228:231], v[132:135]\n"
    "ds_write_b32 v12, v78 offset:20488\n"
    "v_mfma_f32_16x16x32_bf16 v[132:135], a[148:151], a[224:227], v[132:135]\n"
    "v_cvt_pk_bf16_f32 v82, v72, v73\n"
    "v_cvt_pk_bf16_f32 v83, v74, v75\n"
    "v_mfma_f32_16x16x32_bf16 v[132:135], a[144:147], a[220:223], v[132:135]\n"
    "ds_write_b32 v12, v79 offset:20492\n"
    "v_mfma_f32_16x16x32_bf16 v[132:135], a[140:143], a[216:219], v[132:135]\n"
    "ds_read_b128 a[216:219], v13 offset:8192\n"
    "v_cvt_pk_bf16_f32 v80, v68, v69\n"
    "v_mfma_f32_16x16x32_bf16 v[144:147], a[140:143], a[220:223], v[144:147]\n"
    "buffer_load_dwordx4 a[128:131], v14, s[36:39], s68 offen offset:3072 sc1\n"
    "v_mfma_f32_16x16x32_bf16 v[144:147], a[144:147], a[224:227], v[144:147]\n"
    "ds_write_b32 v12, v76 offset:20480\n"
    "v_mfma_f32_16x16x32_bf16 v[144:147], a[148:151], a[228:231], v[144:147]\n"
    "ds_read_b64_tr_b16 a[240:241], v28 offset:17152\n"
    "v_cvt_pk_bf16_f32 v81, v70, v71\n"
    "v_mfma_f32_16x16x32_bf16 v[144:147], a[152:155], a[232:235], v[144:147]\n"
    "ds_write_b32 v12, v77 offset:20484\n"
    "v_mfma_f32_16x16x32_bf16 v[144:147], a[156:159], a[236:239], v[144:147]\n"
    "ds_write_b32 v12, v82 offset:24584\n"
    "v_mfma_f32_16x16x32_bf16 v[144:147], a[160:163], a[168:171], v[144:147]\n"
    "ds_read_b64_tr_b16 a[242:243], v28 offset:17408\n"
    "v_mfma_f32_16x16x32_bf16 v[144:147], a[164:167], a[172:175], v[144:147]\n"
    "ds_write_b32 v12, v83 offset:24588\n"
    "v_mfma_f32_16x16x32_bf16 v[156:159], a[164:167], a[176:179], v[156:159]\n"
    "s_addk_i32 s68, 0x1000\n"
    "ds_read_b64_tr_b16 a[244:245], v29 offset:17152\n"
    "v_mfma_f32_16x16x32_bf16 v[156:159], a[160:163], a[172:175], v[156:159]\n"
    "buffer_load_dwordx4 a[132:135], v14, s[36:39], s68 offen offset:0 sc1\n"
    "v_mfma_f32_16x16x32_bf16 v[156:159], a[156:159], a[168:171], v[156:159]\n"
    "ds_write_b32 v12, v80 offset:24576\n"
    "v_mfma_f32_16x16x32_bf16 v[156:159], a[152:155], a[236:239], v[156:159]\n"
    "ds_read_b64_tr_b16 a[246:247], v29 offset:17408\n"
    "v_mfma_f32_16x16x32_bf16 v[156:159], a[148:151], a[232:235], v[156:159]\n"
    "ds_write_b32 v12, v81 offset:24580\n"
    "v_mfma_f32_16x16x32_bf16 v[156:159], a[144:147], a[228:231], v[156:159]\n"
    "ds_read_b64_tr_b16 a[248:249], v28 offset:18560\n"
    "v_mfma_f32_16x16x32_bf16 v[156:159], a[140:143], a[224:227], v[156:159]\n"
    "ds_read_b64_tr_b16 a[250:251], v28 offset:18816\n"
    "v_mfma_f32_16x16x32_bf16 v[168:171], a[140:143], a[228:231], v[168:171]\n"
    "buffer_load_dwordx4 a[136:139], v14, s[36:39], s68 offen offset:1024 sc1\n"
    "v_mfma_f32_16x16x32_bf16 v[168:171], a[144:147], a[232:235], v[168:171]\n"
    "ds_read_b64_tr_b16 a[252:253], v29 offset:18560\n"
    "v_mfma_f32_16x16x32_bf16 v[168:171], a[148:151], a[236:239], v[168:171]\n"
    "ds_read_b64_tr_b16 a[254:255], v29 offset:18816\n"
    "v_mfma_f32_16x16x32_bf16 v[168:171], a[152:155], a[168:171], v[168:171]\n"
    "v_mfma_f32_16x16x32_bf16 v[168:171], a[156:159], a[172:175], v[168:171]\n"
    "v_mfma_f32_16x16x32_bf16 v[168:171], a[160:163], a[176:179], v[168:171]\n"
    "v_mfma_f32_16x16x32_bf16 v[168:171], a[164:167], a[180:183], v[168:171]\n"
    "s_waitcnt lgkmcnt(4)\n"
    "v_mfma_f32_16x16x32_bf16 v[64:67], a[244:247], v[44:47], 0\n"
    "v_mfma_f32_16x16x32_bf16 v[60:63], a[240:243], v[44:47], 0\n"
    "s_waitcnt vmcnt(14)\n"
    "v_mfma_f32_16x16x32_bf16 v[96:99], a[56:59], a[184:187], v[96:99]\n"
    "buffer_load_dwordx4 a[140:143], v14, s[36:39], s68 offen offset:2048 sc1\n"
    "v_mfma_f32_16x16x32_bf16 v[96:99], a[60:63], a[188:191], v[96:99]\n"
    "ds_read_b64_tr_b16 a[240:241], v28 offset:19968\n"
    "v_mfma_f32_16x16x32_bf16 v[96:99], a[64:67], a[192:195], v[96:99]\n"
    "ds_read_b64_tr_b16 a[242:243], v28 offset:20224\n"
    "v_mfma_f32_16x16x32_bf16 v[96:99], a[68:71], a[196:199], v[96:99]\n"
    "ds_read_b64_tr_b16 a[244:245], v29 offset:19968\n"
    "v_mfma_f32_16x16x32_bf16 v[96:99], a[72:75], a[200:203], v[96:99]\n"
    "ds_read_b64_tr_b16 a[246:247], v29 offset:20224\n"
    "v_mfma_f32_16x16x32_bf16 v[96:99], a[76:79], a[204:207], v[96:99]\n"
    "buffer_load_dwordx4 a[144:147], v14, s[36:39], s68 offen offset:3072 sc1\n"
    "v_mfma_f32_16x16x32_bf16 v[96:99], a[80:83], a[208:211], v[96:99]\n"
    "s_waitcnt lgkmcnt(4)\n"
    "v_cvt_pk_bf16_f32 v78, v64, v65\n"
    "v_cvt_pk_bf16_f32 v79, v66, v67\n"
    "v_mfma_f32_16x16x32_bf16 v[72:75], a[252:255], v[44:47], 0\n"
    "v_mfma_f32_16x16x32_bf16 v[68:71], a[248:251], v[44:47], 0\n"
    "v_mfma_f32_16x16x32_bf16 v[88:91], a[24:27], a[208:211], v[88:91]\n"
    "v_cvt_pk_bf16_f32 v76, v60, v61\n"
    "v_cvt_pk_bf16_f32 v77, v62, v63\n"
    "v_mfma_f32_16x16x32_bf16 v[88:91], a[20:23], a[204:207], v[88:91]\n"
    "ds_write_b32 v12, v78 offset:28680\n"
    "v_mfma_f32_16x16x32_bf16 v[88:91], a[16:19], a[200:203], v[88:91]\n"
    "v_mfma_f32_16x16x32_bf16 v[88:91], a[12:15], a[196:199], v[88:91]\n"
    "s_addk_i32 s68, 0x1000\n"
    "ds_write_b32 v12, v79 offset:28684\n"
    "v_mfma_f32_16x16x32_bf16 v[88:91], a[8:11], a[192:195], v[88:91]\n"
    "buffer_load_dwordx4 a[148:151], v14, s[36:39], s68 offen offset:0 sc1\n"
    "v_mfma_f32_16x16x32_bf16 v[88:91], a[4:7], a[188:191], v[88:91]\n"
    "ds_write_b32 v12, v76 offset:28672\n"
    "v_mfma_f32_16x16x32_bf16 v[88:91], a[0:3], a[184:187], v[88:91]\n"
    "v_cvt_pk_bf16_f32 v82, v72, v73\n"
    "v_cvt_pk_bf16_f32 v83, v74, v75\n"
    "v_mfma_f32_16x16x32_bf16 v[92:95], a[28:31], a[184:187], v[92:95]\n"
    "ds_write_b32 v12, v77 offset:28676\n"
    "v_mfma_f32_16x16x32_bf16 v[92:95], a[32:35], a[188:191], v[92:95]\n"
    "v_cvt_pk_bf16_f32 v80, v68, v69\n"
    "v_cvt_pk_bf16_f32 v81, v70, v71\n"
    "v_mfma_f32_16x16x32_bf16 v[92:95], a[36:39], a[192:195], v[92:95]\n"
    "buffer_load_dwordx4 a[152:155], v14, s[36:39], s68 offen offset:1024 sc1\n"
    "v_mfma_f32_16x16x32_bf16 v[92:95], a[40:43], a[196:199], v[92:95]\n"
    "ds_write_b32 v12, v82 offset:32776\n"
    "v_mfma_f32_16x16x32_bf16 v[92:95], a[44:47], a[200:203], v[92:95]\n"
    "v_fmac_f32 v180, s71, v96\n"
    "v_fmac_f32 v181, s71, v97\n"
    "v_mfma_f32_16x16x32_bf16 v[92:95], a[48:51], a[204:207], v[92:95]\n"
    "ds_write_b32 v12, v83 offset:32780\n"
    "v_mfma_f32_16x16x32_bf16 v[92:95], a[52:55], a[208:211], v[92:95]\n"
    "s_waitcnt lgkmcnt(4)\n"
    "v_add_u32 v37, 1, v37\n"
    "v_fmac_f32 v182, s71, v98\n"
    "v_mfma_f32_16x16x32_bf16 v[64:67], a[244:247], v[44:47], 0\n"
    "v_mfma_f32_16x16x32_bf16 v[60:63], a[240:243], v[44:47], 0\n"
    "v_mfma_f32_16x16x32_bf16 v[108:111], a[80:83], a[212:215], v[108:111]\n"
    "buffer_load_dwordx4 a[156:159], v14, s[36:39], s68 offen offset:2048 sc1\n"
    "v_mfma_f32_16x16x32_bf16 v[108:111], a[76:79], a[208:211], v[108:111]\n"
    "ds_write_b32 v12, v80 offset:32768\n"
    "v_mfma_f32_16x16x32_bf16 v[108:111], a[72:75], a[204:207], v[108:111]\n"
    "v_fmac_f32 v183, s71, v99\n"
    "v_fmac_f32 v172, s71, v88\n"
    "v_mfma_f32_16x16x32_bf16 v[108:111], a[68:71], a[200:203], v[108:111]\n"
    "ds_write_b32 v12, v81 offset:32772\n"
    "v_mfma_f32_16x16x32_bf16 v[108:111], a[64:67], a[196:199], v[108:111]\n"
    "ds_write_b64 v20, v[180:181] offset:2048\n"
    "v_mfma_f32_16x16x32_bf16 v[108:111], a[60:63], a[192:195], v[108:111]\n"
    "buffer_load_dwordx4 a[160:163], v14, s[36:39], s68 offen offset:3072 sc1\n"
    "v_mfma_f32_16x16x32_bf16 v[108:111], a[56:59], a[188:191], v[108:111]\n"
    "v_cvt_pk_bf16_f32 v78, v64, v65\n"
    "v_cvt_pk_bf16_f32 v79, v66, v67\n"
    "v_mfma_f32_16x16x32_bf16 v[100:103], a[0:3], a[188:191], v[100:103]\n"
    "v_cvt_pk_bf16_f32 v76, v60, v61\n"
    "v_cvt_pk_bf16_f32 v77, v62, v63\n"
    "v_mfma_f32_16x16x32_bf16 v[100:103], a[4:7], a[192:195], v[100:103]\n"
    "ds_write_b32 v12, v78 offset:36872\n"
    "v_mfma_f32_16x16x32_bf16 v[100:103], a[8:11], a[196:199], v[100:103]\n"
    "ds_write_b64 v20, v[182:183] offset:2056\n"
    "v_mfma_f32_16x16x32_bf16 v[100:103], a[12:15], a[200:203], v[100:103]\n"
    "s_addk_i32 s68, 0x1000\n"
    "ds_write_b32 v12, v79 offset:36876\n"
    "v_mfma_f32_16x16x32_bf16 v[100:103], a[16:19], a[204:207], v[100:103]\n"
    "buffer_load_dwordx4 a[164:167], v14, s[36:39], s68 offen offset:0 sc1\n"
    "v_mfma_f32_16x16x32_bf16 v[100:103], a[20:23], a[208:211], v[100:103]\n"
    "ds_write_b32 v12, v76 offset:36864\n"
    "v_mfma_f32_16x16x32_bf16 v[100:103], a[24:27], a[212:215], v[100:103]\n"
    "v_fmac_f32 v173, s71, v89\n"
    "v_fmac_f32 v174, s71, v90\n"
    "v_mfma_f32_16x16x32_bf16 v[104:107], a[52:55], a[212:215], v[104:107]\n"
    "ds_write_b32 v12, v77 offset:36868\n"
    "v_mfma_f32_16x16x32_bf16 v[104:107], a[48:51], a[208:211], v[104:107]\n"
    "ds_write_b32 v35, v37\n"
    "v_mfma_f32_16x16x32_bf16 v[104:107], a[44:47], a[204:207], v[104:107]\n"
    "v_fmac_f32 v175, s71, v91\n"
    "v_fmac_f32 v176, s71, v92\n"
    "v_mfma_f32_16x16x32_bf16 v[104:107], a[40:43], a[200:203], v[104:107]\n"
    "ds_write_b64 v20, v[172:173] offset:0\n"
    "v_mfma_f32_16x16x32_bf16 v[104:107], a[36:39], a[196:199], v[104:107]\n"
    "v_fmac_f32 v177, s71, v93\n"
    "v_fmac_f32 v178, s71, v94\n"
    "v_mfma_f32_16x16x32_bf16 v[104:107], a[32:35], a[192:195], v[104:107]\n"
    "ds_read_b32 v38, v36 offset:30592\n"
    "v_fmac_f32 v179, s71, v95\n"
    "v_mfma_f32_16x16x32_bf16 v[104:107], a[28:31], a[188:191], v[104:107]\n"
    "ds_write_b64 v20, v[174:175] offset:8\n"
    "v_mfma_f32_16x16x32_bf16 v[120:123], a[56:59], a[192:195], v[120:123]\n"
    "v_fmac_f32 v192, s71, v108\n"
    "v_fmac_f32 v193, s71, v109\n"
    "v_mfma_f32_16x16x32_bf16 v[120:123], a[60:63], a[196:199], v[120:123]\n"
    "ds_write_b64 v20, v[176:177] offset:1024\n"
    "v_mfma_f32_16x16x32_bf16 v[120:123], a[64:67], a[200:203], v[120:123]\n"
    "v_fmac_f32 v194, s71, v110\n"
    "v_fmac_f32 v195, s71, v111\n"
    "v_mfma_f32_16x16x32_bf16 v[120:123], a[68:71], a[204:207], v[120:123]\n"
    "s_waitcnt lgkmcnt(2)\n"
    "v_cmp_gt_u32 vcc, 11, v38\n"
    "v_fmac_f32 v184, s71, v100\n"
    "v_mfma_f32_16x16x32_bf16 v[120:123], a[72:75], a[208:211], v[120:123]\n"
    "ds_write_b64 v20, v[178:179] offset:1032\n"
    "v_mfma_f32_16x16x32_bf16 v[120:123], a[76:79], a[212:215], v[120:123]\n"
    "s_cbranch_vccnz .Lsl11\n"
    ".Lbk11:\n"
    "v_mfma_f32_16x16x32_bf16 v[120:123], a[80:83], a[216:219], v[120:123]\n"
    "ds_read_b128 a[220:223], v13 offset:26624\n"
    "v_fmac_f32 v185, s71, v101\n"
    "v_mfma_f32_16x16x32_bf16 v[112:115], a[24:27], a[216:219], v[112:115]\n"
    "ds_read_b128 a[224:227], v13 offset:25600\n"
    "v_fmac_f32 v186, s71, v102\n"
    "v_mfma_f32_16x16x32_bf16 v[112:115], a[20:23], a[212:215], v[112:115]\n"
    "ds_read_b128 a[228:231], v13 offset:24576\n"
    "v_fmac_f32 v187, s71, v103\n"
    "v_mfma_f32_16x16x32_bf16 v[112:115], a[16:19], a[208:211], v[112:115]\n"
    "ds_read_b128 a[232:235], v13 offset:23552\n"
    "v_fmac_f32 v188, s71, v104\n"
    "v_mfma_f32_16x16x32_bf16 v[112:115], a[12:15], a[204:207], v[112:115]\n"
    "ds_read_b128 a[236:239], v13 offset:22528\n"
    "v_fmac_f32 v189, s71, v105\n"
    "v_mfma_f32_16x16x32_bf16 v[112:115], a[8:11], a[200:203], v[112:115]\n"
    "ds_read_b128 a[168:171], v13 offset:21504\n"
    "v_fmac_f32 v190, s71, v106\n"
    "v_mfma_f32_16x16x32_bf16 v[112:115], a[4:7], a[196:199], v[112:115]\n"
    "ds_read_b128 a[172:175], v13 offset:20480\n"
    "v_fmac_f32 v191, s71, v107\n"
    "v_mfma_f32_16x16x32_bf16 v[112:115], a[0:3], a[192:195], v[112:115]\n"
    "ds_read_b128 a[176:179], v13 offset:9216\n"
    "v_add_u32 v37, 1, v37\n"
    "v_mfma_f32_16x16x32_bf16 v[116:119], a[28:31], a[192:195], v[116:119]\n"
    "ds_read_b128 a[180:183], v13 offset:27648\n"
    "v_fmac_f32 v204, s71, v120\n"
    "v_mfma_f32_16x16x32_bf16 v[116:119], a[32:35], a[196:199], v[116:119]\n"
    "ds_write_b64 v20, v[192:193] offset:5120\n"
    "v_mfma_f32_16x16x32_bf16 v[116:119], a[36:39], a[200:203], v[116:119]\n"
    "v_fmac_f32 v205, s71, v121\n"
    "v_fmac_f32 v206, s71, v122\n"
    "v_mfma_f32_16x16x32_bf16 v[116:119], a[40:43], a[204:207], v[116:119]\n"
    "ds_write_b64 v20, v[194:195] offset:5128\n"
    "v_mfma_f32_16x16x32_bf16 v[116:119], a[44:47], a[208:211], v[116:119]\n"
    "s_waitcnt vmcnt(0)\n"
    "v_fmac_f32 v207, s71, v123\n"
    "v_mfma_f32_16x16x32_bf16 v[116:119], a[48:51], a[212:215], v[116:119]\n"
    "ds_write_b64 v20, v[184:185] offset:3072\n"
    "v_mfma_f32_16x16x32_bf16 v[116:119], a[52:55], a[216:219], v[116:119]\n"
    "s_waitcnt lgkmcnt(6)\n"
    "ds_read_b128 a[184:187], v13 offset:10240\n"
    "v_fmac_f32 v196, s71, v112\n"
    "v_mfma_f32_16x16x32_bf16 v[96:99], a[164:167], a[220:223], v[96:99]\n"
    "ds_write_b64 v20, v[186:187] offset:3080\n"
    "v_mfma_f32_16x16x32_bf16 v[96:99], a[160:163], a[224:227], v[96:99]\n"
    "v_fmac_f32 v197, s71, v113\n"
    "v_fmac_f32 v198, s71, v114\n"
    "v_mfma_f32_16x16x32_bf16 v[96:99], a[156:159], a[228:231], v[96:99]\n"
    "ds_write_b64 v20, v[188:189] offset:4096\n"
    "v_mfma_f32_16x16x32_bf16 v[96:99], a[152:155], a[232:235], v[96:99]\n"
    "s_waitcnt lgkmcnt(4)\n"
    "v_fmac_f32 v199, s71, v115\n"
    "v_mfma_f32_16x16x32_bf16 v[96:99], a[148:151], a[236:239], v[96:99]\n"
    "ds_write_b64 v20, v[190:191] offset:4104\n"
    "v_mfma_f32_16x16x32_bf16 v[96:99], a[144:147], a[168:171], v[96:99]\n"
    "ds_write_b32 v35, v37\n"
    "v_mfma_f32_16x16x32_bf16 v[96:99], a[140:143], a[172:175], v[96:99]\n"
    "ds_write_b64 v20, v[204:205] offset:8192\n"
    "v_mfma_f32_16x16x32_bf16 v[88:91], a[84:87], a[172:175], v[88:91]\n"
    "v_fmac_f32 v200, s71, v116\n"
    "v_fmac_f32 v201, s71, v117\n"
    "v_mfma_f32_16x16x32_bf16 v[88:91], a[88:91], a[168:171], v[88:91]\n"
    "ds_write_b64 v20, v[206:207] offset:8200\n"
    "v_mfma_f32_16x16x32_bf16 v[88:91], a[92:95], a[236:239], v[88:91]\n"
    "v_fmac_f32 v202, s71, v118\n"
    "v_fmac_f32 v203, s71, v119\n"
    "v_mfma_f32_16x16x32_bf16 v[88:91], a[96:99], a[232:235], v[88:91]\n"
    "ds_write_b64 v20, v[196:197] offset:6144\n"
    "v_mfma_f32_16x16x32_bf16 v[88:91], a[100:103], a[228:231], v[88:91]\n"
    "ds_read_b32 v38, v36 offset:30592\n"
    "v_add_u32 v37, 1, v37\n"
    "v_mfma_f32_16x16x32_bf16 v[88:91], a[104:107], a[224:227], v[88:91]\n"
    "ds_write_b64 v20, v[198:199] offset:6152\n"
    "v_mfma_f32_16x16x32_bf16 v[88:91], a[108:111], a[220:223], v[88:91]\n"
    "v_mfma_f32_16x16x32_bf16 v[92:95], a[136:139], a[220:223], v[92:95]\n"
    "ds_read_b128 a[188:191], v13 offset:28672\n"
    "v_mfma_f32_16x16x32_bf16 v[92:95], a[132:135], a[224:227], v[92:95]\n"
    "ds_write_b64 v20, v[200:201] offset:7168\n"
    "v_mfma_f32_16x16x32_bf16 v[92:95], a[128:131], a[228:231], v[92:95]\n"
    "s_waitcnt lgkmcnt(3)\n"
    "v_cmp_gt_u32 vcc, 12, v38\n"
    "v_mfma_f32_16x16x32_bf16 v[92:95], a[124:127], a[232:235], v[92:95]\n"
    "ds_write_b64 v20, v[202:203] offset:7176\n"
    "v_mfma_f32_16x16x32_bf16 v[92:95], a[120:123], a[236:239], v[92:95]\n"
    "s_cbranch_vccnz .Lsl12\n"
    ".Lbk12:\n"
    "v_mfma_f32_16x16x32_bf16 v[92:95], a[116:119], a[168:171], v[92:95]\n"
    "ds_read_b128 v[180:183], v21 offset:2048\n"
    "v_mfma_f32_16x16x32_bf16 v[92:95], a[112:115], a[172:175], v[92:95]\n"
    "ds_read_b128 v[172:175], v21 offset:0\n"
    "v_mfma_f32_16x16x32_bf16 v[132:135], a[56:59], a[196:199], v[132:135]\n"
    "ds_read_b128 v[176:179], v21 offset:1024\n"
    "v_mfma_f32_16x16x32_bf16 v[132:135], a[60:63], a[200:203], v[132:135]\n"
    "ds_read_b128 v[192:195], v21 offset:5120\n"
    "v_mfma_f32_16x16x32_bf16 v[132:135], a[64:67], a[204:207], v[132:135]\n"
    "ds_read_b128 v[184:187], v21 offset:3072\n"
    "v_mfma_f32_16x16x32_bf16 v[132:135], a[68:71], a[208:211], v[132:135]\n"
    "ds_read_b128 v[188:191], v21 offset:4096\n"
    "v_mfma_f32_16x16x32_bf16 v[132:135], a[72:75], a[212:215], v[132:135]\n"
    "ds_write_b32 v35, v37\n"
    "v_mfma_f32_16x16x32_bf16 v[132:135], a[76:79], a[216:219], v[132:135]\n"
    "s_waitcnt lgkmcnt(6)\n"
    "v_add_f32 v96, v96, v180\n"
    "v_add_f32 v97, v97, v181\n"
    "v_mfma_f32_16x16x32_bf16 v[132:135], a[80:83], a[176:179], v[132:135]\n"
    "v_add_f32 v98, v98, v182\n"
    "v_add_f32 v99, v99, v183\n"
    "v_mfma_f32_16x16x32_bf16 v[124:127], a[24:27], a[176:179], v[124:127]\n"
    "s_waitcnt lgkmcnt(5)\n"
    "v_add_f32 v88, v88, v172\n"
    "v_add_f32 v89, v89, v173\n"
    "v_mfma_f32_16x16x32_bf16 v[124:127], a[20:23], a[216:219], v[124:127]\n"
    "v_add_f32 v90, v90, v174\n"
    "v_add_f32 v91, v91, v175\n"
    "v_mfma_f32_16x16x32_bf16 v[124:127], a[16:19], a[212:215], v[124:127]\n"
    "s_waitcnt lgkmcnt(4)\n"
    "v_add_f32 v92, v92, v176\n"
    "v_add_f32 v93, v93, v177\n"
    "v_mfma_f32_16x16x32_bf16 v[124:127], a[12:15], a[208:211], v[124:127]\n"
    "v_add_f32 v94, v94, v178\n"
    "v_add_f32 v95, v95, v179\n"
    "v_mfma_f32_16x16x32_bf16 v[124:127], a[8:11], a[204:207], v[124:127]\n"
    "v_cvt_pk_bf16_f32 v172, v88, v89\n"
    "v_cvt_pk_bf16_f32 v173, v90, v91\n"
    "v_mfma_f32_16x16x32_bf16 v[124:127], a[4:7], a[200:203], v[124:127]\n"
    "ds_bpermute_b32 v88, v22, v172\n"
    "v_mfma_f32_16x16x32_bf16 v[124:127], a[0:3], a[196:199], v[124:127]\n"
    "ds_bpermute_b32 v89, v22, v173\n"
    "v_mfma_f32_16x16x32_bf16 v[128:131], a[28:31], a[196:199], v[128:131]\n"
    "s_mov_b32 s73, 0\n"
    "v_cvt_pk_bf16_f32 v176, v92, v93\n"
    "v_cvt_pk_bf16_f32 v177, v94, v95\n"
    "v_mfma_f32_16x16x32_bf16 v[128:131], a[32:35], a[200:203], v[128:131]\n"
    "ds_bpermute_b32 v90, v22, v176\n"
    "v_mfma_f32_16x16x32_bf16 v[128:131], a[36:39], a[204:207], v[128:131]\n"
    "ds_bpermute_b32 v91, v22, v177\n"
    "v_mfma_f32_16x16x32_bf16 v[128:131], a[40:43], a[208:211], v[128:131]\n"
    "s_waitcnt lgkmcnt(4)\n"
    "v_add_f32 v108, v108, v192\n"
    "v_add_f32 v109, v109, v193\n"
    "v_mfma_f32_16x16x32_bf16 v[128:131], a[44:47], a[212:215], v[128:131]\n"
    "v_add_f32 v110, v110, v194\n"
    "v_add_f32 v111, v111, v195\n"
    "v_mfma_f32_16x16x32_bf16 v[128:131], a[48:51], a[216:219], v[128:131]\n"
    "v_add_f32 v100, v100, v184\n"
    "v_add_f32 v101, v101, v185\n"
    "v_mfma_f32_16x16x32_bf16 v[128:131], a[52:55], a[176:179], v[128:131]\n"
    "ds_read_b128 a[192:195], v13 offset:11264\n"
    "v_add_f32 v102, v102, v186\n"
    "v_mfma_f32_16x16x32_bf16 v[108:111], a[164:167], a[180:183], v[108:111]\n"
    "s_waitcnt lgkmcnt(1)\n"
    "v_add_f32 v103, v103, v187\n"
    "v_add_f32 v104, v104, v188\n"
    "v_mfma_f32_16x16x32_bf16 v[108:111], a[160:163], a[220:223], v[108:111]\n"
    "buffer_store_dwordx4 v[88:91], v25, s[28:31], s73 offen sc1\n"
    "v_mfma_f32_16x16x32_bf16 v[108:111], a[156:159], a[224:227], v[108:111]\n"
    "v_add_f32 v105, v105, v189\n"
    "v_add_f32 v106, v106, v190\n"
    "v_mfma_f32_16x16x32_bf16 v[108:111], a[152:155], a[228:231], v[108:111]\n"
    "ds_read_b32 v38, v36 offset:30592\n"
    "v_add_f32 v107, v107, v191\n"
    "v_mfma_f32_16x16x32_bf16 v[108:111], a[148:151], a[232:235], v[108:111]\n"
    "v_cvt_pk_bf16_f32 v180, v96, v97\n"
    "v_cvt_pk_bf16_f32 v181, v98, v99\n"
    "v_mfma_f32_16x16x32_bf16 v[108:111], a[144:147], a[236:239], v[108:111]\n"
    "ds_write_b64 v23, v[180:181] offset:14336\n"
    "v_mfma_f32_16x16x32_bf16 v[108:111], a[140:143], a[168:171], v[108:111]\n"
    "v_fmac_f32 v216, s71, v132\n"
    "v_fmac_f32 v217, s71, v133\n"
    "v_mfma_f32_16x16x32_bf16 v[100:103], a[84:87], a[168:171], v[100:103]\n"
    "v_fmac_f32 v218, s71, v134\n"
    "v_fmac_f32 v219, s71, v135\n"
    "v_mfma_f32_16x16x32_bf16 v[100:103], a[88:91], a[236:239], v[100:103]\n"
    "s_waitcnt lgkmcnt(1)\n"
    "v_cmp_gt_u32 vcc, 13, v38\n"
    "v_fmac_f32 v208, s71, v124\n"
    "v_mfma_f32_16x16x32_bf16 v[100:103], a[92:95], a[232:235], v[100:103]\n"
    "ds_write_b64 v20, v[216:217] offset:11264\n"
    "v_mfma_f32_16x16x32_bf16 v[100:103], a[96:99], a[228:231], v[100:103]\n"
    "s_cbranch_vccnz .Lsl13\n"
    ".Lbk13:\n"
    "v_mfma_f32_16x16x32_bf16 v[100:103], a[100:103], a[224:227], v[100:103]\n"
    "ds_read_b128 v[204:207], v21 offset:8192\n"
    "v_fmac_f32 v209, s71, v125\n"
    "v_mfma_f32_16x16x32_bf16 v[100:103], a[104:107], a[220:223], v[100:103]\n"
    "ds_read_b128 v[196:199], v21 offset:6144\n"
    "v_fmac_f32 v210, s71, v126\n"
    "v_mfma_f32_16x16x32_bf16 v[100:103], a[108:111], a[180:183], v[100:103]\n"
    "ds_read_b128 v[200:203], v21 offset:7168\n"
    "v_fmac_f32 v211, s71, v127\n"
    "v_mfma_f32_16x16x32_bf16 v[104:107], a[136:139], a[180:183], v[104:107]\n"
    "ds_read_b128 a[172:175], v13 offset:29696\n"
    "v_fmac_f32 v212, s71, v128\n"
    "v_mfma_f32_16x16x32_bf16 v[104:107], a[132:135], a[220:223], v[104:107]\n"
    "ds_write_b64 v20, v[218:219] offset:11272\n"
    "v_mfma_f32_16x16x32_bf16 v[104:107], a[128:131], a[224:227], v[104:107]\n"
    "v_fmac_f32 v213, s71, v129\n"
    "v_fmac_f32 v214, s71, v130\n"
    "v_mfma_f32_16x16x32_bf16 v[104:107], a[124:127], a[228:231], v[104:107]\n"
    "ds_write_b64 v20, v[208:209] offset:9216\n"
    "v_mfma_f32_16x16x32_bf16 v[104:107], a[120:123], a[232:235], v[104:107]\n"
    "s_waitcnt lgkmcnt(5)\n"
    "v_add_f32 v120, v120, v204\n"
    "v_add_f32 v121, v121, v205\n"
    "v_mfma_f32_16x16x32_bf16 v[104:107], a[116:119], a[236:239], v[104:107]\n"
    "v_add_f32 v122, v122, v206\n"
    "v_add_f32 v123, v123, v207\n"
    "v_mfma_f32_16x16x32_bf16 v[104:107], a[112:115], a[168:171], v[104:107]\n"
    "v_cvt_pk_bf16_f32 v184, v100, v101\n"
    "v_cvt_pk_bf16_f32 v185, v102, v103\n"
    "v_mfma_f32_16x16x32_bf16 v[144:147], a[56:59], a[200:203], v[144:147]\n"
    "ds_bpermute_b32 v100, v22, v184\n"
    "v_mfma_f32_16x16x32_bf16 v[144:147], a[60:63], a[204:207], v[144:147]\n"
    "ds_bpermute_b32 v101, v22, v185\n"
    "v_mfma_f32_16x16x32_bf16 v[144:147], a[64:67], a[208:211], v[144:147]\n"
    "s_waitcnt lgkmcnt(4)\n"
    "v_add_f32 v112, v112, v196\n"
    "v_add_f32 v113, v113, v197\n"
    "v_mfma_f32_16x16x32_bf16 v[144:147], a[68:71], a[212:215], v[144:147]\n"
    "v_add_f32 v114, v114, v198\n"
    "v_add_f32 v115, v115, v199\n"
    "v_mfma_f32_16x16x32_bf16 v[144:147], a[72:75], a[216:219], v[144:147]\n"
    "v_add_f32 v116, v116, v200\n"
    "v_add_f32 v117, v117, v201\n"
    "v_mfma_f32_16x16x32_bf16 v[144:147], a[76:79], a[176:179], v[144:147]\n"
    "v_add_f32 v118, v118, v202\n"
    "v_add_f32 v119, v119, v203\n"
    "v_mfma_f32_16x16x32_bf16 v[144:147], a[80:83], a[184:187], v[144:147]\n"
    "s_mov_b32 s73, 86016\n"
    "v_cvt_pk_bf16_f32 v188, v104, v105\n"
    "v_cvt_pk_bf16_f32 v189, v106, v107\n"
    "v_mfma_f32_16x16x32_bf16 v[136:139], a[24:27], a[184:187], v[136:139]\n"
    "ds_bpermute_b32 v102, v22, v188\n"
    "v_mfma_f32_16x16x32_bf16 v[136:139], a[20:23], a[176:179], v[136:139]\n"
    "ds_bpermute_b32 v103, v22, v189\n"
    "v_mfma_f32_16x16x32_bf16 v[136:139], a[16:19], a[216:219], v[136:139]\n"
    "ds_write_b64 v20, v[210:211] offset:9224\n"
    "v_mfma_f32_16x16x32_bf16 v[136:139], a[12:15], a[212:215], v[136:139]\n"
    "v_fmac_f32 v215, s71, v131\n"
    "v_add_u32 v37, 1, v37\n"
    "v_mfma_f32_16x16x32_bf16 v[136:139], a[8:11], a[208:211], v[136:139]\n"
    "ds_write_b64 v20, v[212:213] offset:10240\n"
    "v_mfma_f32_16x16x32_bf16 v[136:139], a[4:7], a[204:207], v[136:139]\n"
    "s_waitcnt lgkmcnt(2)\n"
    "v_cvt_pk_bf16_f32 v192, v108, v109\n"
    "v_cvt_pk_bf16_f32 v193, v110, v111\n"
    "v_mfma_f32_16x16x32_bf16 v[136:139], a[0:3], a[200:203], v[136:139]\n"
    "ds_write_b64 v20, v[214:215] offset:10248\n"
    "v_mfma_f32_16x16x32_bf16 v[140:143], a[28:31], a[200:203], v[140:143]\n"
    "buffer_store_dwordx4 v[100:103], v25, s[28:31], s73 offen sc1\n"
    "v_mfma_f32_16x16x32_bf16 v[140:143], a[32:35], a[204:207], v[140:143]\n"
    "ds_write_b32 v35, v37\n"
    "v_mfma_f32_16x16x32_bf16 v[140:143], a[36:39], a[208:211], v[140:143]\n"
    "ds_write_b64 v23, v[192:193] offset:14848\n"
    "v_mfma_f32_16x16x32_bf16 v[140:143], a[40:43], a[212:215], v[140:143]\n"
    ".Lpad6:\n"
    "s_cbranch_execz .Lhop_done\n"
    "v_fmac_f32 v228, s71, v144\n"
    "v_fmac_f32 v229, s71, v145\n"
    "v_mfma_f32_16x16x32_bf16 v[140:143], a[44:47], a[216:219], v[140:143]\n"
    "v_fmac_f32 v230, s71, v146\n"
    "v_fmac_f32 v231, s71, v147\n"
    "v_mfma_f32_16x16x32_bf16 v[140:143], a[48:51], a[176:179], v[140:143]\n"
    "ds_write_b64 v20, v[228:229] offset:21504\n"
    "v_mfma_f32_16x16x32_bf16 v[140:143], a[52:55], a[184:187], v[140:143]\n"
    "ds_read_b128 a[196:199], v13 offset:12288\n"
    "v_fmac_f32 v220, s71, v136\n"
    "v_mfma_f32_16x16x32_bf16 v[120:123], a[164:167], a[188:191], v[120:123]\n"
    "ds_read_b32 v38, v36 offset:30592\n"
    "v_fmac_f32 v221, s71, v137\n"
    "v_mfma_f32_16x16x32_bf16 v[120:123], a[160:163], a[180:183], v[120:123]\n"
    "ds_write_b64 v20, v[230:231] offset:21512\n"
    "v_mfma_f32_16x16x32_bf16 v[120:123], a[156:159], a[220:223], v[120:123]\n"
    "v_fmac_f32 v222, s71, v138\n"
    "v_fmac_f32 v223, s71, v139\n"
    "v_mfma_f32_16x16x32_bf16 v[120:123], a[152:155], a[224:227], v[120:123]\n"
    "ds_write_b64 v20, v[220:221] offset:12288\n"
    "v_mfma_f32_16x16x32_bf16 v[120:123], a[148:151], a[228:231], v[120:123]\n"
    "v_mfma_f32_16x16x32_bf16 v[120:123], a[144:147], a[232:235], v[120:123]\n"
    "s_waitcnt lgkmcnt(2)\n"
    "v_cmp_gt_u32 vcc, 14, v38\n"
    "v_mfma_f32_16x16x32_bf16 v[120:123], a[140:143], a[236:239], v[120:123]\n"
    "s_mov_b32 s74, 0\n"
    "ds_write_b64 v20, v[222:223] offset:12296\n"
    "v_mfma_f32_16x16x32_bf16 v[112:115], a[84:87], a[236:239], v[112:115]\n"
    "s_cbranch_vccnz .Lsl14\n"
    ".Lbk14:\n"
    "v_mfma_f32_16x16x32_bf16 v[112:115], a[88:91], a[232:235], v[112:115]\n"
    "ds_read_b64 v[182:183], v24 offset:14336\n"
    "v_fmac_f32 v224, s71, v140\n"
    "v_mfma_f32_16x16x32_bf16 v[112:115], a[92:95], a[228:231], v[112:115]\n"
    "ds_read_b128 v[216:219], v21 offset:11264\n"
    "v_fmac_f32 v225, s71, v141\n"
    "v_mfma_f32_16x16x32_bf16 v[112:115], a[96:99], a[224:227], v[112:115]\n"
    "ds_read_b128 v[208:211], v21 offset:9216\n"
    "v_fmac_f32 v226, s71, v142\n"
    "v_mfma_f32_16x16x32_bf16 v[112:115], a[100:103], a[220:223], v[112:115]\n"
    "ds_read_b128 v[212:215], v21 offset:10240\n"
    "v_fmac_f32 v227, s71, v143\n"
    "v_mfma_f32_16x16x32_bf16 v[112:115], a[104:107], a[180:183], v[112:115]\n"
    "ds_write_b64 v20, v[224:225] offset:13312\n"
    "v_mfma_f32_16x16x32_bf16 v[112:115], a[108:111], a[188:191], v[112:115]\n"
    "v_add_u32 v37, 1, v37\n"
    "v_cvt_pk_bf16_f32 v204, v120, v121\n"
    "v_mfma_f32_16x16x32_bf16 v[116:119], a[136:139], a[188:191], v[116:119]\n"
    "s_waitcnt lgkmcnt(4)\n"
    "ds_read_b128 a[168:171], v13 offset:30720\n"
    "v_cvt_pk_bf16_f32 v205, v122, v123\n"
    "v_mfma_f32_16x16x32_bf16 v[116:119], a[132:135], a[180:183], v[116:119]\n"
    "buffer_store_dwordx2 v[182:183], v26, s[28:31], s74 offen sc1\n"
    "v_mfma_f32_16x16x32_bf16 v[116:119], a[128:131], a[220:223], v[116:119]\n"
    "s_waitcnt lgkmcnt(4)\n"
    "v_add_f32 v132, v132, v216\n"
    "v_add_f32 v133, v133, v217\n"
    "v_mfma_f32_16x16x32_bf16 v[116:119], a[124:127], a[224:227], v[116:119]\n"
    "v_add_f32 v134, v134, v218\n"
    "v_add_f32 v135, v135, v219\n"
    "v_mfma_f32_16x16x32_bf16 v[116:119], a[120:123], a[228:231], v[116:119]\n"
    "s_waitcnt lgkmcnt(3)\n"
    "v_add_f32 v124, v124, v208\n"
    "v_add_f32 v125, v125, v209\n"
    "v_mfma_f32_16x16x32_bf16 v[116:119], a[116:119], a[232:235], v[116:119]\n"
    "v_add_f32 v126, v126, v210\n"
    "v_add_f32 v127, v127, v211\n"
    "v_mfma_f32_16x16x32_bf16 v[116:119], a[112:115], a[236:239], v[116:119]\n"
    "v_cvt_pk_bf16_f32 v196, v112, v113\n"
    "v_cvt_pk_bf16_f32 v197, v114, v115\n"
    "v_mfma_f32_16x16x32_bf16 v[156:159], a[56:59], a[204:207], v[156:159]\n"
    "ds_bpermute_b32 v112, v22, v196\n"
    "v_mfma_f32_16x16x32_bf16 v[156:159], a[60:63], a[208:211], v[156:159]\n"
    "ds_bpermute_b32 v113, v22, v197\n"
    "v_mfma_f32_16x16x32_bf16 v[156:159], a[64:67], a[212:215], v[156:159]\n"
    "s_waitcnt lgkmcnt(2)\n"
    "v_add_f32 v128, v128, v212\n"
    "v_add_f32 v129, v129, v213\n"
    "v_mfma_f32_16x16x32_bf16 v[156:159], a[68:71], a[216:219], v[156:159]\n"
    "v_add_f32 v130, v130, v214\n"
    "v_add_f32 v131, v131, v215\n"
    "v_mfma_f32_16x16x32_bf16 v[156:159], a[72:75], a[176:179], v[156:159]\n"
    "ds_write_b64 v20, v[226:227] offset:13320\n"
    "v_mfma_f32_16x16x32_bf16 v[156:159], a[76:79], a[184:187], v[156:159]\n"
    "ds_write_b32 v35, v37\n"
    "v_mfma_f32_16x16x32_bf16 v[156:159], a[80:83], a[192:195], v[156:159]\n"
    "ds_read_b128 a[200:203], v13 offset:31744\n"
    "v_cvt_pk_bf16_f32 v200, v116, v117\n"
    "v_mfma_f32_16x16x32_bf16 v[148:151], a[24:27], a[192:195], v[148:151]\n"
    "s_mov_b32 s73, 172032\n"
    "v_cvt_pk_bf16_f32 v201, v118, v119\n"
    "v_mfma_f32_16x16x32_bf16 v[148:151], a[20:23], a[184:187], v[148:151]\n"
    "ds_bpermute_b32 v114, v22, v200\n"
    "v_mfma_f32_16x16x32_bf16 v[148:151], a[16:19], a[176:179], v[148:151]\n"
    "ds_bpermute_b32 v115, v22, v201\n"
    "v_mfma_f32_16x16x32_bf16 v[148:151], a[12:15], a[216:219], v[148:151]\n"
    "ds_write_b64 v23, v[204:205] offset:15360\n"
    "v_mfma_f32_16x16x32_bf16 v[148:151], a[8:11], a[212:215], v[148:151]\n"
    "ds_read_b32 v38, v36 offset:30592\n"
    "v_mfma_f32_16x16x32_bf16 v[148:151], a[4:7], a[208:211], v[148:151]\n"
    "v_mfma_f32_16x16x32_bf16 v[148:151], a[0:3], a[204:207], v[148:151]\n"
    "v_fmac_f32 v240, s71, v156\n"
    "v_fmac_f32 v241, s71, v157\n"
    "v_mfma_f32_16x16x32_bf16 v[152:155], a[28:31], a[204:207], v[152:155]\n"
    "s_waitcnt lgkmcnt(2)\n"
    "v_fmac_f32 v242, s71, v158\n"
    "v_fmac_f32 v243, s71, v159\n"
    "v_mfma_f32_16x16x32_bf16 v[152:155], a[32:35], a[208:211], v[152:155]\n"
    "buffer_store_dwordx4 v[112:115], v25, s[28:31], s73 offen sc1\n"
    "v_mfma_f32_16x16x32_bf16 v[152:155], a[36:39], a[212:215], v[152:155]\n"
    "s_waitcnt lgkmcnt(0)\n"
    "v_cmp_gt_u32 vcc, 15, v38\n"
    "v_mfma_f32_16x16x32_bf16 v[152:155], a[40:43], a[216:219], v[152:155]\n"
    "s_mov_b32 s74, 86016\n"
    "ds_write_b64 v20, v[240:241] offset:24576\n"
    "v_mfma_f32_16x16x32_bf16 v[152:155], a[44:47], a[176:179], v[152:155]\n"
    "s_cbranch_vccnz .Lsl15\n"
    ".Lbk15:\n"
    "v_mfma_f32_16x16x32_bf16 v[152:155], a[48:51], a[184:187], v[152:155]\n"
    "ds_read_b64 v[194:195], v24 offset:14848\n"
    "v_mfma_f32_16x16x32_bf16 v[152:155], a[52:55], a[192:195], v[152:155]\n"
    "ds_read_b128 v[228:231], v21 offset:21504\n"
    "v_fmac_f32 v232, s71, v148\n"
    "v_mfma_f32_16x16x32_bf16 v[132:135], a[164:167], a[172:175], v[132:135]\n"
    "ds_read_b128 a[236:239], v13 offset:32768\n"
    "v_fmac_f32 v233, s71, v149\n"
    "v_mfma_f32_16x16x32_bf16 v[132:135], a[160:163], a[188:191], v[132:135]\n"
    "ds_read_b128 v[220:223], v21 offset:12288\n"
    "v_fmac_f32 v234, s71, v150\n"
    "v_mfma_f32_16x16x32_bf16 v[132:135], a[156:159], a[180:183], v[132:135]\n"
    "s_waitcnt lgkmcnt(3)\n"
    "ds_read_b128 v[224:227], v21 offset:13312\n"
    "v_fmac_f32 v235, s71, v151\n"
    "v_mfma_f32_16x16x32_bf16 v[132:135], a[152:155], a[220:223], v[132:135]\n"
    "ds_write_b64 v20, v[242:243] offset:24584\n"
    "v_mfma_f32_16x16x32_bf16 v[132:135], a[148:151], a[224:227], v[132:135]\n"
    "buffer_store_dwordx2 v[194:195], v26, s[28:31], s74 offen sc1\n"
    "v_mfma_f32_16x16x32_bf16 v[132:135], a[144:147], a[228:231], v[132:135]\n"
    "ds_write_b64 v20, v[232:233] offset:22528\n"
    "v_mfma_f32_16x16x32_bf16 v[132:135], a[140:143], a[232:235], v[132:135]\n"
    "s_waitcnt lgkmcnt(5)\n"
    "v_add_f32 v144, v144, v228\n"
    "v_add_f32 v145, v145, v229\n"
    "v_mfma_f32_16x16x32_bf16 v[124:127], a[84:87], a[232:235], v[124:127]\n"
    "v_add_f32 v146, v146, v230\n"
    "v_add_f32 v147, v147, v231\n"
    "v_mfma_f32_16x16x32_bf16 v[124:127], a[88:91], a[228:231], v[124:127]\n"
    "s_waitcnt lgkmcnt(3)\n"
    "v_add_f32 v136, v136, v220\n"
    "v_add_f32 v137, v137, v221\n"
    "v_mfma_f32_16x16x32_bf16 v[124:127], a[92:95], a[224:227], v[124:127]\n"
    "v_add_f32 v138, v138, v222\n"
    "v_add_f32 v139, v139, v223\n"
    "v_mfma_f32_16x16x32_bf16 v[124:127], a[96:99], a[220:223], v[124:127]\n"
    "s_waitcnt lgkmcnt(2)\n"
    "v_add_f32 v140, v140, v224\n"
    "v_add_f32 v141, v141, v225\n"
    "v_mfma_f32_16x16x32_bf16 v[124:127], a[100:103], a[180:183], v[124:127]\n"
    "v_add_f32 v142, v142, v226\n"
    "v_add_f32 v143, v143, v227\n"
    "v_mfma_f32_16x16x32_bf16 v[124:127], a[104:107], a[188:191], v[124:127]\n"
    "ds_write_b64 v20, v[234:235] offset:22536\n"
    "v_mfma_f32_16x16x32_bf16 v[124:127], a[108:111], a[172:175], v[124:127]\n"
    "v_fmac_f32 v236, s71, v152\n"
    "v_fmac_f32 v237, s71, v153\n"
    "v_mfma_f32_16x16x32_bf16 v[128:131], a[136:139], a[172:175], v[128:131]\n"
    "v_fmac_f32 v238, s71, v154\n"
    "v_fmac_f32 v239, s71, v155\n"
    "v_mfma_f32_16x16x32_bf16 v[128:131], a[132:135], a[188:191], v[128:131]\n"
    "ds_write_b64 v20, v[236:237] offset:23552\n"
    "v_mfma_f32_16x16x32_bf16 v[128:131], a[128:131], a[180:183], v[128:131]\n"
    "v_add_u32 v37, 1, v37\n"
    "v_cvt_pk_bf16_f32 v216, v132, v133\n"
    "v_mfma_f32_16x16x32_bf16 v[128:131], a[124:127], a[220:223], v[128:131]\n"
    "ds_write_b64 v20, v[238:239] offset:23560\n"
    "v_mfma_f32_16x16x32_bf16 v[128:131], a[120:123], a[224:227], v[128:131]\n"
    "ds_write_b32 v35, v37\n"
    "v_mfma_f32_16x16x32_bf16 v[128:131], a[116:119], a[228:231], v[128:131]\n"
    "v_cvt_pk_bf16_f32 v217, v134, v135\n"
    "v_mfma_f32_16x16x32_bf16 v[128:131], a[112:115], a[232:235], v[128:131]\n"
    "v_cvt_pk_bf16_f32 v208, v124, v125\n"
    "v_cvt_pk_bf16_f32 v209, v126, v127\n"
    "v_mfma_f32_16x16x32_bf16 v[168:171], a[56:59], a[208:211], v[168:171]\n"
    "ds_bpermute_b32 v124, v22, v208\n"
    "v_mfma_f32_16x16x32_bf16 v[168:171], a[60:63], a[212:215], v[168:171]\n"
    "ds_bpermute_b32 v125, v22, v209\n"
    "v_mfma_f32_16x16x32_bf16 v[168:171], a[64:67], a[216:219], v[168:171]\n"
    "ds_write_b64 v23, v[216:217] offset:15872\n"
    "v_mfma_f32_16x16x32_bf16 v[168:171], a[68:71], a[176:179], v[168:171]\n"
    "ds_read_b32 v38, v36 offset:30592\n"
    "v_mfma_f32_16x16x32_bf16 v[168:171], a[72:75], a[184:187], v[168:171]\n"
    "v_mfma_f32_16x16x32_bf16 v[168:171], a[76:79], a[192:195], v[168:171]\n"
    "v_mfma_f32_16x16x32_bf16 v[168:171], a[80:83], a[196:199], v[168:171]\n"
    "s_mov_b32 s73, 258048\n"
    "v_cvt_pk_bf16_f32 v212, v128, v129\n"
    "v_cvt_pk_bf16_f32 v213, v130, v131\n"
    "v_mfma_f32_16x16x32_bf16 v[160:163], a[24:27], a[196:199], v[160:163]\n"
    "ds_bpermute_b32 v126, v22, v212\n"
    "v_mfma_f32_16x16x32_bf16 v[160:163], a[20:23], a[192:195], v[160:163]\n"
    "ds_bpermute_b32 v127, v22, v213\n"
    "v_mfma_f32_16x16x32_bf16 v[160:163], a[16:19], a[184:187], v[160:163]\n"
    "s_waitcnt lgkmcnt(2)\n"
    "v_cmp_gt_u32 vcc, 16, v38\n"
    "v_mfma_f32_16x16x32_bf16 v[160:163], a[12:15], a[176:179], v[160:163]\n"
    "s_mov_b32 s74, 172032\n"
    "v_mfma_f32_16x16x32_bf16 v[160:163], a[8:11], a[216:219], v[160:163]\n"
    "s_cbranch_vccnz .Lsl16\n"
    ".Lbk16:\n"
    "v_mfma_f32_16x16x32_bf16 v[160:163], a[4:7], a[212:215], v[160:163]\n"
    "ds_read_b64 v[206:207], v24 offset:15360\n"
    "v_mfma_f32_16x16x32_bf16 v[160:163], a[0:3], a[208:211], v[160:163]\n"
    "s_waitcnt lgkmcnt(1)\n"
    "ds_read_b128 v[240:243], v21 offset:24576\n"
    "v_fmac_f32 v252, s71, v168\n"
    "v_mfma_f32_16x16x32_bf16 v[164:167], a[28:31], a[208:211], v[164:167]\n"
    "buffer_store_dwordx4 v[124:127], v25, s[28:31], s73 offen sc1\n"
    "v_mfma_f32_16x16x32_bf16 v[164:167], a[32:35], a[212:215], v[164:167]\n"
    "ds_read_b128 v[232:235], v21 offset:22528\n"
    "v_fmac_f32 v253, s71, v169\n"
    "v_mfma_f32_16x16x32_bf16 v[164:167], a[36:39], a[216:219], v[164:167]\n"
    "ds_read_b128 v[236:239], v21 offset:23552\n"
    "v_fmac_f32 v254, s71, v170\n"
    "v_mfma_f32_16x16x32_bf16 v[164:167], a[40:43], a[176:179], v[164:167]\n"
    "s_waitcnt lgkmcnt(3)\n"
    "v_fmac_f32 v255, s71, v171\n"
    "v_mfma_f32_16x16x32_bf16 v[164:167], a[44:47], a[184:187], v[164:167]\n"
    "buffer_store_dwordx2 v[206:207], v26, s[28:31], s74 offen sc1\n"
    "v_mfma_f32_16x16x32_bf16 v[164:167], a[48:51], a[192:195], v[164:167]\n"
    "ds_write_b64 v20, v[252:253] offset:27648\n"
    "v_mfma_f32_16x16x32_bf16 v[164:167], a[52:55], a[196:199], v[164:167]\n"
    "s_waitcnt lgkmcnt(3)\n"
    "v_add_f32 v156, v156, v240\n"
    "v_add_f32 v157, v157, v241\n"
    "v_mfma_f32_16x16x32_bf16 v[144:147], a[164:167], a[168:171], v[144:147]\n"
    "v_add_f32 v158, v158, v242\n"
    "v_add_f32 v159, v159, v243\n"
    "v_mfma_f32_16x16x32_bf16 v[144:147], a[160:163], a[172:175], v[144:147]\n"
    "s_waitcnt lgkmcnt(2)\n"
    "v_add_f32 v148, v148, v232\n"
    "v_add_f32 v149, v149, v233\n"
    "v_mfma_f32_16x16x32_bf16 v[144:147], a[156:159], a[188:191], v[144:147]\n"
    "v_add_f32 v150, v150, v234\n"
    "v_add_f32 v151, v151, v235\n"
    "v_mfma_f32_16x16x32_bf16 v[144:147], a[152:155], a[180:183], v[144:147]\n"
    "s_waitcnt lgkmcnt(1)\n"
    "v_add_f32 v152, v152, v236\n"
    "v_add_f32 v153, v153, v237\n"
    "v_mfma_f32_16x16x32_bf16 v[144:147], a[148:151], a[220:223], v[144:147]\n"
    "v_add_f32 v154, v154, v238\n"
    "v_add_f32 v155, v155, v239\n"
    "v_mfma_f32_16x16x32_bf16 v[144:147], a[144:147], a[224:227], v[144:147]\n"
    "ds_write_b64 v20, v[254:255] offset:27656\n"
    "v_mfma_f32_16x16x32_bf16 v[144:147], a[140:143], a[228:231], v[144:147]\n"
    "v_fmac_f32 v244, s71, v160\n"
    "v_fmac_f32 v245, s71, v161\n"
    "v_mfma_f32_16x16x32_bf16 v[156:159], a[140:143], a[224:227], v[156:159]\n"
    "v_fmac_f32 v246, s71, v162\n"
    "v_fmac_f32 v247, s71, v163\n"
    "v_mfma_f32_16x16x32_bf16 v[156:159], a[144:147], a[220:223], v[156:159]\n"
    "ds_write_b64 v20, v[244:245] offset:25600\n"
    "v_mfma_f32_16x16x32_bf16 v[156:159], a[148:151], a[180:183], v[156:159]\n"
    "v_fmac_f32 v248, s71, v164\n"
    "v_fmac_f32 v249, s71, v165\n"
    "v_mfma_f32_16x16x32_bf16 v[156:159], a[152:155], a[188:191], v[156:159]\n"
    "ds_write_b64 v20, v[246:247] offset:25608\n"
    "v_mfma_f32_16x16x32_bf16 v[156:159], a[156:159], a[172:175], v[156:159]\n"
    "v_fmac_f32 v250, s71, v166\n"
    "v_fmac_f32 v251, s71, v167\n"
    "v_mfma_f32_16x16x32_bf16 v[156:159], a[160:163], a[168:171], v[156:159]\n"
    "ds_write_b64 v20, v[248:249] offset:26624\n"
    "v_mfma_f32_16x16x32_bf16 v[156:159], a[164:167], a[200:203], v[156:159]\n"
    "v_cvt_pk_bf16_f32 v228, v144, v145\n"
    "v_cvt_pk_bf16_f32 v229, v146, v147\n"
    "v_mfma_f32_16x16x32_bf16 v[136:139], a[108:111], a[168:171], v[136:139]\n"
    "ds_write_b64 v20, v[250:251] offset:26632\n"
    "v_mfma_f32_16x16x32_bf16 v[136:139], a[104:107], a[172:175], v[136:139]\n"
    "ds_write_b64 v23, v[228:229] offset:16384\n"
    "v_mfma_f32_16x16x32_bf16 v[136:139], a[100:103], a[188:191], v[136:139]\n"
    "v_mfma_f32_16x16x32_bf16 v[136:139], a[96:99], a[180:183], v[136:139]\n"
    "v_mfma_f32_16x16x32_bf16 v[136:139], a[92:95], a[220:223], v[136:139]\n"
    "v_mfma_f32_16x16x32_bf16 v[136:139], a[88:91], a[224:227], v[136:139]\n"
    "v_mfma_f32_16x16x32_bf16 v[136:139], a[84:87], a[228:231], v[136:139]\n"
    "v_cvt_pk_bf16_f32 v240, v156, v157\n"
    "v_cvt_pk_bf16_f32 v241, v158, v159\n"
    "v_mfma_f32_16x16x32_bf16 v[140:143], a[112:115], a[228:231], v[140:143]\n"
    "ds_write_b64 v23, v[240:241] offset:16896\n"
    "v_mfma_f32_16x16x32_bf16 v[140:143], a[116:119], a[224:227], v[140:143]\n"
    "v_mfma_f32_16x16x32_bf16 v[140:143], a[120:123], a[220:223], v[140:143]\n"
    "v_mfma_f32_16x16x32_bf16 v[140:143], a[124:127], a[180:183], v[140:143]\n"
    "s_mov_b32 s74, 258048\n"
    "v_mfma_f32_16x16x32_bf16 v[140:143], a[128:131], a[188:191], v[140:143]\n"
    "v_mfma_f32_16x16x32_bf16 v[140:143], a[132:135], a[172:175], v[140:143]\n"
    "s_waitcnt lgkmcnt(0)\n"
    "s_barrier\n"
    "ds_read_b64 v[218:219], v24 offset:15872\n"
    "v_mfma_f32_16x16x32_bf16 v[140:143], a[136:139], a[168:171], v[140:143]\n"
    "v_cvt_pk_bf16_f32 v220, v136, v137\n"
    "v_cvt_pk_bf16_f32 v221, v138, v139\n"
    "v_mfma_f32_16x16x32_bf16 v[168:171], a[164:167], a[236:239], v[168:171]\n"
    "ds_bpermute_b32 v136, v22, v220\n"
    "v_mfma_f32_16x16x32_bf16 v[168:171], a[160:163], a[200:203], v[168:171]\n"
    "ds_bpermute_b32 v137, v22, v221\n"
    "v_mfma_f32_16x16x32_bf16 v[168:171], a[156:159], a[168:171], v[168:171]\n"
    "ds_read_b128 v[252:255], v21 offset:27648\n"
    "v_mfma_f32_16x16x32_bf16 v[168:171], a[152:155], a[172:175], v[168:171]\n"
    "s_waitcnt lgkmcnt(3)\n"
    "ds_read_b128 v[244:247], v21 offset:25600\n"
    "v_mfma_f32_16x16x32_bf16 v[168:171], a[148:151], a[188:191], v[168:171]\n"
    "buffer_store_dwordx2 v[218:219], v26, s[28:31], s74 offen sc1\n"
    "v_mfma_f32_16x16x32_bf16 v[168:171], a[144:147], a[180:183], v[168:171]\n"
    "ds_read_b128 v[248:251], v21 offset:26624\n"
    "v_mfma_f32_16x16x32_bf16 v[168:171], a[140:143], a[220:223], v[168:171]\n"
    "s_mov_b32 s73, 344064\n"
    "v_cvt_pk_bf16_f32 v224, v140, v141\n"
    "v_cvt_pk_bf16_f32 v225, v142, v143\n"
    "v_mfma_f32_16x16x32_bf16 v[148:151], a[84:87], a[224:227], v[148:151]\n"
    "s_mov_b32 s74, 344064\n"
    "ds_bpermute_b32 v138, v22, v224\n"
    "v_mfma_f32_16x16x32_bf16 v[148:151], a[88:91], a[220:223], v[148:151]\n"
    "ds_bpermute_b32 v139, v22, v225\n"
    "v_mfma_f32_16x16x32_bf16 v[148:151], a[92:95], a[180:183], v[148:151]\n"
    "ds_read_b64 v[230:231], v24 offset:16384\n"
    "v_mfma_f32_16x16x32_bf16 v[148:151], a[96:99], a[188:191], v[148:151]\n"
    "s_waitcnt lgkmcnt(4)\n"
    "v_add_f32 v160, v160, v244\n"
    "v_add_f32 v161, v161, v245\n"
    "v_mfma_f32_16x16x32_bf16 v[148:151], a[100:103], a[172:175], v[148:151]\n"
    "v_add_f32 v162, v162, v246\n"
    "v_add_f32 v163, v163, v247\n"
    "v_mfma_f32_16x16x32_bf16 v[148:151], a[104:107], a[168:171], v[148:151]\n"
    "s_waitcnt lgkmcnt(3)\n"
    "v_add_f32 v164, v164, v248\n"
    "v_add_f32 v165, v165, v249\n"
    "v_mfma_f32_16x16x32_bf16 v[148:151], a[108:111], a[200:203], v[148:151]\n"
    "s_waitcnt lgkmcnt(1)\n"
    "v_add_f32 v168, v168, v252\n"
    "v_add_f32 v169, v169, v253\n"
    "v_mfma_f32_16x16x32_bf16 v[152:155], a[136:139], a[200:203], v[152:155]\n"
    "buffer_store_dwordx4 v[136:139], v25, s[28:31], s73 offen sc1\n"
    "v_mfma_f32_16x16x32_bf16 v[152:155], a[132:135], a[168:171], v[152:155]\n"
    "v_add_f32 v170, v170, v254\n"
    "v_add_f32 v171, v171, v255\n"
    "v_mfma_f32_16x16x32_bf16 v[152:155], a[128:131], a[172:175], v[152:155]\n"
    "s_waitcnt lgkmcnt(0)\n"
    "v_add_f32 v166, v166, v250\n"
    "v_add_f32 v167, v167, v251\n"
    "v_mfma_f32_16x16x32_bf16 v[152:155], a[124:127], a[188:191], v[152:155]\n"
    "buffer_store_dwordx2 v[230:231], v26, s[28:31], s74 offen sc1\n"
    "v_mfma_f32_16x16x32_bf16 v[152:155], a[120:123], a[180:183], v[152:155]\n"
    "s_mov_b32 s74, 430080\n"
    "ds_read_b64 v[242:243], v24 offset:16896\n"
    "v_cvt_pk_bf16_f32 v252, v168, v169\n"
    "v_mfma_f32_16x16x32_bf16 v[152:155], a[116:119], a[220:223], v[152:155]\n"
    "v_cvt_pk_bf16_f32 v253, v170, v171\n"
    "v_mfma_f32_16x16x32_bf16 v[152:155], a[112:115], a[224:227], v[152:155]\n"
    "v_cvt_pk_bf16_f32 v232, v148, v149\n"
    "v_cvt_pk_bf16_f32 v233, v150, v151\n"
    "v_mfma_f32_16x16x32_bf16 v[160:163], a[84:87], a[220:223], v[160:163]\n"
    "ds_bpermute_b32 v148, v22, v232\n"
    "v_mfma_f32_16x16x32_bf16 v[160:163], a[88:91], a[180:183], v[160:163]\n"
    "ds_bpermute_b32 v149, v22, v233\n"
    "v_mfma_f32_16x16x32_bf16 v[160:163], a[92:95], a[188:191], v[160:163]\n"
    "ds_write_b64 v23, v[252:253] offset:17408\n"
    "v_mfma_f32_16x16x32_bf16 v[160:163], a[96:99], a[172:175], v[160:163]\n"
    "s_waitcnt lgkmcnt(3)\n"
    "buffer_store_dwordx2 v[242:243], v26, s[28:31], s74 offen sc1\n"
    "v_mfma_f32_16x16x32_bf16 v[160:163], a[100:103], a[168:171], v[160:163]\n"
    "v_mfma_f32_16x16x32_bf16 v[160:163], a[104:107], a[200:203], v[160:163]\n"
    "v_mfma_f32_16x16x32_bf16 v[160:163], a[108:111], a[236:239], v[160:163]\n"
    "s_mov_b32 s73, 430080\n"
    "v_cvt_pk_bf16_f32 v236, v152, v153\n"
    "v_cvt_pk_bf16_f32 v237, v154, v155\n"
    "v_mfma_f32_16x16x32_bf16 v[164:167], a[136:139], a[236:239], v[164:167]\n"
    "s_waitcnt lgkmcnt(0)\n"
    "s_barrier\n"
    "ds_bpermute_b32 v150, v22, v236\n"
    "v_mfma_f32_16x16x32_bf16 v[164:167], a[132:135], a[200:203], v[164:167]\n"
    "s_mov_b32 s74, 516096\n"
    "ds_bpermute_b32 v151, v22, v237\n"
    "v_mfma_f32_16x16x32_bf16 v[164:167], a[128:131], a[168:171], v[164:167]\n"
    "ds_read_b64 v[254:255], v24 offset:17408\n"
    "v_mfma_f32_16x16x32_bf16 v[164:167], a[124:127], a[172:175], v[164:167]\n"
    "v_mfma_f32_16x16x32_bf16 v[164:167], a[120:123], a[188:191], v[164:167]\n"
    "v_mfma_f32_16x16x32_bf16 v[164:167], a[116:119], a[180:183], v[164:167]\n"
    "v_mfma_f32_16x16x32_bf16 v[164:167], a[112:115], a[220:223], v[164:167]\n"
    "v_cvt_pk_bf16_f32 v244, v160, v161\n"
    "v_cvt_pk_bf16_f32 v245, v162, v163\n"
    "s_waitcnt lgkmcnt(1)\n"
    "buffer_store_dwordx4 v[148:151], v25, s[28:31], s73 offen sc1\n"
    "ds_bpermute_b32 v160, v22, v244\n"
    "ds_bpermute_b32 v161, v22, v245\n"
    "s_waitcnt lgkmcnt(2)\n"
    "buffer_store_dwordx2 v[254:255], v26, s[28:31], s74 offen sc1\n"
    "v_cvt_pk_bf16_f32 v248, v164, v165\n"
    "v_cvt_pk_bf16_f32 v249, v166, v167\n"
    "s_mov_b32 s73, 516096\n"
    "ds_bpermute_b32 v162, v22, v248\n"
    "ds_bpermute_b32 v163, v22, v249\n"
    "s_waitcnt lgkmcnt(0)\n"
    "buffer_store_dwordx4 v[160:163], v25, s[28:31], s73 offen sc1\n"
    "s_endpgm\n"
    ".Lpoll0:\n"
    "buffer_load_dwordx2 v[42:43], v15, s[36:39], 0 offen offset:0 sc1\n"
    "s_sub_u32 s72, s72, 1\n"
    "s_cmp_lt_i32 s72, 0\n"
    "s_waitcnt vmcnt(0)\n"
    "s_cbranch_scc1 .Lpoll_ok0\n"
    "v_cmp_eq_u64 s[76:77], v[42:43], v[40:41]\n"
    "s_nop 1\n"
    "s_cmp_eq_u64 s[76:77], -1\n"
    "s_cbranch_scc1 .Lpoll_ok0\n"
    "s_sleep 2\n"
    "s_branch .Lpoll0\n"
    ".Lpoll1:\n"
    "buffer_load_dwordx2 v[42:43], v15, s[36:39], 0 offen offset:1024 sc1\n"
    "s_sub_u32 s72, s72, 1\n"
    "s_cmp_lt_i32 s72, 0\n"
    "s_waitcnt vmcnt(0)\n"
    "s_cbranch_scc1 .Lpoll_ok1\n"
    "v_cmp_eq_u64 s[76:77], v[42:43], v[40:41]\n"
    "s_nop 1\n"
    "s_cmp_eq_u64 s[76:77], -1\n"
    "s_cbranch_scc1 .Lpoll_ok1\n"
    "s_sleep 2\n"
    "s_branch .Lpoll1\n"
    ".Lpoll2:\n"
    "buffer_load_dwordx2 v[42:43], v15, s[36:39], 0 offen offset:2048 sc1\n"
    "s_sub_u32 s72, s72, 1\n"
    "s_cmp_lt_i32 s72, 0\n"
    "s_waitcnt vmcnt(0)\n"
    "s_cbranch_scc1 .Lpoll_ok2\n"
    "v_cmp_eq_u64 s[76:77], v[42:43], v[40:41]\n"
    "s_nop 1\n"
    "s_cmp_eq_u64 s[76:77], -1\n"
    "s_cbranch_scc1 .Lpoll_ok2\n"
    "s_sleep 2\n"
    "s_branch .Lpoll2\n"
    ".Lsl1:\n"
    "ds_read_b32 v38, v36 offset:30592\n"
    "s_waitcnt lgkmcnt(0)\n"
    "v_cmp_gt_u32 vcc, 1, v38\n"
    "s_nop 0\n"
    "s_cbranch_vccnz .Lsl1\n"
    "s_branch .Lbk1\n"
    ".Lsl2:\n"
    "ds_read_b32 v38, v36 offset:30592\n"
    "s_waitcnt lgkmcnt(0)\n"
    "v_cmp_gt_u32 vcc, 2, v38\n"
    "s_nop 0\n"
    "s_cbranch_vccnz .Lsl2\n"
    "s_branch .Lbk2\n"
    ".Lsl3:\n"
    "ds_read_b32 v38, v36 offset:30592\n"
    "s_waitcnt lgkmcnt(0)\n"
    "v_cmp_gt_u32 vcc, 3, v38\n"
    "s_nop 0\n"
    "s_cbranch_vccnz .Lsl3\n"
    "s_branch .Lbk3\n"
    ".Lsl4:\n"
    "ds_read_b32 v38, v36 offset:30592\n"
    "s_waitcnt lgkmcnt(0)\n"
    "v_cmp_gt_u32 vcc, 4, v38\n"
    "s_nop 0\n"
    "s_cbranch_vccnz .Lsl4\n"
    "s_branch .Lbk4\n"
    ".Lsl5:\n"
    "ds_read_b32 v38, v36 offset:30592\n"
    "s_waitcnt lgkmcnt(0)\n"
    "v_cmp_gt_u32 vcc, 5, v38\n"
    "s_nop 0\n"
    "s_cbranch_vccnz .Lsl5\n"
    "s_branch .Lbk5\n"
    ".Lsl6:\n"
    "ds_read_b32 v38, v36 offset:30592\n"
    "s_waitcnt lgkmcnt(0)\n"
    "v_cmp_gt_u32 vcc, 6, v38\n"
    "s_nop 0\n"
    "s_cbranch_vccnz .Lsl6\n"
    "s_branch .Lbk6\n"
    ".Lsl7:\n"
    "ds_read_b32 v38, v36 offset:30592\n"
    "s_waitcnt lgkmcnt(0)\n"
    "v_cmp_gt_u32 vcc, 7, v38\n"
    "s_nop 0\n"
    "s_cbranch_vccnz .Lsl7\n"
    "s_branch .Lbk7\n"
    ".Lsl8:\n"
    "ds_read_b32 v38, v36 offset:30592\n"
    "s_waitcnt lgkmcnt(0)\n"
    "v_cmp_gt_u32 vcc, 8, v38\n"
    "s_nop 0\n"
    "s_cbranch_vccnz .Lsl8\n"
    "s_branch .Lbk8\n"
    ".Lsl9:\n"
    "ds_read_b32 v38, v36 offset:30592\n"
    "s_waitcnt lgkmcnt(0)\n"
    "v_cmp_gt_u32 vcc, 9, v38\n"
    "s_nop 0\n"
    "s_cbranch_vccnz .Lsl9\n"
    "s_branch .Lbk9\n"
    ".Lsl10:\n"
    "ds_read_b32 v38, v36 offset:30592\n"
    "s_waitcnt lgkmcnt(0)\n"
    "v_cmp_gt_u32 vcc, 10, v38\n"
    "s_nop 0\n"
    "s_cbranch_vccnz .Lsl10\n"
    "s_branch .Lbk10\n"
    ".Lsl11:\n"
    "ds_read_b32 v38, v36 offset:30592\n"
    "s_waitcnt lgkmcnt(0)\n"
    "v_cmp_gt_u32 vcc, 11, v38\n"
    "s_nop 0\n"
    "s_cbranch_vccnz .Lsl11\n"
    "s_branch .Lbk11\n"
    ".Lsl12:\n"
    "ds_read_b32 v38, v36 offset:30592\n"
    "s_waitcnt lgkmcnt(0)\n"
    "v_cmp_gt_u32 vcc, 12, v38\n"
    "s_nop 0\n"
    "s_cbranch_vccnz .Lsl12\n"
    "s_branch .Lbk12\n"
    ".Lsl13:\n"
    "ds_read_b32 v38, v36 offset:30592\n"
    "s_waitcnt lgkmcnt(0)\n"
    "v_cmp_gt_u32 vcc, 13, v38\n"
    "s_nop 0\n"
    "s_cbranch_vccnz .Lsl13\n"
    "s_branch .Lbk13\n"
    ".Lsl14:\n"
    "ds_read_b32 v38, v36 offset:30592\n"
    "s_waitcnt lgkmcnt(0)\n"
    "v_cmp_gt_u32 vcc, 14, v38\n"
    "s_nop 0\n"
    "s_cbranch_vccnz .Lsl14\n"
    "s_branch .Lbk14\n"
    ".Lsl15:\n"
    "ds_read_b32 v38, v36 offset:30592\n"
    "s_waitcnt lgkmcnt(0)\n"
    "v_cmp_gt_u32 vcc, 15, v38\n"
    "s_nop 0\n"
    "s_cbranch_vccnz .Lsl15\n"
    "s_branch .Lbk15\n"
    ".Lsl16:\n"
    "ds_read_b32 v38, v36 offset:30592\n"
    "s_waitcnt lgkmcnt(0)\n"
    "v_cmp_gt_u32 vcc, 16, v38\n"
    "s_nop 0\n"
    "s_cbranch_vccnz .Lsl16\n"
    "s_branch .Lbk16\n"
    ".Lcons_end:\n"
    :
    : [bid] "s"(blockIdx.x), [tid] "v"(threadIdx.x), [tab] "s"((const void*)kT)
    : "v4", "v5", "v6", "v7", "v8", "v9", "v10", "v11", "v12", "v13", "v14", "v15", "v16", "v17", "v18", "v19", "v20", "v21", "v22", "v23", "v24", "v25", "v26", "v27", "v28", "v29", "v30", "v31", "v32", "v33", "v34", "v35", "v36", "v37", "v38", "v39", "v40", "v41", "v42", "v43", "v44", "v45", "v46", "v47", "v48", "v49", "v50", "v51", "v52", "v53", "v54", "v55", "v56", "v57", "v58", "v59", "v60", "v61", "v62", "v63", "v64", "v65", "v66", "v67", "v68", "v69", "v70", "v71", "v72", "v73", "v74", "v75", "v76", "v77", "v78", "v79", "v80", "v81", "v82", "v83", "v84", "v85", "v86", "v87", "v88", "v89", "v90", "v91", "v92", "v93", "v94", "v95", "v96", "v97", "v98", "v99", "v100", "v101", "v102", "v103", "v104", "v105", "v106", "v107", "v108", "v109", "v110", "v111", "v112", "v113", "v114", "v115", "v116", "v117", "v118", "v119", "v120", "v121", "v122", "v123", "v124", "v125", "v126", "v127", "v128", "v129", "v130", "v131", "v132", "v133", "v134", "v135", "v136", "v137", "v138", "v139", "v140", "v141", "v142", "v143", "v144", "v145", "v146", "v147", "v148", "v149", "v150", "v151", "v152", "v153", "v154", "v155", "v156", "v157", "v158", "v159", "v160", "v161", "v162", "v163", "v164", "v165", "v166", "v167", "v168", "v169", "v170", "v171", "v172", "v173", "v174", "v175", "v176", "v177", "v178", "v179", "v180", "v181", "v182", "v183", "v184", "v185", "v186", "v187", "v188", "v189", "v190", "v191", "v192", "v193", "v194", "v195", "v196", "v197", "v198", "v199", "v200", "v201", "v202", "v203", "v204", "v205", "v206", "v207", "v208", "v209", "v210", "v211", "v212", "v213", "v214", "v215", "v216", "v217", "v218", "v219", "v220", "v221", "v222", "v223", "v224", "v225", "v226", "v227", "v228", "v229", "v230", "v231", "v232", "v233", "v234", "v235", "v236", "v237", "v238", "v239", "v240", "v241", "v242", "v243", "v244", "v245", "v246", "v247", "v248", "v249", "v250", "v251", "v252", "v253", "v254", "v255", "a0", "a1", "a2", "a3", "a4", "a5", "a6", "a7", "a8", "a9", "a10", "a11", "a12", "a13", "a14", "a15", "a16", "a17", "a18", "a19", "a20", "a21", "a22", "a23", "a24", "a25", "a26", "a27", "a28", "a29", "a30", "a31", "a32", "a33", "a34", "a35", "a36", "a37", "a38", "a39", "a40", "a41", "a42", "a43", "a44", "a45", "a46", "a47", "a48", "a49", "a50", "a51", "a52", "a53", "a54", "a55", "a56", "a57", "a58", "a59", "a60", "a61", "a62", "a63", "a64", "a65", "a66", "a67", "a68", "a69", "a70", "a71", "a72", "a73", "a74", "a75", "a76", "a77", "a78", "a79", "a80", "a81", "a82", "a83", "a84", "a85", "a86", "a87", "a88", "a89", "a90", "a91", "a92", "a93", "a94", "a95", "a96", "a97", "a98", "a99", "a100", "a101", "a102", "a103", "a104", "a105", "a106", "a107", "a108", "a109", "a110", "a111", "a112", "a113", "a114", "a115", "a116", "a117", "a118", "a119", "a120", "a121", "a122", "a123", "a124", "a125", "a126", "a127", "a128", "a129", "a130", "a131", "a132", "a133", "a134", "a135", "a136", "a137", "a138", "a139", "a140", "a141", "a142", "a143", "a144", "a145", "a146", "a147", "a148", "a149", "a150", "a151", "a152", "a153", "a154", "a155", "a156", "a157", "a158", "a159", "a160", "a161", "a162", "a163", "a164", "a165", "a166", "a167", "a168", "a169", "a170", "a171", "a172", "a173", "a174", "a175", "a176", "a177", "a178", "a179", "a180", "a181", "a182", "a183", "a184", "a185", "a186", "a187", "a188", "a189", "a190", "a191", "a192", "a193", "a194", "a195", "a196", "a197", "a198", "a199", "a200", "a201", "a202", "a203", "a204", "a205", "a206", "a207", "a208", "a209", "a210", "a211", "a212", "a213", "a214", "a215", "a216", "a217", "a218", "a219", "a220", "a221", "a222", "a223", "a224", "a225", "a226", "a227", "a228", "a229", "a230", "a231", "a232", "a233", "a234", "a235", "a236", "a237", "a238", "a239", "a240", "a241", "a242", "a243", "a244", "a245", "a246", "a247", "a248", "a249", "a250", "a251", "a252", "a253", "a254", "a255", "s20", "s21", "s22", "s23", "s24", "s25", "s26", "s27", "s28", "s29", "s30", "s31", "s36", "s37", "s38", "s39", "s40", "s41", "s42", "s43", "s44", "s45", "s46", "s47", "s48", "s49", "s50", "s51", "s52", "s53", "s54", "s55", "s56", "s57", "s58", "s59", "s60", "s61", "s62", "s63", "s64", "s65", "s66", "s67", "s68", "s69", "s70", "s71", "s72", "s73", "s74", "s75", "s76", "s77", "s78", "s79", "s80", "s81", "s82", "s83", "s84", "s85", "s86", "s87", "s88", "s89", "s90", "s91", "s92", "s93", "s94", "s95", "s96", "s97", "vcc", "scc", "memory");
}

extern "C" void kernel_launch(void* const* d_in, const int* in_sizes, int n_in,
                              void* d_out, int out_size, void* d_ws, size_t ws_size,
                              hipStream_t stream) {
  dilated_conv7_kernel<<<256, 256, 0, stream>>>(d_in[0], d_in[1], d_out, d_ws);
}
```
